```python
import torch

def _softmax(x, axis=-1):
    return torch.softmax(x, dim=axis)
def _gelu(x):
    return torch.nn.functional.gelu(x, approximate="tanh")
def _silu(x):
    return torch.nn.functional.silu(x)
def _sigmoid(x):
    return torch.sigmoid(x)
def _layernorm(x, axis=-1, eps=1e-5):
    m = x.mean(dim=axis, keepdim=True)
    v = x.var(dim=axis, keepdim=True, unbiased=False)
    return (x - m) / torch.sqrt(v + eps)
def _scatter_add(tgt, idx, src, axis=0):
    return tgt.clone().index_add_(axis, idx, src)
def _segment_sum(src, seg, n):
    out = torch.zeros((n,) + src.shape[1:], dtype=src.dtype)
    return out.index_add_(0, seg, src)
def _pad(x, pw):
    flat = tuple(v for pair in reversed(pw) for v in pair)
    return torch.nn.functional.pad(x, flat)

kernel_name = 'parallel_sum__sliding_block_cached__glu_gated_swiglu__decode'

def setup_inputs():

    _w = (lambda a: (lambda t: t.to(torch.bfloat16) if t.is_floating_point() else t)(torch.as_tensor(a)))
    def _A_setup():
        B, Sq, D = 1, 1, 640
        Hq, Hkv, Sk, Dh = 32, 32, 32768, 192
        assert Hq % Hkv == 0, (Hq, Hkv)
        return {
            "x": _w((lambda *s: torch.randn(*s, dtype=torch.bfloat16) * 0.05)(B, Sq, D)),
            "k_cache": _w((lambda *s: torch.randn(*s, dtype=torch.bfloat16) * 1.6)(B, Hkv, Sk, Dh)),
            "v_cache": _w((lambda *s: torch.randn(*s, dtype=torch.bfloat16) * 0.8)(B, Hkv, Sk, Dh)),
            "wq": _w((lambda *s: torch.randn(*s, dtype=torch.bfloat16) * 1.6)(D, Hq * Dh) * (D ** -0.5)),
            "wo": _w((lambda *s: torch.randn(*s, dtype=torch.bfloat16) * 0.8)(Hq * Dh, D) * ((Hq * Dh) ** -0.5)),
        }
    _A_in = _A_setup()

    def _B_setup():
        B, S, D, F = 1, 1, 640, 1536
        return {
            "x": _w((lambda *s: torch.randn(*s, dtype=torch.bfloat16) * 0.05)(B, S, D)),
            "w1": _w((lambda *s: torch.randn(*s, dtype=torch.bfloat16) * 0.4)(D, F) * (2.0 / D) ** 0.5),
            "w3": _w((lambda *s: torch.randn(*s, dtype=torch.bfloat16) * 0.4)(D, F) * (2.0 / D) ** 0.5),
            "w2": _w((lambda *s: torch.randn(*s, dtype=torch.bfloat16) * 0.4)(F, D) * (2.0 / F) ** 0.5),
        }
    _B_in = _B_setup()

    return {
        "A_x": _A_in["x"],
        "A_k_cache": _A_in["k_cache"],
        "A_v_cache": _A_in["v_cache"],
        "A_wq": _A_in["wq"],
        "A_wo": _A_in["wo"],
        "B_x": _B_in["x"],
        "B_w1": _B_in["w1"],
        "B_w3": _B_in["w3"],
        "B_w2": _B_in["w2"],
    }

def reference(A_x, A_k_cache, A_v_cache, A_wq, A_wo, B_x, B_w1, B_w3, B_w2):
    def _A_ref(x, k_cache, v_cache, wq, wo):
        B, Sq, D = x.shape
        Hq, Dh = 32, 192
        Hkv, Sk = k_cache.shape[1], k_cache.shape[2]
        W = min(4096, max(256, Sk // 4))
        rep = Hq // Hkv
        q = (lambda x, *d: x.permute(*d))((x @ wq).reshape(B, Sq, Hq, Dh), 0, 2, 1, 3)
        k = (lambda x, n, axis: torch.repeat_interleave(x, n, dim=axis))(k_cache[:, :, Sk - W:, :], rep, axis=1)
        v = (lambda x, n, axis: torch.repeat_interleave(x, n, dim=axis))(v_cache[:, :, Sk - W:, :], rep, axis=1)
        scores = torch.einsum("bhqd,bhkd->bhqk", q, k) * (Dh ** -0.5)
        p = _softmax(scores, axis=-1)
        o = torch.einsum("bhqk,bhkd->bhqd", p, v)
        return (lambda x, *d: x.permute(*d))(o, 0, 2, 1, 3).reshape(B, Sq, Hq * Dh) @ wo

    def _B_ref(x, w1, w3, w2):
        gate = torch.einsum("bsd,df->bsf", x, w1)
        up = torch.einsum("bsd,df->bsf", x, w3)
        h = _silu(gate) * up
        out = torch.einsum("bsf,fd->bsd", h, w2)
        return out

    x = A_x
    xn = _layernorm(x, axis=-1)
    return x + _A_ref(xn, A_k_cache, A_v_cache, A_wq, A_wo) + _B_ref(xn, B_w1, B_w3, B_w2)
```

```cpp
#include <hip/hip_runtime.h>
#include <hip/hip_bf16.h>
#include <stdint.h>

#define MIMIC 0

typedef unsigned short u16;
typedef unsigned int u32;
typedef u32 u32x4 __attribute__((ext_vector_type(4)));

constexpr int D = 640, H = 32, DH = 192, SK = 32768, W = 4096, F = 1536;
constexpr int NS = 8, KPS = W / NS;

__device__ __forceinline__ float bf2f(u16 v) { return __uint_as_float(((u32)v) << 16); }
__device__ __forceinline__ float rbf(float f) {
  return f;
}
__device__ __forceinline__ float lo(u32 v) { return __uint_as_float(v << 16); }
__device__ __forceinline__ float hi(u32 v) { return __uint_as_float(v & 0xffff0000u); }

__device__ void layernorm_to_lds(const u16* x, float* xn, float* red) {
  int t = threadIdx.x;
  float s = 0.f;
  for (int i = t; i < D; i += blockDim.x) s += bf2f(x[i]);
  for (int o = 32; o > 0; o >>= 1) s += __shfl_xor(s, o);
  if ((t & 63) == 0) red[t >> 6] = s;
  __syncthreads();
  float tot = 0.f;
  for (int w = 0; w < (int)(blockDim.x >> 6); ++w) tot += red[w];
  float mean = tot / D;
  __syncthreads();
  float v = 0.f;
  for (int i = t; i < D; i += blockDim.x) { float d = bf2f(x[i]) - mean; v += d * d; }
  for (int o = 32; o > 0; o >>= 1) v += __shfl_xor(v, o);
  if ((t & 63) == 0) red[t >> 6] = v;
  __syncthreads();
  float vt = 0.f;
  for (int w = 0; w < (int)(blockDim.x >> 6); ++w) vt += red[w];
  float var = rbf(vt / D);
  float mb = rbf(mean);
  float sd = rbf(sqrtf(rbf(var + 1e-5f)));
  for (int i = t; i < D; i += blockDim.x) xn[i] = rbf(rbf(bf2f(x[i]) - mb) / sd);
  __syncthreads();
}

__global__ void __launch_bounds__(256) k_stage1(const u16* x, const u16* wq, const u16* w1, const u16* w3, float* y1) {
  __shared__ float xn[D];
  __shared__ float red[8];
  __shared__ float part[32][64];
  layernorm_to_lds(x, xn, red);
  int b = blockIdx.x;
  const u16* wp; int ld, c0;
  if (b < 96) { wp = wq; ld = H * DH; c0 = b * 64; }
  else if (b < 120) { wp = w1; ld = F; c0 = (b - 96) * 64; }
  else { wp = w3; ld = F; c0 = (b - 120) * 64; }
  int t = threadIdx.x, r = t >> 3, c = t & 7;
  float acc[8] = {0};
  for (int i = r; i < D; i += 32) {
    u32x4 w = *reinterpret_cast<const u32x4*>(wp + (size_t)i * ld + c0 + c * 8);
    float xv = xn[i];
    for (int k = 0; k < 4; ++k) { acc[2 * k] += xv * lo(w[k]); acc[2 * k + 1] += xv * hi(w[k]); }
  }
  for (int k = 0; k < 8; ++k) part[r][c * 8 + k] = acc[k];
  __syncthreads();
  if (t < 64) {
    float s = 0.f;
    for (int i = 0; i < 32; ++i) s += part[i][t];
    y1[b * 64 + t] = rbf(s);
  }
}

__global__ void __launch_bounds__(512) k_attn(const u16* kc, const u16* vc, const float* y1, float* po, float* pml) {
  __shared__ float sc[KPS];
  __shared__ float red[8];
  __shared__ float opart[8][DH];
  int s = blockIdx.x, h = blockIdx.y;
  int t = threadIdx.x, w = t >> 6, l = t & 63, r = l >> 3, c = l & 7;
  const u16* kb = kc + ((size_t)h * SK + (SK - W) + s * KPS) * DH;
  const u16* vb = vc + ((size_t)h * SK + (SK - W) + s * KPS) * DH;
  float q[24];
  for (int cc = 0; cc < 3; ++cc) for (int k = 0; k < 8; ++k) q[cc * 8 + k] = y1[h * DH + cc * 64 + c * 8 + k];
  const float scale = 0.07216878364870322f;
  float mx = -INFINITY;
  for (int u = w; u < KPS / 8; u += 8) {
    int key = u * 8 + r;
    float d = 0.f;
    for (int cc = 0; cc < 3; ++cc) {
      u32x4 kv = *reinterpret_cast<const u32x4*>(kb + (size_t)key * DH + cc * 64 + c * 8);
      for (int k = 0; k < 4; ++k) { d += q[cc * 8 + 2 * k] * lo(kv[k]); d += q[cc * 8 + 2 * k + 1] * hi(kv[k]); }
    }
    d += __shfl_xor(d, 1); d += __shfl_xor(d, 2); d += __shfl_xor(d, 4);
    d = rbf(rbf(d) * scale);
    if (c == 0) sc[key] = d;
    mx = fmaxf(mx, d);
  }
  for (int o = 32; o > 0; o >>= 1) mx = fmaxf(mx, __shfl_xor(mx, o));
  if (l == 0) red[w] = mx;
  __syncthreads();
  float M = red[0];
  for (int i = 1; i < 8; ++i) M = fmaxf(M, red[i]);
  __syncthreads();
  float p = __expf(sc[t] - M);
  sc[t] = p;
  float ls = p;
  for (int o = 32; o > 0; o >>= 1) ls += __shfl_xor(ls, o);
  if (l == 0) red[w] = ls;
  __syncthreads();
  float L = 0.f;
  for (int i = 0; i < 8; ++i) L += red[i];
  float acc[24] = {0};
  for (int u = w; u < KPS / 8; u += 8) {
    int key = u * 8 + r;
    float pv = sc[key];
    for (int cc = 0; cc < 3; ++cc) {
      u32x4 vv = *reinterpret_cast<const u32x4*>(vb + (size_t)key * DH + cc * 64 + c * 8);
      for (int k = 0; k < 4; ++k) { acc[cc * 8 + 2 * k] += pv * lo(vv[k]); acc[cc * 8 + 2 * k + 1] += pv * hi(vv[k]); }
    }
  }
  for (int i = 0; i < 24; ++i) { float a = acc[i]; a += __shfl_xor(a, 8); a += __shfl_xor(a, 16); a += __shfl_xor(a, 32); acc[i] = a; }
  if (r == 0) for (int cc = 0; cc < 3; ++cc) for (int k = 0; k < 8; ++k) opart[w][cc * 64 + c * 8 + k] = acc[cc * 8 + k];
  __syncthreads();
  if (t < DH) {
    float o = 0.f;
    for (int i = 0; i < 8; ++i) o += opart[i][t];
    po[(h * NS + s) * DH + t] = o;
  }
  if (t == 0) { pml[(h * NS + s) * 2] = M; pml[(h * NS + s) * 2 + 1] = L; }
}

__global__ void __launch_bounds__(256) k_out(const u16* wo, const u16* w2, const float* y1, const float* po, const float* pml, float* py, float* pf) {
  __shared__ float o[DH + 48];
  __shared__ float part[32][64];
  int cg = blockIdx.x, g = blockIdx.y, t = threadIdx.x;
  if (t < DH) {
    float M = -INFINITY;
    for (int s = 0; s < NS; ++s) M = fmaxf(M, pml[(g * NS + s) * 2]);
    float L = 0.f, a = 0.f;
    for (int s = 0; s < NS; ++s) { float f = __expf(pml[(g * NS + s) * 2] - M); L += f * pml[(g * NS + s) * 2 + 1]; a += f * po[(g * NS + s) * DH + t]; }
    o[t] = rbf(a / L);
  } else if (t < DH + 48) {
    int f = g * 48 + (t - DH);
    float gate = y1[H * DH + f], up = y1[H * DH + F + f];
    float si = rbf(gate / (1.f + __expf(-gate)));
    o[t] = rbf(si * up);
  }
  __syncthreads();
  int r = t >> 3, c = t & 7;
  float acc[8] = {0};
  for (int i = r; i < DH; i += 32) {
    u32x4 w = *reinterpret_cast<const u32x4*>(wo + (size_t)(g * DH + i) * D + cg * 64 + c * 8);
    float xv = o[i];
    for (int k = 0; k < 4; ++k) { acc[2 * k] += xv * lo(w[k]); acc[2 * k + 1] += xv * hi(w[k]); }
  }
  for (int k = 0; k < 8; ++k) part[r][c * 8 + k] = acc[k];
  __syncthreads();
  if (t < 64) { float s = 0.f; for (int i = 0; i < 32; ++i) s += part[i][t]; py[g * D + cg * 64 + t] = s; }
  __syncthreads();
  for (int k = 0; k < 8; ++k) acc[k] = 0.f;
  for (int i = r; i < 48; i += 32) {
    u32x4 w = *reinterpret_cast<const u32x4*>(w2 + (size_t)(g * 48 + i) * D + cg * 64 + c * 8);
    float xv = o[DH + i];
    for (int k = 0; k < 4; ++k) { acc[2 * k] += xv * lo(w[k]); acc[2 * k + 1] += xv * hi(w[k]); }
  }
  for (int k = 0; k < 8; ++k) part[r][c * 8 + k] = acc[k];
  __syncthreads();
  if (t < 64) { float s = 0.f; for (int i = 0; i < 32; ++i) s += part[i][t]; pf[g * D + cg * 64 + t] = s; }
}

__global__ void __launch_bounds__(640) k_final(const u16* x, const float* py, const float* pf, __hip_bfloat16* out) {
  int t = threadIdx.x;
  float a = 0.f, b = 0.f;
  for (int g = 0; g < 32; ++g) { a += py[g * D + t]; b += pf[g * D + t]; }
  float r = rbf(bf2f(x[t]) + rbf(a));
  r = r + rbf(b);
  out[t] = __float2bfloat16(r);
}

extern "C" void kernel_launch(void* const* d_in, const int* in_sizes, int n_in, void* d_out, int out_size, void* d_ws, size_t ws_size, hipStream_t stream) {
  const u16* x = (const u16*)d_in[0];
  const u16* kc = (const u16*)d_in[1];
  const u16* vc = (const u16*)d_in[2];
  const u16* wq = (const u16*)d_in[3];
  const u16* wo = (const u16*)d_in[4];
  const u16* w1 = (const u16*)d_in[6];
  const u16* w3 = (const u16*)d_in[7];
  const u16* w2 = (const u16*)d_in[8];
  float* ws = (float*)d_ws;
  float* y1 = ws;
  float* po = ws + 16384;
  float* pml = ws + 16384 + 49152;
  float* py = ws + 81920;
  float* pf = ws + 81920 + 20480;
  k_stage1<<<144, 256, 0, stream>>>(x, wq, w1, w3, y1);
  k_attn<<<dim3(NS, H), 512, 0, stream>>>(kc, vc, y1, po, pml);
  k_out<<<dim3(10, 32), 256, 0, stream>>>(wo, w2, y1, po, pml, py, pf);
  k_final<<<1, 640, 0, stream>>>(x, py, pf, (__hip_bfloat16*)d_out);
}
```

```cpp
#define GPOS 4
#include <hip/hip_runtime.h>
#include <hip/hip_bf16.h>
#include <stdint.h>

typedef unsigned short u16;
typedef unsigned int u32;
typedef unsigned long long u64;
typedef u32 u32x4 __attribute__((ext_vector_type(4)));
typedef u32 u32x2 __attribute__((ext_vector_type(2)));
typedef float f32x2 __attribute__((ext_vector_type(2)));
typedef float f32x4 __attribute__((ext_vector_type(4)));
typedef __attribute__((ext_vector_type(2))) __bf16 bf16x2;
typedef __attribute__((ext_vector_type(8))) __bf16 bf16x8;
#define GAS __attribute__((address_space(1)))
#define LAS __attribute__((address_space(3)))
typedef GAS u64 gu64;
typedef GAS u32 gu32;
#define RLX_AGENT __ATOMIC_RELAXED, __HIP_MEMORY_SCOPE_AGENT

#define POLL_SLEEP 2
#define PD 4
#define G1A 18
#define NST 5
#define S1FIRST 1
#define MAPX 1
#define EPA 18
#define DIAG 0

constexpr int DM = 640, NH = 32, DH = 192, SK = 32768, WIN = 4096, FF = 1536;
constexpr int NS = 8, KPS = WIN / NS;
constexpr u32 WS_EPOCH = 0;
constexpr u32 WS_REC = 4096, REC = 2560;
constexpr u32 R_G1 = 0;
constexpr u32 R_G2 = 1024;
constexpr u32 R_ST = 1792;
constexpr u32 R_G3 = 1920;
constexpr u32 R_CTR = 2432;
constexpr u32 WS_END = WS_REC + NH * NS * REC;
constexpr int WS_STAMP = 1 << 19;
constexpr int NWAVE = 12, NHELP = 4;
constexpr u32 SPIN_CAP = 1u << 22;
constexpr float LOG2E = 1.4426950408889634f;

__device__ __forceinline__ float bf_lo(u32 v) { return __uint_as_float(v << 16); }
__device__ __forceinline__ float bf_hi(u32 v) { return __uint_as_float(v & 0xffff0000u); }
__device__ __forceinline__ float rbf(float f) { return __bfloat162float(__float2bfloat16(f)); }
__device__ __forceinline__ u32 pkbf(float f) { const bf16x2 h = {(__bf16)f, (__bf16)f}; return __builtin_bit_cast(u32, h); }
__device__ __forceinline__ u32x4 ldnt(const char* p) { return __builtin_nontemporal_load(reinterpret_cast<const u32x4*>(p)); }
__device__ __forceinline__ float dot2(u32 a, u32 b, float c) {
  return __builtin_amdgcn_fdot2_f32_bf16(__builtin_bit_cast(bf16x2, a), __builtin_bit_cast(bf16x2, b), c, false);
}
__device__ __forceinline__ f32x4 mfma(u32x4 a, u32x4 b, f32x4 c) {
  return __builtin_amdgcn_mfma_f32_16x16x32_bf16(__builtin_bit_cast(bf16x8, a), __builtin_bit_cast(bf16x8, b), c, 0, 0, 0);
}
template <int CTRL> __device__ __forceinline__ float dpp(float x) {
  return __builtin_bit_cast(float, __builtin_amdgcn_mov_dpp(__builtin_bit_cast(int, x), CTRL, 0xf, 0xf, true));
}
constexpr int XOR1 = 0xB1, XOR2 = 0x4E, HMIR = 0x141, ROR8 = 0x128, ROR4 = 0x124;
__device__ __forceinline__ float swap16_add(float a, float b) {
  auto s = __builtin_amdgcn_permlane16_swap(__float_as_uint(a), __float_as_uint(b), false, false);
  return __uint_as_float(s[0]) + __uint_as_float(s[1]);
}
__device__ __forceinline__ float swap32_add(float a, float b) {
  auto s = __builtin_amdgcn_permlane32_swap(__float_as_uint(a), __float_as_uint(b), false, false);
  return __uint_as_float(s[0]) + __uint_as_float(s[1]);
}
__device__ __forceinline__ float swap16_max(float a) {
  auto s = __builtin_amdgcn_permlane16_swap(__float_as_uint(a), __float_as_uint(a), false, false);
  return fmaxf(__uint_as_float(s[0]), __uint_as_float(s[1]));
}
__device__ __forceinline__ float swap32_max(float a) {
  auto s = __builtin_amdgcn_permlane32_swap(__float_as_uint(a), __float_as_uint(a), false, false);
  return fmaxf(__uint_as_float(s[0]), __uint_as_float(s[1]));
}
__device__ __forceinline__ float sum_hi3(float x) { x += dpp<ROR8>(x); x = swap16_add(x, x); return swap32_add(x, x); }
__device__ __forceinline__ float max_hi3(float x) { x = fmaxf(x, dpp<ROR8>(x)); x = swap16_max(x); return swap32_max(x); }
__device__ __forceinline__ float sum_lo3(float x) { x += dpp<XOR1>(x); x += dpp<XOR2>(x); x += dpp<HMIR>(x); return x; }
__device__ __forceinline__ float max_lo3(float x) { x = fmaxf(x, dpp<XOR1>(x)); x = fmaxf(x, dpp<XOR2>(x)); x = fmaxf(x, dpp<HMIR>(x)); return x; }
__device__ __forceinline__ float treduce8(const float* a, bool b3) {
  float v[4];
#pragma unroll
  for (int k = 0; k < 4; ++k) {
    const float keep = b3 ? a[k + 4] : a[k], send = b3 ? a[k] : a[k + 4];
    v[k] = keep + dpp<ROR8>(send);
  }
  const float u0 = swap16_add(v[0], v[2]), u1 = swap16_add(v[1], v[3]);
  return swap32_add(u0, u1);
}
__device__ __forceinline__ float treduce4(float a0, float a1, float a2, float a3) {
  return swap32_add(swap16_add(a0, a2), swap16_add(a1, a3));
}
__device__ __forceinline__ void lds_arrive(u32* c, int l) {
  asm volatile("s_waitcnt lgkmcnt(0)" ::: "memory");
  if (l == 0) __hip_atomic_fetch_add((LAS u32*)c, 1u, __ATOMIC_RELAXED, __HIP_MEMORY_SCOPE_WORKGROUP);
  asm volatile("" ::: "memory");
}
__device__ __forceinline__ u32 lds_ticket(u32* c, int l) {
  asm volatile("s_waitcnt lgkmcnt(0)" ::: "memory");
  u32 t = 0;
  if (l == 0) t = __hip_atomic_fetch_add((LAS u32*)c, 1u, __ATOMIC_RELAXED, __HIP_MEMORY_SCOPE_WORKGROUP);
  t = __builtin_amdgcn_readfirstlane(t);
  asm volatile("" ::: "memory");
  return t;
}
__device__ __forceinline__ void lds_wait(u32* c, u32 n) {
  asm volatile("" ::: "memory");
  while (__builtin_amdgcn_readfirstlane(__hip_atomic_load((LAS u32*)c, __ATOMIC_RELAXED, __HIP_MEMORY_SCOPE_WORKGROUP)) < n) __builtin_amdgcn_s_sleep(1);
  asm volatile("" ::: "memory");
}
__device__ __forceinline__ u64 mktag(u64 epoch) { return ((epoch + 1) << 24) | (1ull << 63); }
__device__ __forceinline__ u64 mkgran(u64 tagsh, float v) { return tagsh | (u64)(__float_as_uint(v) >> 8); }
__device__ __forceinline__ float granval(u64 x) { return __uint_as_float(((u32)x) << 8); }
__device__ __forceinline__ bool granok(u64 x, u64 tagsh) { return ((x ^ tagsh) >> 24) == 0; }
__device__ __forceinline__ void st_gran(gu64* p, u64 v) { __hip_atomic_store(p, v, RLX_AGENT); }
__device__ __forceinline__ u64 ld_gran(gu64* p) { return __hip_atomic_load(p, RLX_AGENT); }
__device__ __forceinline__ void st_val(gu32* p, float v) { __hip_atomic_store(p, __float_as_uint(v) | 1u, RLX_AGENT); }
#define SCHED() __builtin_amdgcn_sched_barrier(0)
__device__ __forceinline__ void wg_barrier() {
  asm volatile("s_waitcnt lgkmcnt(0)" ::: "memory");
  __builtin_amdgcn_s_barrier();
  asm volatile("" ::: "memory");
}
#define STAMP(i)
#define RETRY(n)

enum { C_S1 = 0, C_PROG, C_Q, C_N };
constexpr u32 OOB = 0xfffffff0u;
constexpr u32 POOL = (8 - NST) * 64;

__global__ void __launch_bounds__(768, 3)
fused_decode_kernel(const u16* __restrict__ x, const char* __restrict__ kc, const char* __restrict__ vc,
                    const char* __restrict__ wq, const char* __restrict__ wo, const char* __restrict__ w1,
                    const char* __restrict__ w3, const char* __restrict__ w2, u16* __restrict__ out, char* ws) {
  __shared__ __attribute__((aligned(16))) float lds[640 + 96 + 32 + 64 + 16 * 64 + 16 * 192 + 16 * 128 + 8 * 64];
  u32* xn_s = (u32*)lds;
  u32* q_s = (u32*)(lds + 640);
  float* m_s = lds + 736;
  float* l_s = lds + 744;
  u32* cnt = (u32*)(lds + 752);
  u32* h_s = (u32*)(lds + 768);
  float* s1_s = lds + 832;
  float* op_s = s1_s + 16 * 64;
  float* yp_s = op_s + 16 * 192;
  float* fin_s = yp_s + 16 * 128;
  STAMP(0);
  const int b = blockIdx.x;
  const int g = MAPX ? b >> 3 : (b & 7) * 4 + (b >> 6), s = MAPX ? b & 7 : (b >> 3) & 7;
  const int tid = threadIdx.x;
  const int w = __builtin_amdgcn_readfirstlane(tid >> 6), l = tid & 63;
  const int r = l >> 3, j = l & 7;
  const __amdgpu_buffer_rsrc_t wsr = __builtin_amdgcn_make_buffer_rsrc(ws, 0, WS_END, 0x00020000);
  const u32 grec = WS_REC + g * NS * REC, myrec = grec + s * REC;
  const bool is_red = (s == (g >> 2)) && !(g & 1);
  const bool red_b = (g & 2) != 0;
  u32x4 msk;
#pragma unroll
  for (int d = 0; d < 4; ++d) msk[d] = (j >> 1) == d ? ((j & 1) ? 0xffff0000u : 0x0000ffffu) : 0u;
  const int hh = l >> 5;
  const bool useful = ((l >> 3) & 1) == hh;
  const int dcol = 8 * j + 4 * ((l >> 4) & 1);
#define STR2(x) #x
#define STR(x) STR2(x)
#define PACE() asm volatile("s_waitcnt vmcnt(" STR(PD) ")" ::: "memory")

  if (w >= 4) {
    const int sw = w - 4;
    const size_t hbase = ((size_t)g * SK + (SK - WIN)) * (DH * 2);
    const __amdgpu_buffer_rsrc_t rk = __builtin_amdgcn_make_buffer_rsrc((void*)(kc + hbase), 0, WIN * DH * 2, 0x00020000);
    const __amdgpu_buffer_rsrc_t rv = __builtin_amdgcn_make_buffer_rsrc((void*)(vc + hbase), 0, WIN * DH * 2, 0x00020000);
    const u32 ll = (u32)(r * 384 + j * 16);
    const u32 sbase = (s * NST * 8 + sw) * 3072;
    constexpr int NB = NST - 1;
    u32x4 K[NB][3], V[NB][3], KB[3], VB[3];
#define LOAD3(dst, rs, off) do { _Pragma("unroll") for (int c = 0; c < 3; ++c) { PACE(); dst[c] = __builtin_amdgcn_raw_buffer_load_b128(rs, ll + c * 128, off, 2); } SCHED(); } while (0)
    wg_barrier();
#pragma unroll
    for (int u = S1FIRST ? 0 : 1; u < NB; ++u) { LOAD3(K[u], rk, sbase + u * 24576); if (u == GPOS) lds_arrive(cnt + C_PROG, l); }
    LOAD3(KB, rk, sbase + NB * 24576);
    if (NB == GPOS) lds_arrive(cnt + C_PROG, l);
    STAMP(1);
#pragma unroll
    for (int u = 0; u < 3; ++u) { LOAD3(V[u], rv, sbase + u * 24576); if (NB + 1 + u == GPOS) lds_arrive(cnt + C_PROG, l); }
    STAMP(2);
    lds_wait(cnt + C_Q, 1);
    STAMP(3);
    u32 tk = 0xffffffffu;
    const char* ctrp = ws + grec + R_CTR;
    const u32 zero_ = 0, one_ = 1;
#define CLAIM() do { if (POOL) { u64 sv_; asm volatile("s_mov_b64 %1, exec\n\ts_mov_b64 exec, 1\n\tglobal_atomic_add %0, %2, %3, %4 sc0\n\ts_mov_b64 exec, %1" \
      : "=&v"(tk), "=&s"(sv_) : "v"(zero_), "v"(one_), "s"(ctrp) : "memory"); } } while (0)
#define TICKET(t_) do { if (POOL) asm volatile("s_waitcnt vmcnt(6)" : "+v"(tk) :: "memory"); t_ = __builtin_amdgcn_readfirstlane(tk); } while (0)
    u32x4 q[3];
#pragma unroll
    for (int c = 0; c < 3; ++c) q[c] = *reinterpret_cast<const u32x4*>(q_s + c * 32 + j * 4);
#define DOTS(Kx, d_) do { float d0 = 0.f, d1 = 0.f, d2 = 0.f; _Pragma("unroll") for (int k = 0; k < 4; ++k) { d0 = dot2(Kx[0][k], q[0][k], d0); d1 = dot2(Kx[1][k], q[1][k], d1); d2 = dot2(Kx[2][k], q[2][k], d2); } \
      d_ = d0 + d1 + d2; } while (0)
#define SFIN(d_, fl_, fr_) do { const float t_ = rbf(rbf(sum_lo3(d_)) * 0.07216878364870322f) * LOG2E; fl_ = floorf(t_); fr_ = __builtin_amdgcn_exp2f(t_ - fl_); } while (0)
#define SCORE(Kx, fl_, fr_) do { float dd_; DOTS(Kx, dd_); SFIN(dd_, fl_, fr_); } while (0)
    float mw, ls;
    f32x4 acc[3];
#pragma unroll
    for (int c = 0; c < 3; ++c) acc[c] = f32x4{0.f, 0.f, 0.f, 0.f};
#define PVX(pk_, Vx) do { const u32x4 a_ = msk & (pk_); _Pragma("unroll") for (int c = 0; c < 3; ++c) acc[c] = mfma(a_, Vx[c], acc[c]); \
      asm volatile("" : "+v"(acc[0]), "+v"(acc[1]), "+v"(acc[2])); SCHED(); } while (0)
    {
      float fl[NB], fr[NB];
#pragma unroll
      for (int u = 0; u < NB; ++u) {
        SCORE(K[u], fl[u], fr[u]);
        asm volatile("" : "+v"(fl[u]), "+v"(fr[u]));
        SCHED();
        if (u + 3 == NB - 1) CLAIM();
        if (u + 3 < NB) LOAD3(V[u + 3], rv, sbase + (u + 3) * 24576);
      }
      LOAD3(VB, rv, sbase + NB * 24576);
      float mx = fl[0];
#pragma unroll
      for (int u = 1; u < NB; ++u) mx = fmaxf(mx, fl[u]);
      mw = max_hi3(mx) + 1.f;
      ls = 0.f;
#pragma unroll
      for (int u = 0; u < NB; ++u) { const float e = ldexpf(fr[u], (int)(fl[u] - mw)); ls += e; PVX(pkbf(e), V[u]); }
    }
    STAMP(4);
#define WEIGHT(d_, a_) do { float fl_, fr_; SFIN(d_, fl_, fr_); \
      if (__any(fl_ >= mw)) { const float mn_ = max_hi3(fl_) + 1.f; const float al_ = ldexpf(1.f, (int)(mw - mn_)); mw = mn_; ls *= al_; \
        _Pragma("unroll") for (int c = 0; c < 3; ++c) acc[c] *= al_; } \
      const float e_ = ldexpf(fr_, (int)(fl_ - mw)); ls += e_; a_ = msk & pkbf(e_); } while (0)
    u32x4 WO[5];
    u32 xraw;
    const __amdgpu_buffer_rsrc_t rw = __builtin_amdgcn_make_buffer_rsrc(
        (void*)((sw < 6) ? wo + (size_t)(g * DH + sw * 32) * (DM * 2) : w2 + (size_t)(g * 48 + (sw - 6) * 32) * (DM * 2)), 0, 32 * DM * 2, 0x00020000);
    const u32 wl = ((sw == 7) ? (r & 3) : r) * (4 * DM * 2) + j * 16;
    for (;;) {
      u32 t;
      TICKET(t);
      if (t >= POOL) break;
      CLAIM();
      const u32 uo = (NST * 64 + t) * 3072;
      float d;
      DOTS(KB, d);
      asm volatile("" : "+v"(d));
      SCHED();
#pragma unroll
      for (int c = 0; c < 3; ++c) KB[c] = __builtin_amdgcn_raw_buffer_load_b128(rk, ll + c * 128, uo, 2);
      SCHED();
      u32x4 a;
      WEIGHT(d, a);
#pragma unroll
      for (int c = 0; c < 3; ++c) {
        acc[c] = mfma(a, VB[c], acc[c]);
        asm volatile("" : "+v"(acc[c]));
        SCHED();
        VB[c] = __builtin_amdgcn_raw_buffer_load_b128(rv, ll + c * 128, uo, 2);
        SCHED();
      }
      RETRY(1u << 20);
    }
    {
      float d;
      DOTS(KB, d);
      asm volatile("" : "+v"(d));
      SCHED();
#pragma unroll
      for (int i = 0; i < 4; ++i) WO[i] = __builtin_amdgcn_raw_buffer_load_b128(rw, wl + i * (DM * 2), s * 128, 2);
      WO[4] = __builtin_amdgcn_raw_buffer_load_b128(rw, wl, (s & 3) * (DM * 2) + (8 + (s >> 2)) * 128, 2);
      xraw = __builtin_nontemporal_load(x + (red_b ? 512 + s * 16 + (l & 15) : s * 64 + l));
      SCHED();
      u32x4 a;
      WEIGHT(d, a);
      ls = sum_hi3(ls);
      if (l == 0) { m_s[sw] = mw; l_s[sw] = ls; }
      SCHED();
#pragma unroll
      for (int c = 0; c < 3; ++c) acc[c] = mfma(a, VB[c], acc[c]);
    }
    STAMP(5);
    if (useful) {
#pragma unroll
      for (int c = 0; c < 3; ++c) *reinterpret_cast<f32x4*>(op_s + (sw * 2 + hh) * 192 + c * 64 + dcol) = acc[c];
    }
    wg_barrier();
    STAMP(6);
    u32 opk[4];
    if (sw < 6) {
      const u32 goff = grec + j * REC + R_G2 + sw * 128 + r * 16;
      const u32 soff = grec + j * REC + R_ST;
      u32x2 SS;
      u32x4 O;
      bool ok = false;
      u32 spins = 0;
      for (;;) {
        if (!ok) {
          SS = __builtin_amdgcn_raw_buffer_load_b64(wsr, soff, 0, 16);
          O = __builtin_amdgcn_raw_buffer_load_b128(wsr, goff, 0, 16);
          ok = (O[0] & O[1] & O[2] & O[3] & SS[0] & SS[1] & 1) != 0;
        }
        if (__all(ok) || ++spins > SPIN_CAP) break;
        __builtin_amdgcn_s_sleep(POLL_SLEEP);
      }
      STAMP(7);
      const float ms = __uint_as_float(SS[0] & ~1u);
      const float f = __builtin_amdgcn_exp2f(ms - max_lo3(ms));
      const float sc = f * __builtin_amdgcn_rcpf(sum_lo3(f * __uint_as_float(SS[1])));
#pragma unroll
      for (int i = 0; i < 4; ++i) opk[i] = pkbf(sum_lo3(sc * __uint_as_float(O[i])));
    } else {
      const u32x4 hq = *reinterpret_cast<const u32x4*>(h_s + (sw - 6) * 32 + 4 * r);
#pragma unroll
      for (int i = 0; i < 4; ++i) opk[i] = hq[i];
    }
    {
      f32x4 y1 = {0.f, 0.f, 0.f, 0.f}, y2 = y1;
      const int s3 = s & 3;
      const u32 o2 = s3 == 0 ? opk[0] : s3 == 1 ? opk[1] : s3 == 2 ? opk[2] : opk[3];
      y2 = mfma(msk & o2, WO[4], y2);
#pragma unroll
      for (int i = 0; i < 4; ++i) y1 = mfma(msk & opk[i], WO[i], y1);
      if (useful) {
        *reinterpret_cast<f32x4*>(yp_s + (sw * 2 + hh) * 128 + dcol) = y1;
        *reinterpret_cast<f32x4*>(yp_s + (sw * 2 + hh) * 128 + 64 + dcol) = y2;
      }
    }
    wg_barrier();
    STAMP(8);
    if (is_red) {
      const u32 goff = red_b ? WS_REC + ((4 * sw + ((l >> 2) & 3)) * NS + (s & 4) + (l >> 4)) * REC + R_G3 + 256 + (s & 3) * 64 + (l & 3) * 16
                             : WS_REC + ((4 * sw + (l >> 4)) * NS + s) * REC + R_G3 + (l & 15) * 16;
      u32x4 Y;
      bool ok = false;
      u32 spins = 0;
      for (;;) {
        if (!ok) {
          Y = __builtin_amdgcn_raw_buffer_load_b128(wsr, goff, 0, 16);
          ok = (Y[0] & Y[1] & Y[2] & Y[3] & 1) != 0;
        }
        if (__all(ok) || ++spins > SPIN_CAP) break;
        __builtin_amdgcn_s_sleep(1);
      }
      float t = treduce4(__uint_as_float(Y[0]), __uint_as_float(Y[1]), __uint_as_float(Y[2]), __uint_as_float(Y[3]));
      const int k2 = 2 * ((l >> 4) & 1) + (l >> 5);
      if (red_b) {
        t += dpp<ROR4>(t); t += dpp<ROR8>(t);
        if ((l & 12) == 0) fin_s[sw * 64 + 4 * (l & 3) + k2] = t;
      } else {
        fin_s[sw * 64 + 4 * (l & 15) + k2] = t;
      }
      wg_barrier();
      if (sw == 0) {
        if (l < (red_b ? 16 : 64)) {
          float y = 0.f;
#pragma unroll
          for (int i = 0; i < 8; ++i) y += fin_s[i * 64 + l];
          out[red_b ? 512 + s * 16 + l : s * 64 + l] = __bfloat16_as_ushort(__float2bfloat16(__uint_as_float(xraw << 16) + y));
        }
        if (b == 0 && l == 0) st_gran((gu64*)(ws + WS_EPOCH), ((u64)cnt[14] | ((u64)cnt[15] << 32)) + 1);
      }
    }
  } else {
    const u32x4 xa = ldnt((const char*)(x + l * 8));
    const u32 xb = __builtin_nontemporal_load(reinterpret_cast<const u32*>(x + 512 + l * 2));
    const u32x2 ep2 = __builtin_amdgcn_raw_buffer_load_b64(wsr, WS_EPOCH, 0, EPA);
    {
      const u32x4 z = {0u, 0u, 0u, 0u};
      __builtin_amdgcn_raw_buffer_store_b128(z, wsr, (w == 3 && l < 49) ? myrec + R_G2 + l * 16 : OOB, 0, 16);
      __builtin_amdgcn_raw_buffer_store_b128(z, wsr, (w == 3 && l < (s == 0 ? 33 : 32)) ? myrec + R_G3 + l * 16 : OOB, 0, 16);
    }
    SCHED();
    if (tid < 14) cnt[tid] = 0;
    SCHED();
    const int qd = g >> 2, wi = (g & 3) * 8 + s;
    const int lnA = (45 * wi) / 80, lnB = (45 * wi + 44) / 80;
    const int tb = (lnA + 1) * 80 - 45 * wi;
    u32x4 S1[12];
    {
      const char* pA = lnA < 12 ? wq + (size_t)(qd * 768 + lnA * 64) * 2 : (lnA < 15 ? w1 : w3) + (size_t)(qd * 192 + (lnA % 3) * 64) * 2;
      const char* pB = lnB < 12 ? wq + (size_t)(qd * 768 + lnB * 64) * 2 : (lnB < 15 ? w1 : w3) + (size_t)(qd * 192 + (lnB % 3) * 64) * 2;
      const u32 rsA = lnA < 12 ? NH * DH * 2 : FF * 2, rsB = lnB < 12 ? NH * DH * 2 : FF * 2;
      const u32 vA = r * rsA + j * 16, vB = r * rsB + j * 16;
#pragma unroll
      for (int i = 0; i < 12; ++i) {
        const int t = min(w + 4 * i, 44);
        const bool inA = t < tb;
        const char* pb = (inA ? pA : pB) + (u32)((inA ? 80 - tb + t : t - tb) * 8 * (inA ? rsA : rsB));
        S1[i] = ldnt(pb + (inA ? vA : vB));
      }
    }
    SCHED();
    wg_barrier();
    SCHED();
    {
      float xv[10];
#pragma unroll
      for (int k = 0; k < 4; ++k) { xv[2 * k] = bf_lo(xa[k]); xv[2 * k + 1] = bf_hi(xa[k]); }
      xv[8] = bf_lo(xb); xv[9] = bf_hi(xb);
      float sm = 0.f;
#pragma unroll
      for (int k = 0; k < 10; ++k) sm += xv[k];
      sm = sum_hi3(sum_lo3(sm));
      const float mean = sm * (1.f / DM);
      float sv = 0.f;
#pragma unroll
      for (int k = 0; k < 10; ++k) { const float d = xv[k] - mean; sv = fmaf(d, d, sv); }
      sv = sum_hi3(sum_lo3(sv));
      const float var = rbf(sv * (1.f / DM));
      const float mb = rbf(mean);
      const float sd = rbf(sqrtf(rbf(var + 1e-5f)));
      u32 xp[10];
#pragma unroll
      for (int k = 0; k < 10; ++k) xp[k] = pkbf(rbf(xv[k] - mb) / sd);
      *reinterpret_cast<u32x4*>(xn_s + l * 8) = u32x4{xp[0], xp[1], xp[2], xp[3]};
      *reinterpret_cast<u32x4*>(xn_s + l * 8 + 4) = u32x4{xp[4], xp[5], xp[6], xp[7]};
      *reinterpret_cast<u32x2*>(xn_s + 512 + l * 2) = u32x2{xp[8], xp[9]};
      asm volatile("s_waitcnt lgkmcnt(0)" ::: "memory");
    }
    const u64 tagsh = mktag((u64)ep2[0] | ((u64)ep2[1] << 32));
    const u32 taglo = (u32)tagsh, taghi = (u32)(tagsh >> 32);
    {
      u32 xr[12];
#pragma unroll
      for (int i = 0; i < 12; ++i) { const int t = min(w + 4 * i, 44); xr[i] = xn_s[(t < tb ? 80 - tb + t : t - tb) * 8 + r]; }
      if (w != 0) xr[11] = 0u;
      f32x4 a1 = {0.f, 0.f, 0.f, 0.f}, a2 = a1;
#pragma unroll
      for (int i = 0; i < 12; ++i) {
        const bool inA = min(w + 4 * i, 44) < tb;
        a1 = mfma(msk & (inA ? xr[i] : 0u), S1[i], a1);
        a2 = mfma(msk & (inA ? 0u : xr[i]), S1[i], a2);
      }
      if (useful) {
        *reinterpret_cast<f32x4*>(s1_s + (w * 2 + hh) * 64 + dcol) = a1;
        *reinterpret_cast<f32x4*>(s1_s + (8 + w * 2 + hh) * 64 + dcol) = a2;
      }
      asm volatile("s_waitcnt vmcnt(0)" ::: "memory");
      if (lds_ticket(cnt + C_S1, l) == 3) {
        float va = 0.f, vb = 0.f;
#pragma unroll
        for (int t = 0; t < 8; ++t) { va += s1_s[t * 64 + l]; vb += s1_s[(8 + t) * 64 + l]; }
        __builtin_amdgcn_raw_buffer_store_b64(u32x2{taglo | (__float_as_uint(va) >> 8), taghi}, wsr, myrec + R_G1 + l * 8, 0, G1A);
        __builtin_amdgcn_raw_buffer_store_b64(u32x2{taglo | (__float_as_uint(vb) >> 8), taghi}, wsr, lnB != lnA ? myrec + R_G1 + 512 + l * 8 : OOB, 0, G1A);
      }
    }
    STAMP(1);
    lds_wait(cnt + C_PROG, 8);
    if (w != 2 && (w != 1 || is_red)) {
      u32 ga[10];
      bool use3[3];
#pragma unroll
      for (int k = 0; k < 3; ++k) {
        const int fc = (g & 3) * 48 + min(l, 47);
        const int ln = w == 0 ? (g & 3) * 3 + k : 12 + 3 * min(k, 1) + (fc >> 6);
        const int col = w == 0 ? l : (fc & 63);
        const int f0 = (80 * ln) / 45, f1 = (80 * ln + 79) / 45;
        use3[k] = f0 + 2 <= f1;
#pragma unroll
        for (int i = 0; i < 3; ++i) {
          const int pw = min(f0 + i, f1);
          ga[k * 3 + i] = WS_REC + (qd * 32 + pw) * REC + R_G1 + (45 * pw < 80 * ln ? 512 : 0) + col * 8;
        }
      }
      ga[9] = grec + j * REC + R_G1;
      if (w == 1) {
        ga[9] = WS_REC + ((l & 31) * NS + (red_b ? (s & 4) + hh : s)) * REC + R_G1;
#pragma unroll
        for (int i = 0; i < 9; ++i) ga[i] = ga[9] + (red_b ? 2 * REC : 0);
      }
      u32x2 G[10];
      bool ok = false;
      u32 spins = 0;
      for (;;) {
        if (!ok) {
#pragma unroll
          for (int i = 0; i < 10; ++i) if (w == 0 || (w == 3 && i < 6) || (w == 1 && i >= 8)) G[i] = __builtin_amdgcn_raw_buffer_load_b64(wsr, ga[i], 0, G1A);
          u32 d_ = 0;
#pragma unroll
          for (int i = 0; i < 10; ++i) if (w == 0 || (w == 3 && i < 6) || (w == 1 && i >= 8)) d_ |= (G[i][1] ^ taghi) | ((G[i][0] ^ taglo) & 0xff000000u);
          ok = (d_ == 0);
        }
        if (__all(ok) || ++spins > SPIN_CAP) break;
        RETRY(w == 0 ? 1 : w == 3 ? 1000 : 100000);
        __builtin_amdgcn_s_sleep(8);
      }
      float t[3];
#pragma unroll
      for (int k = 0; k < 3; ++k) t[k] = __uint_as_float(G[3 * k][0] << 8) + __uint_as_float(G[3 * k + 1][0] << 8) + (use3[k] ? __uint_as_float(G[3 * k + 2][0] << 8) : 0.f);
      if (w == 0) {
#pragma unroll
        for (int k = 0; k < 3; ++k) ((u16*)q_s)[64 * k + l] = __bfloat16_as_ushort(__float2bfloat16(t[k]));
        if (l == 0) { cnt[14] = ep2[0]; cnt[15] = ep2[1]; }
        lds_arrive(cnt + C_Q, l);
      } else if (w == 3) {
        const float gate = rbf(t[0]), up = rbf(t[1]);
        const float hv = rbf(gate / (1.f + __expf(-gate))) * up;
        h_s[l] = l < 48 ? pkbf(hv) : 0u;
      }
    }
    STAMP(2);
    __builtin_amdgcn_s_setprio(3);
    wg_barrier();
    STAMP(5);
    {
      const int col = min(tid, 191);
      float pv[16];
#pragma unroll
      for (int i = 0; i < 16; ++i) pv[i] = op_s[i * 192 + col];
      const float mj = m_s[j];
      const float M = max_lo3(mj);
      const float fj = __builtin_amdgcn_exp2f(mj - M);
      float o = 0.f;
      if (w == 3) {
        const float L = sum_lo3(l_s[j] * fj);
        o = (l & 1) ? L : M;
      } else {
#pragma unroll
        for (int i = 0; i < 8; ++i) o = fmaf(__uint_as_float(__builtin_amdgcn_readlane(__float_as_uint(fj), i)), pv[2 * i] + pv[2 * i + 1], o);
      }
      __builtin_amdgcn_raw_buffer_store_b32(__float_as_uint(o) | 1u, wsr, tid < 194 ? myrec + R_G2 + tid * 4 : OOB, 0, 16);
    }
    STAMP(6);
    wg_barrier();
    STAMP(7);
    if (w < 2) {
      float y = 0.f;
#pragma unroll
      for (int i = 0; i < 16; ++i) y += yp_s[i * 128 + tid];
      __builtin_amdgcn_raw_buffer_store_b32(__float_as_uint(y) | 1u, wsr, myrec + R_G3 + tid * 4, 0, 16);
    }
    STAMP(8);
    if (is_red) wg_barrier();
  }
  STAMP(9);
}

extern "C" void kernel_launch(void* const* d_in, const int* in_sizes, int n_in, void* d_out, int out_size, void* d_ws,
                              size_t ws_size, hipStream_t stream) {
  fused_decode_kernel<<<256, 768, 0, stream>>>((const u16*)d_in[0], (const char*)d_in[1], (const char*)d_in[2],
                                               (const char*)d_in[3], (const char*)d_in[4], (const char*)d_in[6],
                                               (const char*)d_in[7], (const char*)d_in[8], (u16*)d_out, (char*)d_ws);
}
```

```cpp
#define GPOS 4
#define VAUX 0
#include <hip/hip_runtime.h>
#include <hip/hip_bf16.h>
#include <stdint.h>

typedef unsigned short u16;
typedef unsigned int u32;
typedef unsigned long long u64;
typedef u32 u32x4 __attribute__((ext_vector_type(4)));
typedef u32 u32x2 __attribute__((ext_vector_type(2)));
typedef float f32x2 __attribute__((ext_vector_type(2)));
typedef float f32x4 __attribute__((ext_vector_type(4)));
typedef __attribute__((ext_vector_type(2))) __bf16 bf16x2;
typedef __attribute__((ext_vector_type(8))) __bf16 bf16x8;
#define GAS __attribute__((address_space(1)))
#define LAS __attribute__((address_space(3)))
typedef GAS u64 gu64;
typedef GAS u32 gu32;
#define RLX_AGENT __ATOMIC_RELAXED, __HIP_MEMORY_SCOPE_AGENT

#define POLL_SLEEP 2
#define PD 4
#define G1A 18
#define NST 5
#define S1FIRST 1
#define KAUX 2
#define WAUX 2
#define IPROBE 0
#define MAPX 1
#define EPA 18
#define DIAG 0

constexpr int DM = 640, NH = 32, DH = 192, SK = 32768, WIN = 4096, FF = 1536;
constexpr int NS = 8, KPS = WIN / NS;
constexpr u32 WS_EPOCH = 0;
constexpr u32 WS_REC = 4096, REC = 2560;
constexpr u32 R_G1 = 0;
constexpr u32 R_G2 = 1024;
constexpr u32 R_ST = 1792;
constexpr u32 R_G3 = 1920;
constexpr u32 R_CTR = 2432;
constexpr u32 WS_END = WS_REC + NH * NS * REC;
constexpr int WS_STAMP = 1 << 19;
constexpr int NWAVE = 12, NHELP = 4;
constexpr u32 SPIN_CAP = 1u << 22;
constexpr float LOG2E = 1.4426950408889634f;

__device__ __forceinline__ float bf_lo(u32 v) { return __uint_as_float(v << 16); }
__device__ __forceinline__ float bf_hi(u32 v) { return __uint_as_float(v & 0xffff0000u); }
__device__ __forceinline__ float rbf(float f) { return __bfloat162float(__float2bfloat16(f)); }
__device__ __forceinline__ u32 pkbf(float f) { const bf16x2 h = {(__bf16)f, (__bf16)f}; return __builtin_bit_cast(u32, h); }
__device__ __forceinline__ u32x4 ldnt(const char* p) { return __builtin_nontemporal_load(reinterpret_cast<const u32x4*>(p)); }
__device__ __forceinline__ float dot2(u32 a, u32 b, float c) {
  return __builtin_amdgcn_fdot2_f32_bf16(__builtin_bit_cast(bf16x2, a), __builtin_bit_cast(bf16x2, b), c, false);
}
__device__ __forceinline__ f32x4 mfma(u32x4 a, u32x4 b, f32x4 c) {
  return __builtin_amdgcn_mfma_f32_16x16x32_bf16(__builtin_bit_cast(bf16x8, a), __builtin_bit_cast(bf16x8, b), c, 0, 0, 0);
}
template <int CTRL> __device__ __forceinline__ float dpp(float x) {
  return __builtin_bit_cast(float, __builtin_amdgcn_mov_dpp(__builtin_bit_cast(int, x), CTRL, 0xf, 0xf, true));
}
constexpr int XOR1 = 0xB1, XOR2 = 0x4E, HMIR = 0x141, ROR8 = 0x128, ROR4 = 0x124;
__device__ __forceinline__ float swap16_add(float a, float b) {
  auto s = __builtin_amdgcn_permlane16_swap(__float_as_uint(a), __float_as_uint(b), false, false);
  return __uint_as_float(s[0]) + __uint_as_float(s[1]);
}
__device__ __forceinline__ float swap32_add(float a, float b) {
  auto s = __builtin_amdgcn_permlane32_swap(__float_as_uint(a), __float_as_uint(b), false, false);
  return __uint_as_float(s[0]) + __uint_as_float(s[1]);
}
__device__ __forceinline__ float swap16_max(float a) {
  auto s = __builtin_amdgcn_permlane16_swap(__float_as_uint(a), __float_as_uint(a), false, false);
  return fmaxf(__uint_as_float(s[0]), __uint_as_float(s[1]));
}
__device__ __forceinline__ float swap32_max(float a) {
  auto s = __builtin_amdgcn_permlane32_swap(__float_as_uint(a), __float_as_uint(a), false, false);
  return fmaxf(__uint_as_float(s[0]), __uint_as_float(s[1]));
}
__device__ __forceinline__ float sum_hi3(float x) { x += dpp<ROR8>(x); x = swap16_add(x, x); return swap32_add(x, x); }
__device__ __forceinline__ float max_hi3(float x) { x = fmaxf(x, dpp<ROR8>(x)); x = swap16_max(x); return swap32_max(x); }
__device__ __forceinline__ float sum_lo3(float x) { x += dpp<XOR1>(x); x += dpp<XOR2>(x); x += dpp<HMIR>(x); return x; }
__device__ __forceinline__ float max_lo3(float x) { x = fmaxf(x, dpp<XOR1>(x)); x = fmaxf(x, dpp<XOR2>(x)); x = fmaxf(x, dpp<HMIR>(x)); return x; }
__device__ __forceinline__ float treduce8(const float* a, bool b3) {
  float v[4];
#pragma unroll
  for (int k = 0; k < 4; ++k) {
    const float keep = b3 ? a[k + 4] : a[k], send = b3 ? a[k] : a[k + 4];
    v[k] = keep + dpp<ROR8>(send);
  }
  const float u0 = swap16_add(v[0], v[2]), u1 = swap16_add(v[1], v[3]);
  return swap32_add(u0, u1);
}
__device__ __forceinline__ float treduce4(float a0, float a1, float a2, float a3) {
  return swap32_add(swap16_add(a0, a2), swap16_add(a1, a3));
}
__device__ __forceinline__ void lds_arrive(u32* c, int l) {
  asm volatile("s_waitcnt lgkmcnt(0)" ::: "memory");
  if (l == 0) __hip_atomic_fetch_add((LAS u32*)c, 1u, __ATOMIC_RELAXED, __HIP_MEMORY_SCOPE_WORKGROUP);
  asm volatile("" ::: "memory");
}
__device__ __forceinline__ u32 lds_ticket(u32* c, int l) {
  asm volatile("s_waitcnt lgkmcnt(0)" ::: "memory");
  u32 t = 0;
  if (l == 0) t = __hip_atomic_fetch_add((LAS u32*)c, 1u, __ATOMIC_RELAXED, __HIP_MEMORY_SCOPE_WORKGROUP);
  t = __builtin_amdgcn_readfirstlane(t);
  asm volatile("" ::: "memory");
  return t;
}
__device__ __forceinline__ void lds_wait(u32* c, u32 n) {
  asm volatile("" ::: "memory");
  while (__builtin_amdgcn_readfirstlane(__hip_atomic_load((LAS u32*)c, __ATOMIC_RELAXED, __HIP_MEMORY_SCOPE_WORKGROUP)) < n) __builtin_amdgcn_s_sleep(1);
  asm volatile("" ::: "memory");
}
__device__ __forceinline__ u64 mktag(u64 epoch) { return ((epoch + 1) << 24) | (1ull << 63); }
__device__ __forceinline__ u64 mkgran(u64 tagsh, float v) { return tagsh | (u64)(__float_as_uint(v) >> 8); }
__device__ __forceinline__ float granval(u64 x) { return __uint_as_float(((u32)x) << 8); }
__device__ __forceinline__ bool granok(u64 x, u64 tagsh) { return ((x ^ tagsh) >> 24) == 0; }
__device__ __forceinline__ void st_gran(gu64* p, u64 v) { __hip_atomic_store(p, v, RLX_AGENT); }
__device__ __forceinline__ u64 ld_gran(gu64* p) { return __hip_atomic_load(p, RLX_AGENT); }
__device__ __forceinline__ void st_val(gu32* p, float v) { __hip_atomic_store(p, __float_as_uint(v) | 1u, RLX_AGENT); }
#define SCHED() __builtin_amdgcn_sched_barrier(0)
__device__ __forceinline__ void wg_barrier() {
  asm volatile("s_waitcnt lgkmcnt(0)" ::: "memory");
  __builtin_amdgcn_s_barrier();
  asm volatile("" ::: "memory");
}
#define STAMP(i)
#define RETRY(n)

enum { C_S1 = 0, C_PROG, C_Q, C_N };
constexpr u32 OOB = 0xfffffff0u;
constexpr u32 POOL = (8 - NST) * 64;

__global__ void __launch_bounds__(768, 3)
fused_decode_kernel(const u16* __restrict__ x, const char* __restrict__ kc, const char* __restrict__ vc,
                    const char* __restrict__ wq, const char* __restrict__ wo, const char* __restrict__ w1,
                    const char* __restrict__ w3, const char* __restrict__ w2, u16* __restrict__ out, char* ws) {
  __shared__ __attribute__((aligned(16))) float lds[640 + 96 + 32 + 64 + 16 * 64 + 16 * 192 + 16 * 128 + 8 * 64];
  u32* xn_s = (u32*)lds;
  u32* q_s = (u32*)(lds + 640);
  float* m_s = lds + 736;
  float* l_s = lds + 744;
  u32* cnt = (u32*)(lds + 752);
  u32* h_s = (u32*)(lds + 768);
  float* s1_s = lds + 832;
  float* op_s = s1_s + 16 * 64;
  float* yp_s = op_s + 16 * 192;
  float* fin_s = yp_s + 16 * 128;
  STAMP(0);
  const int b = blockIdx.x;
  const int g = MAPX ? b >> 3 : (b & 7) * 4 + (b >> 6), s = MAPX ? b & 7 : (b >> 3) & 7;
  const int tid = threadIdx.x;
  const int w = __builtin_amdgcn_readfirstlane(tid >> 6), l = tid & 63;
  const int r = l >> 3, j = l & 7;
  const __amdgpu_buffer_rsrc_t wsr = __builtin_amdgcn_make_buffer_rsrc(ws, 0, WS_END, 0x00020000);
  const u32 grec = WS_REC + g * NS * REC, myrec = grec + s * REC;
  const bool is_red = (s == (g >> 2)) && !(g & 1);
  const bool red_b = (g & 2) != 0;
  u32x4 msk;
#pragma unroll
  for (int d = 0; d < 4; ++d) msk[d] = (j >> 1) == d ? ((j & 1) ? 0xffff0000u : 0x0000ffffu) : 0u;
  const int hh = l >> 5;
  const bool useful = ((l >> 3) & 1) == hh;
  const int dcol = 8 * j + 4 * ((l >> 4) & 1);
#define STR2(x) #x
#define STR(x) STR2(x)
#define PACE() asm volatile("s_waitcnt vmcnt(" STR(PD) ")" ::: "memory")

  if (w >= 4) {
    const int sw = w - 4;
    const size_t hbase = ((size_t)g * SK + (SK - WIN)) * (DH * 2);
    const __amdgpu_buffer_rsrc_t rk = __builtin_amdgcn_make_buffer_rsrc((void*)(kc + hbase), 0, WIN * DH * 2, 0x00020000);
    const __amdgpu_buffer_rsrc_t rv = __builtin_amdgcn_make_buffer_rsrc((void*)(vc + hbase), 0, WIN * DH * 2, 0x00020000);
    const u32 ll = (u32)(r * 384 + j * 16);
    const u32 sbase = (s * NST * 8 + sw) * 3072;
    constexpr int NB = NST - 1;
    u32x4 K[NB][3], V[NB][3], KB[3], VB[3];
#define AUX_rk KAUX
#define AUX_rv VAUX
#define LOAD3(dst, rs, off) do { _Pragma("unroll") for (int c = 0; c < 3; ++c) { PACE(); dst[c] = __builtin_amdgcn_raw_buffer_load_b128(rs, ll + c * 128, off, AUX_##rs); } SCHED(); } while (0)
    wg_barrier();
#pragma unroll
    for (int u = S1FIRST ? 0 : 1; u < NB; ++u) { LOAD3(K[u], rk, sbase + u * 24576); if (u == GPOS) lds_arrive(cnt + C_PROG, l); }
    LOAD3(KB, rk, sbase + NB * 24576);
    if (NB == GPOS) lds_arrive(cnt + C_PROG, l);
    STAMP(1);
#pragma unroll
    for (int u = 0; u < 3; ++u) { LOAD3(V[u], rv, sbase + u * 24576); if (NB + 1 + u == GPOS) lds_arrive(cnt + C_PROG, l); }
    STAMP(2);
    lds_wait(cnt + C_Q, 1);
    STAMP(3);
    u32 tk = 0xffffffffu;
    const char* ctrp = ws + grec + R_CTR;
    const u32 zero_ = 0, one_ = 1;
#define CLAIM() do { if (POOL) { u64 sv_; asm volatile("s_mov_b64 %1, exec\n\ts_mov_b64 exec, 1\n\tglobal_atomic_add %0, %2, %3, %4 sc0\n\ts_mov_b64 exec, %1" \
      : "=&v"(tk), "=&s"(sv_) : "v"(zero_), "v"(one_), "s"(ctrp) : "memory"); } } while (0)
#define TICKET(t_) do { if (POOL) asm volatile("s_waitcnt vmcnt(6)" : "+v"(tk) :: "memory"); t_ = __builtin_amdgcn_readfirstlane(tk); } while (0)
    u32x4 q[3];
#pragma unroll
    for (int c = 0; c < 3; ++c) q[c] = *reinterpret_cast<const u32x4*>(q_s + c * 32 + j * 4);
#define DOTS(Kx, d_) do { float d0 = 0.f, d1 = 0.f, d2 = 0.f; _Pragma("unroll") for (int k = 0; k < 4; ++k) { d0 = dot2(Kx[0][k], q[0][k], d0); d1 = dot2(Kx[1][k], q[1][k], d1); d2 = dot2(Kx[2][k], q[2][k], d2); } \
      d_ = d0 + d1 + d2; } while (0)
#define SFIN(d_, fl_, fr_) do { const float t_ = rbf(rbf(sum_lo3(d_)) * 0.07216878364870322f) * LOG2E; fl_ = floorf(t_); fr_ = __builtin_amdgcn_exp2f(t_ - fl_); } while (0)
#define SCORE(Kx, fl_, fr_) do { float dd_; DOTS(Kx, dd_); SFIN(dd_, fl_, fr_); } while (0)
    float mw, ls;
    f32x4 acc[3];
#pragma unroll
    for (int c = 0; c < 3; ++c) acc[c] = f32x4{0.f, 0.f, 0.f, 0.f};
#define PVX(pk_, Vx) do { const u32x4 a_ = msk & (pk_); _Pragma("unroll") for (int c = 0; c < 3; ++c) acc[c] = mfma(a_, Vx[c], acc[c]); \
      asm volatile("" : "+v"(acc[0]), "+v"(acc[1]), "+v"(acc[2])); SCHED(); } while (0)
    {
      float fl[NB], fr[NB];
#pragma unroll
      for (int u = 0; u < NB; ++u) {
        SCORE(K[u], fl[u], fr[u]);
        asm volatile("" : "+v"(fl[u]), "+v"(fr[u]));
        SCHED();
        if (u + 3 == NB - 1) CLAIM();
        if (u + 3 < NB) LOAD3(V[u + 3], rv, sbase + (u + 3) * 24576);
      }
      LOAD3(VB, rv, sbase + NB * 24576);
      float mx = fl[0];
#pragma unroll
      for (int u = 1; u < NB; ++u) mx = fmaxf(mx, fl[u]);
      mw = max_hi3(mx) + 1.f;
      ls = 0.f;
#pragma unroll
      for (int u = 0; u < NB; ++u) { const float e = ldexpf(fr[u], (int)(fl[u] - mw)); ls += e; PVX(pkbf(e), V[u]); }
    }
    STAMP(4);
#define WEIGHT(d_, a_) do { float fl_, fr_; SFIN(d_, fl_, fr_); \
      if (__any(fl_ >= mw)) { const float mn_ = max_hi3(fl_) + 1.f; const float al_ = ldexpf(1.f, (int)(mw - mn_)); mw = mn_; ls *= al_; \
        _Pragma("unroll") for (int c = 0; c < 3; ++c) acc[c] *= al_; } \
      const float e_ = ldexpf(fr_, (int)(fl_ - mw)); ls += e_; a_ = msk & pkbf(e_); } while (0)
    u32x4 WO[5];
    u32 xraw;
    const __amdgpu_buffer_rsrc_t rw = __builtin_amdgcn_make_buffer_rsrc(
        (void*)((sw < 6) ? wo + (size_t)(g * DH + sw * 32) * (DM * 2) : w2 + (size_t)(g * 48 + (sw - 6) * 32) * (DM * 2)), 0, 32 * DM * 2, 0x00020000);
    const u32 wl = ((sw == 7) ? (r & 3) : r) * (4 * DM * 2) + j * 16;
    for (;;) {
      u32 t;
      TICKET(t);
      if (t >= POOL) break;
      CLAIM();
      const u32 uo = (NST * 64 + t) * 3072;
      float d;
      DOTS(KB, d);
      asm volatile("" : "+v"(d));
      SCHED();
#pragma unroll
      for (int c = 0; c < 3; ++c) KB[c] = __builtin_amdgcn_raw_buffer_load_b128(rk, ll + c * 128, uo, KAUX);
      SCHED();
      u32x4 a;
      WEIGHT(d, a);
#pragma unroll
      for (int c = 0; c < 3; ++c) {
        acc[c] = mfma(a, VB[c], acc[c]);
        asm volatile("" : "+v"(acc[c]));
        SCHED();
        VB[c] = __builtin_amdgcn_raw_buffer_load_b128(rv, ll + c * 128, uo, VAUX);
        SCHED();
      }
      RETRY(1u << 20);
    }
    {
      float d;
      DOTS(KB, d);
      asm volatile("" : "+v"(d));
      SCHED();
#pragma unroll
      for (int i = 0; i < 4; ++i) WO[i] = __builtin_amdgcn_raw_buffer_load_b128(rw, wl + i * (DM * 2), s * 128, WAUX);
      WO[4] = __builtin_amdgcn_raw_buffer_load_b128(rw, wl, (s & 3) * (DM * 2) + (8 + (s >> 2)) * 128, WAUX);
      xraw = __builtin_nontemporal_load(x + (red_b ? 512 + s * 16 + (l & 15) : s * 64 + l));
      SCHED();
      u32x4 a;
      WEIGHT(d, a);
      ls = sum_hi3(ls);
      if (l == 0) { m_s[sw] = mw; l_s[sw] = ls; }
      SCHED();
#pragma unroll
      for (int c = 0; c < 3; ++c) acc[c] = mfma(a, VB[c], acc[c]);
    }
    STAMP(5);
    if (useful) {
#pragma unroll
      for (int c = 0; c < 3; ++c) *reinterpret_cast<f32x4*>(op_s + (sw * 2 + hh) * 192 + c * 64 + dcol) = acc[c];
    }
    wg_barrier();
    STAMP(6);
    u32 opk[4];
    if (sw < 6) {
      const u32 goff = grec + j * REC + R_G2 + sw * 128 + r * 16;
      const u32 soff = grec + j * REC + R_ST;
      u32x2 SS;
      u32x4 O;
      bool ok = false;
      u32 spins = 0;
      for (;;) {
        if (!ok) {
          SS = __builtin_amdgcn_raw_buffer_load_b64(wsr, soff, 0, 16);
          O = __builtin_amdgcn_raw_buffer_load_b128(wsr, goff, 0, 16);
          ok = (O[0] & O[1] & O[2] & O[3] & SS[0] & SS[1] & 1) != 0;
        }
        if (__all(ok) || ++spins > SPIN_CAP) break;
        __builtin_amdgcn_s_sleep(POLL_SLEEP);
      }
      STAMP(7);
      const float ms = __uint_as_float(SS[0] & ~1u);
      const float f = __builtin_amdgcn_exp2f(ms - max_lo3(ms));
      const float sc = f * __builtin_amdgcn_rcpf(sum_lo3(f * __uint_as_float(SS[1])));
#pragma unroll
      for (int i = 0; i < 4; ++i) opk[i] = pkbf(sum_lo3(sc * __uint_as_float(O[i])));
    } else {
      const u32x4 hq = *reinterpret_cast<const u32x4*>(h_s + (sw - 6) * 32 + 4 * r);
#pragma unroll
      for (int i = 0; i < 4; ++i) opk[i] = hq[i];
    }
    {
      f32x4 y1 = {0.f, 0.f, 0.f, 0.f}, y2 = y1;
      const int s3 = s & 3;
      const u32 o2 = s3 == 0 ? opk[0] : s3 == 1 ? opk[1] : s3 == 2 ? opk[2] : opk[3];
      y2 = mfma(msk & o2, WO[4], y2);
#pragma unroll
      for (int i = 0; i < 4; ++i) y1 = mfma(msk & opk[i], WO[i], y1);
      if (useful) {
        *reinterpret_cast<f32x4*>(yp_s + (sw * 2 + hh) * 128 + dcol) = y1;
        *reinterpret_cast<f32x4*>(yp_s + (sw * 2 + hh) * 128 + 64 + dcol) = y2;
      }
    }
    wg_barrier();
    STAMP(8);
    if (is_red) {
      const u32 goff = red_b ? WS_REC + ((4 * sw + ((l >> 2) & 3)) * NS + (s & 4) + (l >> 4)) * REC + R_G3 + 256 + (s & 3) * 64 + (l & 3) * 16
                             : WS_REC + ((4 * sw + (l >> 4)) * NS + s) * REC + R_G3 + (l & 15) * 16;
      u32x4 Y;
      bool ok = false;
      u32 spins = 0;
      for (;;) {
        if (!ok) {
          Y = __builtin_amdgcn_raw_buffer_load_b128(wsr, goff, 0, 16);
          ok = (Y[0] & Y[1] & Y[2] & Y[3] & 1) != 0;
        }
        if (__all(ok) || ++spins > SPIN_CAP) break;
        __builtin_amdgcn_s_sleep(1);
      }
      float t = treduce4(__uint_as_float(Y[0]), __uint_as_float(Y[1]), __uint_as_float(Y[2]), __uint_as_float(Y[3]));
      const int k2 = 2 * ((l >> 4) & 1) + (l >> 5);
      if (red_b) {
        t += dpp<ROR4>(t); t += dpp<ROR8>(t);
        if ((l & 12) == 0) fin_s[sw * 64 + 4 * (l & 3) + k2] = t;
      } else {
        fin_s[sw * 64 + 4 * (l & 15) + k2] = t;
      }
      wg_barrier();
      if (sw == 0) {
        if (l < (red_b ? 16 : 64)) {
          float y = 0.f;
#pragma unroll
          for (int i = 0; i < 8; ++i) y += fin_s[i * 64 + l];
          out[red_b ? 512 + s * 16 + l : s * 64 + l] = __bfloat16_as_ushort(__float2bfloat16(__uint_as_float(xraw << 16) + y));
        }
        if (b == 0 && l == 0) st_gran((gu64*)(ws + WS_EPOCH), ((u64)cnt[14] | ((u64)cnt[15] << 32)) + 1);
      }
    }
  } else {
    const u32x4 xa = ldnt((const char*)(x + l * 8));
    const u32 xb = __builtin_nontemporal_load(reinterpret_cast<const u32*>(x + 512 + l * 2));
    const u32x2 ep2 = __builtin_amdgcn_raw_buffer_load_b64(wsr, WS_EPOCH, 0, EPA);
    {
      const u32x4 z = {0u, 0u, 0u, 0u};
      __builtin_amdgcn_raw_buffer_store_b128(z, wsr, (w == 3 && l < 49) ? myrec + R_G2 + l * 16 : OOB, 0, 16);
      __builtin_amdgcn_raw_buffer_store_b128(z, wsr, (w == 3 && l < (s == 0 ? 33 : 32)) ? myrec + R_G3 + l * 16 : OOB, 0, 16);
    }
    SCHED();
    if (tid < 14) cnt[tid] = 0;
    SCHED();
    const int qd = g >> 2, wi = (g & 3) * 8 + s;
    const int lnA = (45 * wi) / 80, lnB = (45 * wi + 44) / 80;
    const int tb = (lnA + 1) * 80 - 45 * wi;
    u32x4 S1[12];
    {
      const char* pA = lnA < 12 ? wq + (size_t)(qd * 768 + lnA * 64) * 2 : (lnA < 15 ? w1 : w3) + (size_t)(qd * 192 + (lnA % 3) * 64) * 2;
      const char* pB = lnB < 12 ? wq + (size_t)(qd * 768 + lnB * 64) * 2 : (lnB < 15 ? w1 : w3) + (size_t)(qd * 192 + (lnB % 3) * 64) * 2;
      const u32 rsA = lnA < 12 ? NH * DH * 2 : FF * 2, rsB = lnB < 12 ? NH * DH * 2 : FF * 2;
      const u32 vA = r * rsA + j * 16, vB = r * rsB + j * 16;
#pragma unroll
      for (int i = 0; i < 12; ++i) {
        const int t = min(w + 4 * i, 44);
        const bool inA = t < tb;
        const char* pb = (inA ? pA : pB) + (u32)((inA ? 80 - tb + t : t - tb) * 8 * (inA ? rsA : rsB));
        S1[i] = WAUX ? ldnt(pb + (inA ? vA : vB)) : *reinterpret_cast<const u32x4*>(pb + (inA ? vA : vB));
      }
    }
    SCHED();
    wg_barrier();
    SCHED();
    {
      float xv[10];
#pragma unroll
      for (int k = 0; k < 4; ++k) { xv[2 * k] = bf_lo(xa[k]); xv[2 * k + 1] = bf_hi(xa[k]); }
      xv[8] = bf_lo(xb); xv[9] = bf_hi(xb);
      float sm = 0.f;
#pragma unroll
      for (int k = 0; k < 10; ++k) sm += xv[k];
      sm = sum_hi3(sum_lo3(sm));
      const float mean = sm * (1.f / DM);
      float sv = 0.f;
#pragma unroll
      for (int k = 0; k < 10; ++k) { const float d = xv[k] - mean; sv = fmaf(d, d, sv); }
      sv = sum_hi3(sum_lo3(sv));
      const float var = rbf(sv * (1.f / DM));
      const float mb = rbf(mean);
      const float sd = rbf(sqrtf(rbf(var + 1e-5f)));
      u32 xp[10];
#pragma unroll
      for (int k = 0; k < 10; ++k) xp[k] = pkbf(rbf(xv[k] - mb) / sd);
      *reinterpret_cast<u32x4*>(xn_s + l * 8) = u32x4{xp[0], xp[1], xp[2], xp[3]};
      *reinterpret_cast<u32x4*>(xn_s + l * 8 + 4) = u32x4{xp[4], xp[5], xp[6], xp[7]};
      *reinterpret_cast<u32x2*>(xn_s + 512 + l * 2) = u32x2{xp[8], xp[9]};
      asm volatile("s_waitcnt lgkmcnt(0)" ::: "memory");
    }
    const u64 tagsh = mktag((u64)ep2[0] | ((u64)ep2[1] << 32));
    const u32 taglo = (u32)tagsh, taghi = (u32)(tagsh >> 32);
    {
      u32 xr[12];
#pragma unroll
      for (int i = 0; i < 12; ++i) { const int t = min(w + 4 * i, 44); xr[i] = xn_s[(t < tb ? 80 - tb + t : t - tb) * 8 + r]; }
      if (w != 0) xr[11] = 0u;
      f32x4 a1 = {0.f, 0.f, 0.f, 0.f}, a2 = a1;
#pragma unroll
      for (int i = 0; i < 12; ++i) {
        const bool inA = min(w + 4 * i, 44) < tb;
        a1 = mfma(msk & (inA ? xr[i] : 0u), S1[i], a1);
        a2 = mfma(msk & (inA ? 0u : xr[i]), S1[i], a2);
      }
      if (useful) {
        *reinterpret_cast<f32x4*>(s1_s + (w * 2 + hh) * 64 + dcol) = a1;
        *reinterpret_cast<f32x4*>(s1_s + (8 + w * 2 + hh) * 64 + dcol) = a2;
      }
      asm volatile("s_waitcnt vmcnt(0)" ::: "memory");
      if (lds_ticket(cnt + C_S1, l) == 3) {
        float va = 0.f, vb = 0.f;
#pragma unroll
        for (int t = 0; t < 8; ++t) { va += s1_s[t * 64 + l]; vb += s1_s[(8 + t) * 64 + l]; }
        __builtin_amdgcn_raw_buffer_store_b64(u32x2{taglo | (__float_as_uint(va) >> 8), taghi}, wsr, myrec + R_G1 + l * 8, 0, G1A);
        __builtin_amdgcn_raw_buffer_store_b64(u32x2{taglo | (__float_as_uint(vb) >> 8), taghi}, wsr, lnB != lnA ? myrec + R_G1 + 512 + l * 8 : OOB, 0, G1A);
      }
    }
    STAMP(1);
    lds_wait(cnt + C_PROG, 8);
    if (w != 2 && (w != 1 || is_red)) {
      u32 ga[10];
      bool use3[3];
#pragma unroll
      for (int k = 0; k < 3; ++k) {
        const int fc = (g & 3) * 48 + min(l, 47);
        const int ln = w == 0 ? (g & 3) * 3 + k : 12 + 3 * min(k, 1) + (fc >> 6);
        const int col = w == 0 ? l : (fc & 63);
        const int f0 = (80 * ln) / 45, f1 = (80 * ln + 79) / 45;
        use3[k] = f0 + 2 <= f1;
#pragma unroll
        for (int i = 0; i < 3; ++i) {
          const int pw = min(f0 + i, f1);
          ga[k * 3 + i] = WS_REC + (qd * 32 + pw) * REC + R_G1 + (45 * pw < 80 * ln ? 512 : 0) + col * 8;
        }
      }
      ga[9] = grec + j * REC + R_G1;
      if (w == 1) {
        ga[9] = WS_REC + ((l & 31) * NS + (red_b ? (s & 4) + hh : s)) * REC + R_G1;
#pragma unroll
        for (int i = 0; i < 9; ++i) ga[i] = ga[9] + (red_b ? 2 * REC : 0);
      }
      u32x2 G[10];
      bool ok = false;
      u32 spins = 0;
      for (;;) {
        if (!ok) {
#pragma unroll
          for (int i = 0; i < 10; ++i) if (w == 0 || (w == 3 && i < 6) || (w == 1 && i >= 8)) G[i] = __builtin_amdgcn_raw_buffer_load_b64(wsr, ga[i], 0, G1A);
          u32 d_ = 0;
#pragma unroll
          for (int i = 0; i < 10; ++i) if (w == 0 || (w == 3 && i < 6) || (w == 1 && i >= 8)) d_ |= (G[i][1] ^ taghi) | ((G[i][0] ^ taglo) & 0xff000000u);
          ok = (d_ == 0);
        }
        if (__all(ok) || ++spins > SPIN_CAP) break;
        RETRY(w == 0 ? 1 : w == 3 ? 1000 : 100000);
        __builtin_amdgcn_s_sleep(8);
      }
      float t[3];
#pragma unroll
      for (int k = 0; k < 3; ++k) t[k] = __uint_as_float(G[3 * k][0] << 8) + __uint_as_float(G[3 * k + 1][0] << 8) + (use3[k] ? __uint_as_float(G[3 * k + 2][0] << 8) : 0.f);
      if (w == 0) {
#pragma unroll
        for (int k = 0; k < 3; ++k) ((u16*)q_s)[64 * k + l] = __bfloat16_as_ushort(__float2bfloat16(t[k]));
        if (l == 0) { cnt[14] = ep2[0]; cnt[15] = ep2[1]; }
        lds_arrive(cnt + C_Q, l);
      } else if (w == 3) {
        const float gate = rbf(t[0]), up = rbf(t[1]);
        const float hv = rbf(gate / (1.f + __expf(-gate))) * up;
        h_s[l] = l < 48 ? pkbf(hv) : 0u;
      }
    }
    STAMP(2);
    __builtin_amdgcn_s_setprio(3);
    wg_barrier();
    STAMP(5);
    {
      const int col = min(tid, 191);
      float pv[16];
#pragma unroll
      for (int i = 0; i < 16; ++i) pv[i] = op_s[i * 192 + col];
      const float mj = m_s[j];
      const float M = max_lo3(mj);
      const float fj = __builtin_amdgcn_exp2f(mj - M);
      float o = 0.f;
      if (w == 3) {
        const float L = sum_lo3(l_s[j] * fj);
        o = (l & 1) ? L : M;
      } else {
#pragma unroll
        for (int i = 0; i < 8; ++i) o = fmaf(__uint_as_float(__builtin_amdgcn_readlane(__float_as_uint(fj), i)), pv[2 * i] + pv[2 * i + 1], o);
      }
      __builtin_amdgcn_raw_buffer_store_b32(__float_as_uint(o) | 1u, wsr, tid < 194 ? myrec + R_G2 + tid * 4 : OOB, 0, 16);
    }
    STAMP(6);
    wg_barrier();
    STAMP(7);
    if (w < 2) {
      float y = 0.f;
#pragma unroll
      for (int i = 0; i < 16; ++i) y += yp_s[i * 128 + tid];
      __builtin_amdgcn_raw_buffer_store_b32(__float_as_uint(y) | 1u, wsr, myrec + R_G3 + tid * 4, 0, 16);
    }
    STAMP(8);
    if (is_red) wg_barrier();
  }
  STAMP(9);
}

extern "C" void kernel_launch(void* const* d_in, const int* in_sizes, int n_in, void* d_out, int out_size, void* d_ws,
                              size_t ws_size, hipStream_t stream) {
  fused_decode_kernel<<<256, 768, 0, stream>>>((const u16*)d_in[0], (const char*)d_in[1], (const char*)d_in[2],
                                               (const char*)d_in[3], (const char*)d_in[4], (const char*)d_in[6],
                                               (const char*)d_in[7], (const char*)d_in[8], (u16*)d_out, (char*)d_ws);
}
```

```cpp
#define GPOS 5
#define NST 8
#define MAPX 0
#define LEAK 0
#include <hip/hip_runtime.h>
#include <hip/hip_bf16.h>
#include <stdint.h>

typedef unsigned short u16;
typedef unsigned int u32;
typedef unsigned long long u64;
typedef u32 u32x4 __attribute__((ext_vector_type(4)));
typedef u32 u32x2 __attribute__((ext_vector_type(2)));
typedef float f32x2 __attribute__((ext_vector_type(2)));
typedef float f32x4 __attribute__((ext_vector_type(4)));
typedef __attribute__((ext_vector_type(2))) __bf16 bf16x2;
typedef __attribute__((ext_vector_type(8))) __bf16 bf16x8;
#define GAS __attribute__((address_space(1)))
#define LAS __attribute__((address_space(3)))
typedef GAS u64 gu64;
typedef GAS u32 gu32;
#define RLX_AGENT __ATOMIC_RELAXED, __HIP_MEMORY_SCOPE_AGENT

#define POLL_SLEEP 2
#define PD 4
#define G1A 18
#define S1FIRST 1
#define EPA 18
#define DIAG 0

constexpr int DM = 640, NH = 32, DH = 192, SK = 32768, WIN = 4096, FF = 1536;
constexpr int NS = 8, KPS = WIN / NS;
constexpr u32 WS_EPOCH = 0;
constexpr u32 WS_REC = 4096, REC = 2560;
constexpr u32 R_G1 = 0;
constexpr u32 R_G2 = 1024;
constexpr u32 R_ST = 1792;
constexpr u32 R_G3 = 1920;
constexpr u32 R_CTR = 2432;
constexpr u32 WS_END = WS_REC + NH * NS * REC;
constexpr int WS_STAMP = 1 << 19;
constexpr int NWAVE = 12, NHELP = 4;
constexpr u32 SPIN_CAP = 1u << 22;
constexpr float LOG2E = 1.4426950408889634f;

__device__ __forceinline__ float bf_lo(u32 v) { return __uint_as_float(v << 16); }
__device__ __forceinline__ float bf_hi(u32 v) { return __uint_as_float(v & 0xffff0000u); }
__device__ __forceinline__ float rbf(float f) { return __bfloat162float(__float2bfloat16(f)); }
__device__ __forceinline__ u32 pkbf(float f) { const bf16x2 h = {(__bf16)f, (__bf16)f}; return __builtin_bit_cast(u32, h); }
__device__ __forceinline__ u32x4 ldnt(const char* p) { return __builtin_nontemporal_load(reinterpret_cast<const u32x4*>(p)); }
__device__ __forceinline__ float dot2(u32 a, u32 b, float c) {
  return __builtin_amdgcn_fdot2_f32_bf16(__builtin_bit_cast(bf16x2, a), __builtin_bit_cast(bf16x2, b), c, false);
}
__device__ __forceinline__ f32x4 mfma(u32x4 a, u32x4 b, f32x4 c) {
  return __builtin_amdgcn_mfma_f32_16x16x32_bf16(__builtin_bit_cast(bf16x8, a), __builtin_bit_cast(bf16x8, b), c, 0, 0, 0);
}
template <int CTRL> __device__ __forceinline__ float dpp(float x) {
  return __builtin_bit_cast(float, __builtin_amdgcn_mov_dpp(__builtin_bit_cast(int, x), CTRL, 0xf, 0xf, true));
}
constexpr int XOR1 = 0xB1, XOR2 = 0x4E, HMIR = 0x141, ROR8 = 0x128, ROR4 = 0x124;
__device__ __forceinline__ float swap16_add(float a, float b) {
  auto s = __builtin_amdgcn_permlane16_swap(__float_as_uint(a), __float_as_uint(b), false, false);
  return __uint_as_float(s[0]) + __uint_as_float(s[1]);
}
__device__ __forceinline__ float swap32_add(float a, float b) {
  auto s = __builtin_amdgcn_permlane32_swap(__float_as_uint(a), __float_as_uint(b), false, false);
  return __uint_as_float(s[0]) + __uint_as_float(s[1]);
}
__device__ __forceinline__ float swap16_max(float a) {
  auto s = __builtin_amdgcn_permlane16_swap(__float_as_uint(a), __float_as_uint(a), false, false);
  return fmaxf(__uint_as_float(s[0]), __uint_as_float(s[1]));
}
__device__ __forceinline__ float swap32_max(float a) {
  auto s = __builtin_amdgcn_permlane32_swap(__float_as_uint(a), __float_as_uint(a), false, false);
  return fmaxf(__uint_as_float(s[0]), __uint_as_float(s[1]));
}
__device__ __forceinline__ float sum_hi3(float x) { x += dpp<ROR8>(x); x = swap16_add(x, x); return swap32_add(x, x); }
__device__ __forceinline__ float max_hi3(float x) { x = fmaxf(x, dpp<ROR8>(x)); x = swap16_max(x); return swap32_max(x); }
__device__ __forceinline__ float sum_lo3(float x) { x += dpp<XOR1>(x); x += dpp<XOR2>(x); x += dpp<HMIR>(x); return x; }
__device__ __forceinline__ float max_lo3(float x) { x = fmaxf(x, dpp<XOR1>(x)); x = fmaxf(x, dpp<XOR2>(x)); x = fmaxf(x, dpp<HMIR>(x)); return x; }
__device__ __forceinline__ float treduce8(const float* a, bool b3) {
  float v[4];
#pragma unroll
  for (int k = 0; k < 4; ++k) {
    const float keep = b3 ? a[k + 4] : a[k], send = b3 ? a[k] : a[k + 4];
    v[k] = keep + dpp<ROR8>(send);
  }
  const float u0 = swap16_add(v[0], v[2]), u1 = swap16_add(v[1], v[3]);
  return swap32_add(u0, u1);
}
__device__ __forceinline__ float treduce4(float a0, float a1, float a2, float a3) {
  return swap32_add(swap16_add(a0, a2), swap16_add(a1, a3));
}
__device__ __forceinline__ void lds_arrive(u32* c, int l) {
  asm volatile("s_waitcnt lgkmcnt(0)" ::: "memory");
  if (l == 0) __hip_atomic_fetch_add((LAS u32*)c, 1u, __ATOMIC_RELAXED, __HIP_MEMORY_SCOPE_WORKGROUP);
  asm volatile("" ::: "memory");
}
__device__ __forceinline__ u32 lds_ticket(u32* c, int l) {
  asm volatile("s_waitcnt lgkmcnt(0)" ::: "memory");
  u32 t = 0;
  if (l == 0) t = __hip_atomic_fetch_add((LAS u32*)c, 1u, __ATOMIC_RELAXED, __HIP_MEMORY_SCOPE_WORKGROUP);
  t = __builtin_amdgcn_readfirstlane(t);
  asm volatile("" ::: "memory");
  return t;
}
__device__ __forceinline__ void lds_wait(u32* c, u32 n) {
  asm volatile("" ::: "memory");
  while (__builtin_amdgcn_readfirstlane(__hip_atomic_load((LAS u32*)c, __ATOMIC_RELAXED, __HIP_MEMORY_SCOPE_WORKGROUP)) < n) __builtin_amdgcn_s_sleep(1);
  asm volatile("" ::: "memory");
}
__device__ __forceinline__ u64 mktag(u64 epoch) { return ((epoch + 1) << 24) | (1ull << 63); }
__device__ __forceinline__ u64 mkgran(u64 tagsh, float v) { return tagsh | (u64)(__float_as_uint(v) >> 8); }
__device__ __forceinline__ float granval(u64 x) { return __uint_as_float(((u32)x) << 8); }
__device__ __forceinline__ bool granok(u64 x, u64 tagsh) { return ((x ^ tagsh) >> 24) == 0; }
__device__ __forceinline__ void st_gran(gu64* p, u64 v) { __hip_atomic_store(p, v, RLX_AGENT); }
__device__ __forceinline__ u64 ld_gran(gu64* p) { return __hip_atomic_load(p, RLX_AGENT); }
__device__ __forceinline__ void st_val(gu32* p, float v) { __hip_atomic_store(p, __float_as_uint(v) | 1u, RLX_AGENT); }
#define SCHED() __builtin_amdgcn_sched_barrier(0)
__device__ __forceinline__ void wg_barrier() {
  asm volatile("s_waitcnt lgkmcnt(0)" ::: "memory");
  __builtin_amdgcn_s_barrier();
  asm volatile("" ::: "memory");
}
#define STAMP(i) do { if (LEAK == ((w < 4) ? 100 : 200) + (i) && (w == 0 || w == 4) && l == 0) { leak_s[0] = __builtin_amdgcn_s_memrealtime(); } } while (0)
#define RETRY(n)

enum { C_S1 = 0, C_PROG, C_Q, C_N };
constexpr u32 OOB = 0xfffffff0u;
constexpr u32 POOL = (8 - NST) * 64;

__global__ void __launch_bounds__(768, 3)
fused_decode_kernel(const u16* __restrict__ x, const char* __restrict__ kc, const char* __restrict__ vc,
                    const char* __restrict__ wq, const char* __restrict__ wo, const char* __restrict__ w1,
                    const char* __restrict__ w3, const char* __restrict__ w2, u16* __restrict__ out, char* ws) {
  __shared__ __attribute__((aligned(16))) float lds[640 + 96 + 32 + 64 + 16 * 64 + 16 * 192 + 16 * 128 + 8 * 64];
  u32* xn_s = (u32*)lds;
  u32* q_s = (u32*)(lds + 640);
  float* m_s = lds + 736;
  float* l_s = lds + 744;
  u32* cnt = (u32*)(lds + 752);
  u32* h_s = (u32*)(lds + 768);
  float* s1_s = lds + 832;
  float* op_s = s1_s + 16 * 64;
  float* yp_s = op_s + 16 * 192;
  float* fin_s = yp_s + 16 * 128;
  const u64 tstart = __builtin_amdgcn_s_memrealtime();
  __shared__ u64 leak_s[2];
  const int b = blockIdx.x;
  const int g = MAPX ? b >> 3 : (b & 7) * 4 + (b >> 6), s = MAPX ? b & 7 : (b >> 3) & 7;
  const int tid = threadIdx.x;
  const int w = __builtin_amdgcn_readfirstlane(tid >> 6), l = tid & 63;
  const int r = l >> 3, j = l & 7;
  const __amdgpu_buffer_rsrc_t wsr = __builtin_amdgcn_make_buffer_rsrc(ws, 0, WS_END, 0x00020000);
  const u32 grec = WS_REC + g * NS * REC, myrec = grec + s * REC;
  const bool is_red = (s == (g >> 2)) && !(g & 1);
  const bool red_b = (g & 2) != 0;
  u32x4 msk;
#pragma unroll
  for (int d = 0; d < 4; ++d) msk[d] = (j >> 1) == d ? ((j & 1) ? 0xffff0000u : 0x0000ffffu) : 0u;
  const int hh = l >> 5;
  const bool useful = ((l >> 3) & 1) == hh;
  const int dcol = 8 * j + 4 * ((l >> 4) & 1);
#define STR2(x) #x
#define STR(x) STR2(x)
#define PACE() asm volatile("s_waitcnt vmcnt(" STR(PD) ")" ::: "memory")

  if (w >= 4) {
    const int sw = w - 4;
    const size_t hbase = ((size_t)g * SK + (SK - WIN)) * (DH * 2);
    const __amdgpu_buffer_rsrc_t rk = __builtin_amdgcn_make_buffer_rsrc((void*)(kc + hbase), 0, WIN * DH * 2, 0x00020000);
    const __amdgpu_buffer_rsrc_t rv = __builtin_amdgcn_make_buffer_rsrc((void*)(vc + hbase), 0, WIN * DH * 2, 0x00020000);
    const u32 ll = (u32)(r * 384 + j * 16);
    const u32 sbase = (s * NST * 8 + sw) * 3072;
    constexpr int NB = NST - 1;
    u32x4 K[NB][3], V[NB][3], KB[3], VB[3];
#define LOAD3(dst, rs, off) do { _Pragma("unroll") for (int c = 0; c < 3; ++c) { PACE(); dst[c] = __builtin_amdgcn_raw_buffer_load_b128(rs, ll + c * 128, off, 2); } SCHED(); } while (0)
    wg_barrier();
#pragma unroll
    for (int u = S1FIRST ? 0 : 1; u < NB; ++u) { LOAD3(K[u], rk, sbase + u * 24576); if (u == GPOS) lds_arrive(cnt + C_PROG, l); }
    LOAD3(KB, rk, sbase + NB * 24576);
    if (NB == GPOS) lds_arrive(cnt + C_PROG, l);
    STAMP(1);
#pragma unroll
    for (int u = 0; u < 3; ++u) { LOAD3(V[u], rv, sbase + u * 24576); if (NB + 1 + u == GPOS) lds_arrive(cnt + C_PROG, l); }
    STAMP(2);
    lds_wait(cnt + C_Q, 1);
    STAMP(3);
    u32 tk = 0xffffffffu;
    const char* ctrp = ws + grec + R_CTR;
    const u32 zero_ = 0, one_ = 1;
#define CLAIM() do { if (POOL) { u64 sv_; asm volatile("s_mov_b64 %1, exec\n\ts_mov_b64 exec, 1\n\tglobal_atomic_add %0, %2, %3, %4 sc0\n\ts_mov_b64 exec, %1" \
      : "=&v"(tk), "=&s"(sv_) : "v"(zero_), "v"(one_), "s"(ctrp) : "memory"); } } while (0)
#define TICKET(t_) do { if (POOL) asm volatile("s_waitcnt vmcnt(6)" : "+v"(tk) :: "memory"); t_ = __builtin_amdgcn_readfirstlane(tk); } while (0)
    u32x4 q[3];
#pragma unroll
    for (int c = 0; c < 3; ++c) q[c] = *reinterpret_cast<const u32x4*>(q_s + c * 32 + j * 4);
#define DOTS(Kx, d_) do { float d0 = 0.f, d1 = 0.f, d2 = 0.f; _Pragma("unroll") for (int k = 0; k < 4; ++k) { d0 = dot2(Kx[0][k], q[0][k], d0); d1 = dot2(Kx[1][k], q[1][k], d1); d2 = dot2(Kx[2][k], q[2][k], d2); } \
      d_ = d0 + d1 + d2; } while (0)
#define SFIN(d_, fl_, fr_) do { const float t_ = rbf(rbf(sum_lo3(d_)) * 0.07216878364870322f) * LOG2E; fl_ = floorf(t_); fr_ = __builtin_amdgcn_exp2f(t_ - fl_); } while (0)
#define SCORE(Kx, fl_, fr_) do { float dd_; DOTS(Kx, dd_); SFIN(dd_, fl_, fr_); } while (0)
    float mw, ls;
    f32x4 acc[3];
#pragma unroll
    for (int c = 0; c < 3; ++c) acc[c] = f32x4{0.f, 0.f, 0.f, 0.f};
#define PVX(pk_, Vx) do { const u32x4 a_ = msk & (pk_); _Pragma("unroll") for (int c = 0; c < 3; ++c) acc[c] = mfma(a_, Vx[c], acc[c]); \
      asm volatile("" : "+v"(acc[0]), "+v"(acc[1]), "+v"(acc[2])); SCHED(); } while (0)
    {
      float fl[NB], fr[NB];
#pragma unroll
      for (int u = 0; u < NB; ++u) {
        SCORE(K[u], fl[u], fr[u]);
        asm volatile("" : "+v"(fl[u]), "+v"(fr[u]));
        SCHED();
        if (u + 3 == NB - 1) CLAIM();
        if (u + 3 < NB) LOAD3(V[u + 3], rv, sbase + (u + 3) * 24576);
      }
      LOAD3(VB, rv, sbase + NB * 24576);
      float mx = fl[0];
#pragma unroll
      for (int u = 1; u < NB; ++u) mx = fmaxf(mx, fl[u]);
      mw = max_hi3(mx) + 1.f;
      ls = 0.f;
#pragma unroll
      for (int u = 0; u < NB; ++u) { const float e = ldexpf(fr[u], (int)(fl[u] - mw)); ls += e; PVX(pkbf(e), V[u]); }
    }
    STAMP(4);
#define WEIGHT(d_, a_) do { float fl_, fr_; SFIN(d_, fl_, fr_); \
      if (__any(fl_ >= mw)) { const float mn_ = max_hi3(fl_) + 1.f; const float al_ = ldexpf(1.f, (int)(mw - mn_)); mw = mn_; ls *= al_; \
        _Pragma("unroll") for (int c = 0; c < 3; ++c) acc[c] *= al_; } \
      const float e_ = ldexpf(fr_, (int)(fl_ - mw)); ls += e_; a_ = msk & pkbf(e_); } while (0)
    u32x4 WO[5];
    u32 xraw;
    const __amdgpu_buffer_rsrc_t rw = __builtin_amdgcn_make_buffer_rsrc(
        (void*)((sw < 6) ? wo + (size_t)(g * DH + sw * 32) * (DM * 2) : w2 + (size_t)(g * 48 + (sw - 6) * 32) * (DM * 2)), 0, 32 * DM * 2, 0x00020000);
    const u32 wl = ((sw == 7) ? (r & 3) : r) * (4 * DM * 2) + j * 16;
    for (;;) {
      u32 t;
      TICKET(t);
      if (t >= POOL) break;
      CLAIM();
      const u32 uo = (NST * 64 + t) * 3072;
      float d;
      DOTS(KB, d);
      asm volatile("" : "+v"(d));
      SCHED();
#pragma unroll
      for (int c = 0; c < 3; ++c) KB[c] = __builtin_amdgcn_raw_buffer_load_b128(rk, ll + c * 128, uo, 2);
      SCHED();
      u32x4 a;
      WEIGHT(d, a);
#pragma unroll
      for (int c = 0; c < 3; ++c) {
        acc[c] = mfma(a, VB[c], acc[c]);
        asm volatile("" : "+v"(acc[c]));
        SCHED();
        VB[c] = __builtin_amdgcn_raw_buffer_load_b128(rv, ll + c * 128, uo, 2);
        SCHED();
      }
      RETRY(1u << 20);
    }
    {
      float d;
      DOTS(KB, d);
      asm volatile("" : "+v"(d));
      SCHED();
#pragma unroll
      for (int i = 0; i < 4; ++i) WO[i] = __builtin_amdgcn_raw_buffer_load_b128(rw, wl + i * (DM * 2), s * 128, 2);
      WO[4] = __builtin_amdgcn_raw_buffer_load_b128(rw, wl, (s & 3) * (DM * 2) + (8 + (s >> 2)) * 128, 2);
      xraw = __builtin_nontemporal_load(x + (red_b ? 512 + s * 16 + (l & 15) : s * 64 + l));
      SCHED();
      u32x4 a;
      WEIGHT(d, a);
      ls = sum_hi3(ls);
      if (l == 0) { m_s[sw] = mw; l_s[sw] = ls; }
      SCHED();
#pragma unroll
      for (int c = 0; c < 3; ++c) acc[c] = mfma(a, VB[c], acc[c]);
    }
    STAMP(5);
    if (useful) {
#pragma unroll
      for (int c = 0; c < 3; ++c) *reinterpret_cast<f32x4*>(op_s + (sw * 2 + hh) * 192 + c * 64 + dcol) = acc[c];
    }
    wg_barrier();
    STAMP(6);
    u32 opk[4];
    if (sw < 6) {
      const u32 goff = grec + j * REC + R_G2 + sw * 128 + r * 16;
      const u32 soff = grec + j * REC + R_ST;
      u32x2 SS;
      u32x4 O;
      bool ok = false;
      u32 spins = 0;
      for (;;) {
        if (!ok) {
          SS = __builtin_amdgcn_raw_buffer_load_b64(wsr, soff, 0, 16);
          O = __builtin_amdgcn_raw_buffer_load_b128(wsr, goff, 0, 16);
          ok = (O[0] & O[1] & O[2] & O[3] & SS[0] & SS[1] & 1) != 0;
        }
        if (__all(ok) || ++spins > SPIN_CAP) break;
        __builtin_amdgcn_s_sleep(POLL_SLEEP);
      }
      STAMP(7);
      const float ms = __uint_as_float(SS[0] & ~1u);
      const float f = __builtin_amdgcn_exp2f(ms - max_lo3(ms));
      const float sc = f * __builtin_amdgcn_rcpf(sum_lo3(f * __uint_as_float(SS[1])));
#pragma unroll
      for (int i = 0; i < 4; ++i) opk[i] = pkbf(sum_lo3(sc * __uint_as_float(O[i])));
    } else {
      const u32x4 hq = *reinterpret_cast<const u32x4*>(h_s + (sw - 6) * 32 + 4 * r);
#pragma unroll
      for (int i = 0; i < 4; ++i) opk[i] = hq[i];
    }
    {
      f32x4 y1 = {0.f, 0.f, 0.f, 0.f}, y2 = y1;
      const int s3 = s & 3;
      const u32 o2 = s3 == 0 ? opk[0] : s3 == 1 ? opk[1] : s3 == 2 ? opk[2] : opk[3];
      y2 = mfma(msk & o2, WO[4], y2);
#pragma unroll
      for (int i = 0; i < 4; ++i) y1 = mfma(msk & opk[i], WO[i], y1);
      if (useful) {
        *reinterpret_cast<f32x4*>(yp_s + (sw * 2 + hh) * 128 + dcol) = y1;
        *reinterpret_cast<f32x4*>(yp_s + (sw * 2 + hh) * 128 + 64 + dcol) = y2;
      }
    }
    wg_barrier();
    STAMP(8);
    if (is_red) {
      const u32 goff = red_b ? WS_REC + ((4 * sw + ((l >> 2) & 3)) * NS + (s & 4) + (l >> 4)) * REC + R_G3 + 256 + (s & 3) * 64 + (l & 3) * 16
                             : WS_REC + ((4 * sw + (l >> 4)) * NS + s) * REC + R_G3 + (l & 15) * 16;
      u32x4 Y;
      bool ok = false;
      u32 spins = 0;
      for (;;) {
        if (!ok) {
          Y = __builtin_amdgcn_raw_buffer_load_b128(wsr, goff, 0, 16);
          ok = (Y[0] & Y[1] & Y[2] & Y[3] & 1) != 0;
        }
        if (__all(ok) || ++spins > SPIN_CAP) break;
        __builtin_amdgcn_s_sleep(1);
      }
      float t = treduce4(__uint_as_float(Y[0]), __uint_as_float(Y[1]), __uint_as_float(Y[2]), __uint_as_float(Y[3]));
      const int k2 = 2 * ((l >> 4) & 1) + (l >> 5);
      if (red_b) {
        t += dpp<ROR4>(t); t += dpp<ROR8>(t);
        if ((l & 12) == 0) fin_s[sw * 64 + 4 * (l & 3) + k2] = t;
      } else {
        fin_s[sw * 64 + 4 * (l & 15) + k2] = t;
      }
      wg_barrier();
      if (sw == 0) {
        if (l < (red_b ? 16 : 64)) {
          float y = 0.f;
#pragma unroll
          for (int i = 0; i < 8; ++i) y += fin_s[i * 64 + l];
          out[red_b ? 512 + s * 16 + l : s * 64 + l] = __bfloat16_as_ushort(__float2bfloat16(__uint_as_float(xraw << 16) + y));
        }
        if (b == 0 && l == 0) st_gran((gu64*)(ws + WS_EPOCH), ((u64)cnt[14] | ((u64)cnt[15] << 32)) + 1);
        if (b == 0) { const u64 tp = LEAK == 0 ? tstart : LEAK == 299 ? __builtin_amdgcn_s_memrealtime() : leak_s[0]; const u64 tend = tp + 4000; while (__builtin_amdgcn_s_memrealtime() < tend) __builtin_amdgcn_s_sleep(1); }
      }
    }
  } else {
    const u32x4 xa = ldnt((const char*)(x + l * 8));
    const u32 xb = __builtin_nontemporal_load(reinterpret_cast<const u32*>(x + 512 + l * 2));
    const u32x2 ep2 = __builtin_amdgcn_raw_buffer_load_b64(wsr, WS_EPOCH, 0, EPA);
    {
      const u32x4 z = {0u, 0u, 0u, 0u};
      __builtin_amdgcn_raw_buffer_store_b128(z, wsr, (w == 3 && l < 49) ? myrec + R_G2 + l * 16 : OOB, 0, 16);
      __builtin_amdgcn_raw_buffer_store_b128(z, wsr, (w == 3 && l < (s == 0 ? 33 : 32)) ? myrec + R_G3 + l * 16 : OOB, 0, 16);
    }
    SCHED();
    if (tid < 14) cnt[tid] = 0;
    SCHED();
    const int qd = g >> 2, wi = (g & 3) * 8 + s;
    const int lnA = (45 * wi) / 80, lnB = (45 * wi + 44) / 80;
    const int tb = (lnA + 1) * 80 - 45 * wi;
    u32x4 S1[12];
    {
      const char* pA = lnA < 12 ? wq + (size_t)(qd * 768 + lnA * 64) * 2 : (lnA < 15 ? w1 : w3) + (size_t)(qd * 192 + (lnA % 3) * 64) * 2;
      const char* pB = lnB < 12 ? wq + (size_t)(qd * 768 + lnB * 64) * 2 : (lnB < 15 ? w1 : w3) + (size_t)(qd * 192 + (lnB % 3) * 64) * 2;
      const u32 rsA = lnA < 12 ? NH * DH * 2 : FF * 2, rsB = lnB < 12 ? NH * DH * 2 : FF * 2;
      const u32 vA = r * rsA + j * 16, vB = r * rsB + j * 16;
#pragma unroll
      for (int i = 0; i < 12; ++i) {
        const int t = min(w + 4 * i, 44);
        const bool inA = t < tb;
        const char* pb = (inA ? pA : pB) + (u32)((inA ? 80 - tb + t : t - tb) * 8 * (inA ? rsA : rsB));
        S1[i] = ldnt(pb + (inA ? vA : vB));
      }
    }
    SCHED();
    wg_barrier();
    SCHED();
    {
      float xv[10];
#pragma unroll
      for (int k = 0; k < 4; ++k) { xv[2 * k] = bf_lo(xa[k]); xv[2 * k + 1] = bf_hi(xa[k]); }
      xv[8] = bf_lo(xb); xv[9] = bf_hi(xb);
      float sm = 0.f;
#pragma unroll
      for (int k = 0; k < 10; ++k) sm += xv[k];
      sm = sum_hi3(sum_lo3(sm));
      const float mean = sm * (1.f / DM);
      float sv = 0.f;
#pragma unroll
      for (int k = 0; k < 10; ++k) { const float d = xv[k] - mean; sv = fmaf(d, d, sv); }
      sv = sum_hi3(sum_lo3(sv));
      const float var = rbf(sv * (1.f / DM));
      const float mb = rbf(mean);
      const float sd = rbf(sqrtf(rbf(var + 1e-5f)));
      u32 xp[10];
#pragma unroll
      for (int k = 0; k < 10; ++k) xp[k] = pkbf(rbf(xv[k] - mb) / sd);
      *reinterpret_cast<u32x4*>(xn_s + l * 8) = u32x4{xp[0], xp[1], xp[2], xp[3]};
      *reinterpret_cast<u32x4*>(xn_s + l * 8 + 4) = u32x4{xp[4], xp[5], xp[6], xp[7]};
      *reinterpret_cast<u32x2*>(xn_s + 512 + l * 2) = u32x2{xp[8], xp[9]};
      asm volatile("s_waitcnt lgkmcnt(0)" ::: "memory");
    }
    const u64 tagsh = mktag((u64)ep2[0] | ((u64)ep2[1] << 32));
    const u32 taglo = (u32)tagsh, taghi = (u32)(tagsh >> 32);
    {
      u32 xr[12];
#pragma unroll
      for (int i = 0; i < 12; ++i) { const int t = min(w + 4 * i, 44); xr[i] = xn_s[(t < tb ? 80 - tb + t : t - tb) * 8 + r]; }
      if (w != 0) xr[11] = 0u;
      f32x4 a1 = {0.f, 0.f, 0.f, 0.f}, a2 = a1;
#pragma unroll
      for (int i = 0; i < 12; ++i) {
        const bool inA = min(w + 4 * i, 44) < tb;
        a1 = mfma(msk & (inA ? xr[i] : 0u), S1[i], a1);
        a2 = mfma(msk & (inA ? 0u : xr[i]), S1[i], a2);
      }
      if (useful) {
        *reinterpret_cast<f32x4*>(s1_s + (w * 2 + hh) * 64 + dcol) = a1;
        *reinterpret_cast<f32x4*>(s1_s + (8 + w * 2 + hh) * 64 + dcol) = a2;
      }
      asm volatile("s_waitcnt vmcnt(0)" ::: "memory");
      if (lds_ticket(cnt + C_S1, l) == 3) {
        float va = 0.f, vb = 0.f;
#pragma unroll
        for (int t = 0; t < 8; ++t) { va += s1_s[t * 64 + l]; vb += s1_s[(8 + t) * 64 + l]; }
        __builtin_amdgcn_raw_buffer_store_b64(u32x2{taglo | (__float_as_uint(va) >> 8), taghi}, wsr, myrec + R_G1 + l * 8, 0, G1A);
        __builtin_amdgcn_raw_buffer_store_b64(u32x2{taglo | (__float_as_uint(vb) >> 8), taghi}, wsr, lnB != lnA ? myrec + R_G1 + 512 + l * 8 : OOB, 0, G1A);
      }
    }
    STAMP(1);
    lds_wait(cnt + C_PROG, 8);
    if (w != 2 && (w != 1 || is_red)) {
      u32 ga[10];
      bool use3[3];
#pragma unroll
      for (int k = 0; k < 3; ++k) {
        const int fc = (g & 3) * 48 + min(l, 47);
        const int ln = w == 0 ? (g & 3) * 3 + k : 12 + 3 * min(k, 1) + (fc >> 6);
        const int col = w == 0 ? l : (fc & 63);
        const int f0 = (80 * ln) / 45, f1 = (80 * ln + 79) / 45;
        use3[k] = f0 + 2 <= f1;
#pragma unroll
        for (int i = 0; i < 3; ++i) {
          const int pw = min(f0 + i, f1);
          ga[k * 3 + i] = WS_REC + (qd * 32 + pw) * REC + R_G1 + (45 * pw < 80 * ln ? 512 : 0) + col * 8;
        }
      }
      ga[9] = grec + j * REC + R_G1;
      if (w == 1) {
        ga[9] = WS_REC + ((l & 31) * NS + (red_b ? (s & 4) + hh : s)) * REC + R_G1;
#pragma unroll
        for (int i = 0; i < 9; ++i) ga[i] = ga[9] + (red_b ? 2 * REC : 0);
      }
      u32x2 G[10];
      bool ok = false;
      u32 spins = 0;
      for (;;) {
        if (!ok) {
#pragma unroll
          for (int i = 0; i < 10; ++i) if (w == 0 || (w == 3 && i < 6) || (w == 1 && i >= 8)) G[i] = __builtin_amdgcn_raw_buffer_load_b64(wsr, ga[i], 0, G1A);
          u32 d_ = 0;
#pragma unroll
          for (int i = 0; i < 10; ++i) if (w == 0 || (w == 3 && i < 6) || (w == 1 && i >= 8)) d_ |= (G[i][1] ^ taghi) | ((G[i][0] ^ taglo) & 0xff000000u);
          ok = (d_ == 0);
        }
        if (__all(ok) || ++spins > SPIN_CAP) break;
        RETRY(w == 0 ? 1 : w == 3 ? 1000 : 100000);
        __builtin_amdgcn_s_sleep(8);
      }
      float t[3];
#pragma unroll
      for (int k = 0; k < 3; ++k) t[k] = __uint_as_float(G[3 * k][0] << 8) + __uint_as_float(G[3 * k + 1][0] << 8) + (use3[k] ? __uint_as_float(G[3 * k + 2][0] << 8) : 0.f);
      if (w == 0) {
#pragma unroll
        for (int k = 0; k < 3; ++k) ((u16*)q_s)[64 * k + l] = __bfloat16_as_ushort(__float2bfloat16(t[k]));
        if (l == 0) { cnt[14] = ep2[0]; cnt[15] = ep2[1]; }
        lds_arrive(cnt + C_Q, l);
      } else if (w == 3) {
        const float gate = rbf(t[0]), up = rbf(t[1]);
        const float hv = rbf(gate / (1.f + __expf(-gate))) * up;
        h_s[l] = l < 48 ? pkbf(hv) : 0u;
      }
    }
    STAMP(2);
    __builtin_amdgcn_s_setprio(3);
    wg_barrier();
    STAMP(5);
    {
      const int col = min(tid, 191);
      float pv[16];
#pragma unroll
      for (int i = 0; i < 16; ++i) pv[i] = op_s[i * 192 + col];
      const float mj = m_s[j];
      const float M = max_lo3(mj);
      const float fj = __builtin_amdgcn_exp2f(mj - M);
      float o = 0.f;
      if (w == 3) {
        const float L = sum_lo3(l_s[j] * fj);
        o = (l & 1) ? L : M;
      } else {
#pragma unroll
        for (int i = 0; i < 8; ++i) o = fmaf(__uint_as_float(__builtin_amdgcn_readlane(__float_as_uint(fj), i)), pv[2 * i] + pv[2 * i + 1], o);
      }
      __builtin_amdgcn_raw_buffer_store_b32(__float_as_uint(o) | 1u, wsr, tid < 194 ? myrec + R_G2 + tid * 4 : OOB, 0, 16);
    }
    STAMP(6);
    wg_barrier();
    STAMP(7);
    if (w < 2) {
      float y = 0.f;
#pragma unroll
      for (int i = 0; i < 16; ++i) y += yp_s[i * 128 + tid];
      __builtin_amdgcn_raw_buffer_store_b32(__float_as_uint(y) | 1u, wsr, myrec + R_G3 + tid * 4, 0, 16);
    }
    STAMP(8);
    if (is_red) wg_barrier();
  }
}

extern "C" void kernel_launch(void* const* d_in, const int* in_sizes, int n_in, void* d_out, int out_size, void* d_ws,
                              size_t ws_size, hipStream_t stream) {
  fused_decode_kernel<<<256, 768, 0, stream>>>((const u16*)d_in[0], (const char*)d_in[1], (const char*)d_in[2],
                                               (const char*)d_in[3], (const char*)d_in[4], (const char*)d_in[6],
                                               (const char*)d_in[7], (const char*)d_in[8], (u16*)d_out, (char*)d_ws);
}
```

```cpp
#define PD 7
#define D1 24
#define D2 12
#define FSLEEP 4
#define TU 100
#define TB 0
#include <hip/hip_runtime.h>
#include <hip/hip_bf16.h>
#include <stdint.h>

typedef unsigned short u16;
typedef unsigned int u32;
typedef unsigned long long u64;
typedef u32 u32x2 __attribute__((ext_vector_type(2)));
typedef u32 u32x4 __attribute__((ext_vector_type(4)));
typedef float f32x4 __attribute__((ext_vector_type(4)));
typedef __attribute__((ext_vector_type(2))) __bf16 bf16x2;
typedef __attribute__((ext_vector_type(8))) __bf16 bf16x8;
#define LAS __attribute__((address_space(3)))

#define POLL_SLEEP 2
#define GPOS 5
#define PRE 0
#define WG16 16
#define WOPACE 1
#define DIAG 0

constexpr int DM = 640, NH = 32, DH = 192, SK = 32768, WIN = 4096, FF = 1536;
constexpr int NS = 8, KPS = WIN / NS;
constexpr u32 WS_EPOCH = 0;
constexpr u32 WS_REC = 4096, REC = 2560;
constexpr u32 R_G1 = 0;
constexpr u32 R_G2 = 1024;
constexpr u32 R_ST = R_G2 + 768;
constexpr u32 R_G3 = 1920;
constexpr u32 WS_END = WS_REC + NH * NS * REC;
constexpr int WS_STAMP = 1 << 19;
constexpr int NWAVE = 12, NHELP = 4;
constexpr u32 SPIN_CAP = 1u << 22;
constexpr float LOG2E = 1.4426950408889634f;
constexpr u32 OOB = 0xfffffff0u;
constexpr int NT = 2, SC1 = 16;

__device__ __forceinline__ float bf_lo(u32 v) { return __uint_as_float(v << 16); }
__device__ __forceinline__ float bf_hi(u32 v) { return __uint_as_float(v & 0xffff0000u); }
__device__ __forceinline__ float rbf(float f) { return (float)(__bf16)f; }
__device__ __forceinline__ u32 pkbf(float f) { const bf16x2 h = {(__bf16)f, (__bf16)f}; return __builtin_bit_cast(u32, h); }
__device__ __forceinline__ float dot2(u32 a, u32 b, float c) {
  return __builtin_amdgcn_fdot2_f32_bf16(__builtin_bit_cast(bf16x2, a), __builtin_bit_cast(bf16x2, b), c, false);
}
__device__ __forceinline__ f32x4 mfma(u32x4 a, u32x4 b, f32x4 c) {
  return __builtin_amdgcn_mfma_f32_16x16x32_bf16(__builtin_bit_cast(bf16x8, a), __builtin_bit_cast(bf16x8, b), c, 0, 0, 0);
}
__device__ __forceinline__ __amdgpu_buffer_rsrc_t mkrsrc(const void* p, u32 bytes) {
  return __builtin_amdgcn_make_buffer_rsrc(const_cast<void*>(p), 0, bytes, 0x00020000);
}
template <int CTRL> __device__ __forceinline__ float dpp(float x) {
  return __builtin_bit_cast(float, __builtin_amdgcn_mov_dpp(__builtin_bit_cast(int, x), CTRL, 0xf, 0xf, true));
}
constexpr int XOR1 = 0xB1, XOR2 = 0x4E, HMIR = 0x141, ROR8 = 0x128, ROR4 = 0x124;
__device__ __forceinline__ float swap16_add(float a, float b) {
  auto s = __builtin_amdgcn_permlane16_swap(__float_as_uint(a), __float_as_uint(b), false, false);
  return __uint_as_float(s[0]) + __uint_as_float(s[1]);
}
__device__ __forceinline__ float swap32_add(float a, float b) {
  auto s = __builtin_amdgcn_permlane32_swap(__float_as_uint(a), __float_as_uint(b), false, false);
  return __uint_as_float(s[0]) + __uint_as_float(s[1]);
}
__device__ __forceinline__ float swap16_max(float a) {
  auto s = __builtin_amdgcn_permlane16_swap(__float_as_uint(a), __float_as_uint(a), false, false);
  return fmaxf(__uint_as_float(s[0]), __uint_as_float(s[1]));
}
__device__ __forceinline__ float swap32_max(float a) {
  auto s = __builtin_amdgcn_permlane32_swap(__float_as_uint(a), __float_as_uint(a), false, false);
  return fmaxf(__uint_as_float(s[0]), __uint_as_float(s[1]));
}
__device__ __forceinline__ float sum_hi3(float x) { x += dpp<ROR8>(x); x = swap16_add(x, x); return swap32_add(x, x); }
__device__ __forceinline__ float max_hi3(float x) { x = fmaxf(x, dpp<ROR8>(x)); x = swap16_max(x); return swap32_max(x); }
__device__ __forceinline__ float sum_lo3(float x) { x += dpp<XOR1>(x); x += dpp<XOR2>(x); x += dpp<HMIR>(x); return x; }
__device__ __forceinline__ float max_lo3(float x) { x = fmaxf(x, dpp<XOR1>(x)); x = fmaxf(x, dpp<XOR2>(x)); x = fmaxf(x, dpp<HMIR>(x)); return x; }
__device__ __forceinline__ float treduce4(float a0, float a1, float a2, float a3) {
  return swap32_add(swap16_add(a0, a2), swap16_add(a1, a3));
}
__device__ __forceinline__ void lds_arrive(u32* c, int l) {
  asm volatile("s_waitcnt lgkmcnt(0)" ::: "memory");
  if (l == 0) __hip_atomic_fetch_add((LAS u32*)c, 1u, __ATOMIC_RELAXED, __HIP_MEMORY_SCOPE_WORKGROUP);
  asm volatile("" ::: "memory");
}
__device__ __forceinline__ u32 lds_ticket(u32* c, int l) {
  asm volatile("s_waitcnt lgkmcnt(0)" ::: "memory");
  u32 t = 0;
  if (l == 0) t = __hip_atomic_fetch_add((LAS u32*)c, 1u, __ATOMIC_RELAXED, __HIP_MEMORY_SCOPE_WORKGROUP);
  t = __builtin_amdgcn_readfirstlane(t);
  asm volatile("" ::: "memory");
  return t;
}
__device__ __forceinline__ void lds_wait(u32* c, u32 n) {
  asm volatile("" ::: "memory");
  while (__builtin_amdgcn_readfirstlane(__hip_atomic_load((LAS u32*)c, __ATOMIC_RELAXED, __HIP_MEMORY_SCOPE_WORKGROUP)) < n) __builtin_amdgcn_s_sleep(1);
  asm volatile("" ::: "memory");
}
#define SCHED() __builtin_amdgcn_sched_barrier(0)
__device__ __forceinline__ void wg_barrier() {
  asm volatile("s_waitcnt lgkmcnt(0)" ::: "memory");
  __builtin_amdgcn_s_barrier();
  asm volatile("" ::: "memory");
}
#define STAMP(i)
#define RETRY(n)
#define STR2(x) #x
#define STR(x) STR2(x)
#define PACE() asm volatile("s_waitcnt vmcnt(" STR(PD) ")" ::: "memory")

enum { C_S1 = 0, C_PROG, C_Q, C_FIN, C_EPOCH };

__global__ void __launch_bounds__(768, 3)
fused_decode_kernel(const u16* __restrict__ x, const char* __restrict__ kc, const char* __restrict__ vc,
                    const char* __restrict__ wq, const char* __restrict__ wo, const char* __restrict__ w1,
                    const char* __restrict__ w3, const char* __restrict__ w2, u16* __restrict__ out, char* ws) {
  __shared__ __attribute__((aligned(16))) float lds[640 + 96 + 32 + 64 + 24 * 64 + 16 * 192 + 16 * 128];
  u32* xn_s = (u32*)lds;
  u32* q_s = (u32*)(lds + 640);
  float* m_s = lds + 736;
  float* l_s = lds + 744;
  u32* cnt = (u32*)(lds + 752);
  u32* h_s = (u32*)(lds + 768);
  float* s1_s = lds + 832;
  float* op_s = s1_s + 24 * 64;
  float* yp_s = op_s + 16 * 192;
  STAMP(0);
  const int b = blockIdx.x;
  const int g = (b & 7) * 4 + (b >> 6), s = (b >> 3) & 7;
  const int tid = threadIdx.x;
  const int w = __builtin_amdgcn_readfirstlane(tid >> 6), l = tid & 63;
  const int r = l >> 3, j = l & 7;
  const __amdgpu_buffer_rsrc_t wsr = mkrsrc(ws, WS_END);
  const u32 grec = WS_REC + g * NS * REC, myrec = grec + s * REC;
  const bool is_red = (s == (g >> 2)) && !(g & 1);
  const bool red_b = (g & 2) != 0;
  const int hh = l >> 5;
  const bool useful = ((l >> 3) & 1) == hh;
  const int dcol = 8 * j + 4 * ((l >> 4) & 1);
#define MAKE_MSK() u32x4 msk; _Pragma("unroll") for (int d = 0; d < 4; ++d) msk[d] = (j >> 1) == d ? ((j & 1) ? 0xffff0000u : 0x0000ffffu) : 0u

  if (w >= 4) {
    const int sw = w - 4;
    const size_t kvbase = ((size_t)g * SK + (SK - WIN) + (size_t)s * KPS) * (DH * 2);
    const __amdgpu_buffer_rsrc_t rk = mkrsrc(kc + kvbase, KPS * DH * 2), rv = mkrsrc(vc + kvbase, KPS * DH * 2);
    const u32 ll = (u32)(sw * 3072 + r * 384 + j * 16);
    u32x4 K[8][3], V[8][3];
    const u32 tz = TU ? (u32)__builtin_amdgcn_s_memrealtime() + TB + sw * TU / 8 : 0u;
#define GATE(n) do { if (TU && (n) >= 1) { while ((int)((u32)__builtin_amdgcn_s_memrealtime() - (tz + (n) * TU)) < 0) __builtin_amdgcn_s_sleep(1); asm volatile("" ::: "memory"); } } while (0)
#define LOAD1(dst, rs, u, c) do { PACE(); dst[u][c] = __builtin_amdgcn_raw_buffer_load_b128(rs, ll + (c) * 128, (u) * 24576, NT); } while (0)
#define LOAD3(dst, rs, u) do { GATE(8 + (u)); LOAD1(dst, rs, u, 0); LOAD1(dst, rs, u, 1); LOAD1(dst, rs, u, 2); SCHED(); } while (0)
#pragma unroll
    for (int i = 0; i < PRE; ++i) LOAD1(K, rk, i / 3, i % 3);
    SCHED();
    wg_barrier();
#pragma unroll
    for (int i = PRE; i < 24; ++i) { if (i % 3 == 0) GATE(i / 3); LOAD1(K, rk, i / 3, i % 3); if (i == GPOS * 3 + 2) lds_arrive(cnt + C_PROG, l); }
    SCHED();
    STAMP(1);
    LOAD3(V, rv, 0); LOAD3(V, rv, 1); LOAD3(V, rv, 2);
    STAMP(2);
    lds_wait(cnt + C_Q, 1);
    STAMP(3);
    MAKE_MSK();
    u32 ppk[8];
    {
      u32x4 q[3];
      float p[8];
#pragma unroll
      for (int c = 0; c < 3; ++c) q[c] = *reinterpret_cast<const u32x4*>(q_s + c * 32 + j * 4);
#define QK(u) do { float d = 0.f; _Pragma("unroll") for (int c = 0; c < 3; ++c) _Pragma("unroll") for (int k = 0; k < 4; ++k) d = dot2(K[u][c][k], q[c][k], d); \
      p[u] = rbf(rbf(sum_lo3(d)) * 0.07216878364870322f); } while (0)
#define QK2(u) do { QK(u); QK(u + 1); asm volatile("" : "+v"(p[u]), "+v"(p[u + 1])); SCHED(); } while (0)
      QK2(0); LOAD3(V, rv, 3);
      QK2(2); LOAD3(V, rv, 4);
      QK2(4); LOAD3(V, rv, 5);
      QK2(6);
      const float mw = max_hi3(fmaxf(fmaxf(fmaxf(p[0], p[1]), fmaxf(p[2], p[3])), fmaxf(fmaxf(p[4], p[5]), fmaxf(p[6], p[7]))));
      float ls = 0.f;
#pragma unroll
      for (int u = 0; u < 8; ++u) { const float e = __builtin_amdgcn_exp2f((p[u] - mw) * LOG2E); ls += e; ppk[u] = pkbf(e); }
      ls = sum_hi3(ls);
      if (l == 0) { m_s[sw] = mw; l_s[sw] = ls; }
    }
    STAMP(4);
    f32x4 acc[3];
#pragma unroll
    for (int c = 0; c < 3; ++c) acc[c] = f32x4{0.f, 0.f, 0.f, 0.f};
#define PV(u) do { const u32x4 a_ = msk & ppk[u]; _Pragma("unroll") for (int c = 0; c < 3; ++c) acc[c] = mfma(a_, V[u][c], acc[c]); \
      asm volatile("" : "+v"(acc[0]), "+v"(acc[1]), "+v"(acc[2])); SCHED(); } while (0)
    LOAD3(V, rv, 6);
    PV(0); PV(1); PV(2); PV(3);
    LOAD3(V, rv, 7);
    u32x4 WO[5];
    const int ocol = red_b ? 512 + s * 16 + 4 * (sw & 3) : s * 64 + 8 * sw + 4 * (l & 1);
    const int ok2 = 2 * ((l >> 4) & 1) + hh;
    u32 xraw;
    {
      const __amdgpu_buffer_rsrc_t rw = mkrsrc((sw < 6) ? wo + (size_t)(g * DH + sw * 32) * (DM * 2) : w2 + (size_t)(g * 48 + (sw - 6) * 32) * (DM * 2), 32 * DM * 2);
      const u32 wl = ((sw == 7) ? (r & 3) : r) * (4 * DM * 2) + j * 16;
      PV(4);
      if (WG16) GATE(WG16);
      WO[0] = __builtin_amdgcn_raw_buffer_load_b128(rw, wl, s * 128, NT);
      WO[1] = __builtin_amdgcn_raw_buffer_load_b128(rw, wl + DM * 2, s * 128, NT);
      SCHED();
      PV(5); PV(6);
      WO[2] = __builtin_amdgcn_raw_buffer_load_b128(rw, wl + 2 * DM * 2, s * 128, NT);
      WO[3] = __builtin_amdgcn_raw_buffer_load_b128(rw, wl + 3 * DM * 2, s * 128, NT);
      WO[4] = __builtin_amdgcn_raw_buffer_load_b128(rw, wl, (s & 3) * (DM * 2) + (8 + (s >> 2)) * 128, NT);
      xraw = x[ocol + ok2];
      SCHED();
    }
    PV(7);
    STAMP(5);
    if (useful) {
#pragma unroll
      for (int c = 0; c < 3; ++c) *reinterpret_cast<f32x4*>(op_s + (sw * 2 + hh) * 192 + c * 64 + dcol) = acc[c];
    }
    wg_barrier();
    STAMP(6);
    u32 opk[4];
    if (sw < 6) {
      const u32 goff = grec + j * REC + R_G2 + sw * 128 + r * 16;
      const u32 soff = grec + j * REC + R_ST;
      u32x2 SS;
      u32x4 O;
      if (D1) __builtin_amdgcn_s_sleep(D1);
      bool ok = false;
      u32 spins = 0;
      for (;;) {
        if (!ok) {
          SS = __builtin_amdgcn_raw_buffer_load_b64(wsr, soff, 0, SC1);
          O = __builtin_amdgcn_raw_buffer_load_b128(wsr, goff, 0, SC1);
          ok = (O[0] & O[1] & O[2] & O[3] & SS[0] & SS[1] & 1) != 0;
        }
        if (__all(ok) || ++spins > SPIN_CAP) break;
        __builtin_amdgcn_s_sleep(POLL_SLEEP);
      }
      STAMP(7);
      const float ms = __uint_as_float(SS[0] & ~1u);
      const float f = __builtin_amdgcn_exp2f((ms - max_lo3(ms)) * LOG2E);
      const float sc = f * __builtin_amdgcn_rcpf(sum_lo3(f * __uint_as_float(SS[1])));
#pragma unroll
      for (int i = 0; i < 4; ++i) opk[i] = pkbf(sum_lo3(sc * __uint_as_float(O[i])));
    } else {
      const u32x4 hq = *reinterpret_cast<const u32x4*>(h_s + (sw - 6) * 32 + 4 * r);
#pragma unroll
      for (int i = 0; i < 4; ++i) opk[i] = hq[i];
    }
    {
      f32x4 y1 = {0.f, 0.f, 0.f, 0.f}, y2 = y1;
      const u32 o2 = (s & 2) ? ((s & 1) ? opk[3] : opk[2]) : ((s & 1) ? opk[1] : opk[0]);
      y2 = mfma(msk & o2, WO[4], y2);
#pragma unroll
      for (int i = 0; i < 4; ++i) y1 = mfma(msk & opk[i], WO[i], y1);
      if (useful) {
        *reinterpret_cast<f32x4*>(yp_s + (sw * 2 + hh) * 128 + dcol) = y1;
        *reinterpret_cast<f32x4*>(yp_s + (sw * 2 + hh) * 128 + 64 + dcol) = y2;
      }
    }
    wg_barrier();
    STAMP(8);
    if (is_red && !(red_b && sw >= 4)) {
      const u32 goff = red_b ? WS_REC + ((l & 31) * NS + (s & 4) + hh) * REC + R_G3 + 256 + (s & 3) * 64 + sw * 16
                             : WS_REC + ((l >> 1) * NS + s) * REC + R_G3 + sw * 32 + (l & 1) * 16;
      u32x4 Y, Z;
      if (D2) __builtin_amdgcn_s_sleep(D2);
      bool ok = false;
      u32 spins = 0;
      for (;;) {
        if (!ok) {
          Y = __builtin_amdgcn_raw_buffer_load_b128(wsr, goff, 0, SC1);
          Z = __builtin_amdgcn_raw_buffer_load_b128(wsr, red_b ? goff + 2 * REC : OOB, 0, SC1);
          ok = (Y[0] & Y[1] & Y[2] & Y[3] & (red_b ? Z[0] & Z[1] & Z[2] & Z[3] : 1u) & 1) != 0;
        }
        if (__all(ok) || ++spins > SPIN_CAP) break;
        __builtin_amdgcn_s_sleep(FSLEEP);
      }
      const f32x4 yz = __builtin_bit_cast(f32x4, Y) + __builtin_bit_cast(f32x4, Z);
      float t = treduce4(yz[0], yz[1], yz[2], yz[3]);
      t += dpp<ROR8>(t); t += dpp<ROR4>(t); t += dpp<XOR2>(t);
      const float t1 = t + dpp<XOR1>(t);
      if ((l & (red_b ? 15 : 14)) == 0) out[ocol + ok2] = __builtin_bit_cast(u16, (__bf16)(__uint_as_float(xraw << 16) + (red_b ? t1 : t)));
      if (b == 0 && sw == 0) __builtin_amdgcn_raw_buffer_store_b32(cnt[C_EPOCH], wsr, l == 0 ? WS_EPOCH : OOB, 0, SC1);
    }
  } else {
    const __amdgpu_buffer_rsrc_t rx = mkrsrc(x, DM * 2);
    const u32x4 xa = __builtin_amdgcn_raw_buffer_load_b128(rx, l * 16, 0, 0);
    const u32 xb = __builtin_amdgcn_raw_buffer_load_b32(rx, l * 4, 1024, 0);
    const int qd = g >> 2, wi = (g & 3) * 8 + s;
    u32x4 S1[12];
    int row0[3];
#pragma unroll
    for (int k = 0; k < 3; ++k) {
      const int z = 9 * wi + (k < 2 ? w + 4 * k : 8), ln = z >> 4;
      row0[k] = (z & 15) * 40 + (k == 2 ? w * 8 : 0);
      const bool isq = ln < 12;
      const u32 rs = isq ? NH * DH * 2 : FF * 2;
      const char* p = isq ? wq + qd * 1536 + ln * 128 : (ln < 15 ? w1 + (ln - 12) * 128 : w3 + (ln - 15) * 128) + qd * 384;
      const __amdgpu_buffer_rsrc_t rr = mkrsrc(p, (DM - 1) * rs + 128);
      const u32 vo = r * rs + j * 16;
      if (k < 2) {
#pragma unroll
        for (int i = 0; i < 5; ++i) S1[5 * k + i] = __builtin_amdgcn_raw_buffer_load_b128(rr, vo, (row0[k] + 8 * i) * rs, NT);
      } else {
        S1[10] = __builtin_amdgcn_raw_buffer_load_b128(rr, vo, row0[2] * rs, NT);
        S1[11] = __builtin_amdgcn_raw_buffer_load_b128(rr, w == 0 ? vo + (row0[2] + 32) * rs : OOB, 0, NT);
      }
    }
    const u32 epoch = __builtin_amdgcn_raw_buffer_load_b32(wsr, WS_EPOCH, 0, SC1);
    {
      const u32x4 z = {0u, 0u, 0u, 0u};
      __builtin_amdgcn_raw_buffer_store_b128(z, wsr, (w == 3 && l < 49) ? myrec + R_G2 + l * 16 : (w == 2 && l < 32) ? myrec + R_G3 + l * 16 : OOB, 0, SC1);
    }
    if (tid < 8) cnt[tid] = 0;
    SCHED();
    wg_barrier();
    SCHED();
    {
      float xv[10];
#pragma unroll
      for (int k = 0; k < 4; ++k) { xv[2 * k] = bf_lo(xa[k]); xv[2 * k + 1] = bf_hi(xa[k]); }
      xv[8] = bf_lo(xb); xv[9] = bf_hi(xb);
      float sm = 0.f;
#pragma unroll
      for (int k = 0; k < 10; ++k) sm += xv[k];
      sm = sum_hi3(sum_lo3(sm));
      const float mean = sm * (1.f / DM);
      float sv = 0.f;
#pragma unroll
      for (int k = 0; k < 10; ++k) { const float d = xv[k] - mean; sv = fmaf(d, d, sv); }
      sv = sum_hi3(sum_lo3(sv));
      const float var = rbf(sv * (1.f / DM));
      const float mb = rbf(mean);
      const float sd = rbf(sqrtf(rbf(var + 1e-5f)));
      u32 xp[10];
#pragma unroll
      for (int k = 0; k < 10; ++k) xp[k] = pkbf(rbf(xv[k] - mb) / sd);
      *reinterpret_cast<u32x4*>(xn_s + l * 8) = u32x4{xp[0], xp[1], xp[2], xp[3]};
      *reinterpret_cast<u32x4*>(xn_s + l * 8 + 4) = u32x4{xp[4], xp[5], xp[6], xp[7]};
      *reinterpret_cast<u32x2*>(xn_s + 512 + l * 2) = u32x2{xp[8], xp[9]};
      asm volatile("s_waitcnt lgkmcnt(0)" ::: "memory");
    }
    MAKE_MSK();
    const u32 tag = epoch + 1;
    {
      u32 xr[12];
#pragma unroll
      for (int i = 0; i < 10; ++i) xr[i] = xn_s[row0[i / 5] + 8 * (i % 5) + r];
      xr[10] = xn_s[row0[2] + r];
      xr[11] = w == 0 ? xn_s[row0[2] + 32 + r] : 0u;
      f32x4 a[3];
#pragma unroll
      for (int k = 0; k < 3; ++k) a[k] = f32x4{0.f, 0.f, 0.f, 0.f};
#pragma unroll
      for (int i = 0; i < 12; ++i) a[i / 5] = mfma(msk & xr[i], S1[i], a[i / 5]);
      if (useful) {
#pragma unroll
        for (int k = 0; k < 3; ++k) *reinterpret_cast<f32x4*>(s1_s + (k * 8 + w * 2 + hh) * 64 + dcol) = a[k];
      }
      asm volatile("s_waitcnt vmcnt(0)" ::: "memory");
      if (lds_ticket(cnt + C_S1, l) == 3) {
        const int nA = 16 - ((9 * wi) & 15);
        float va = 0.f, vb = 0.f;
#pragma unroll
        for (int c = 0; c < 8; ++c) {
          const float v = s1_s[(2 * c) * 64 + l] + s1_s[(2 * c + 1) * 64 + l];
          if (c < nA) va += v; else vb += v;
        }
        float v8 = 0.f;
#pragma unroll
        for (int t = 16; t < 24; ++t) v8 += s1_s[t * 64 + l];
        if (8 < nA) va += v8; else vb += v8;
        __builtin_amdgcn_raw_buffer_store_b64(u32x2{__float_as_uint(va), tag}, wsr, myrec + R_G1 + l * 8, 0, SC1);
        __builtin_amdgcn_raw_buffer_store_b64(u32x2{__float_as_uint(vb), tag}, wsr, nA <= 8 ? myrec + R_G1 + 512 + l * 8 : OOB, 0, SC1);
      }
    }
    STAMP(1);
    {
      u32 ga[10];
      bool use3[3];
#pragma unroll
      for (int k = 0; k < 3; ++k) {
        const int fc = (g & 3) * 48 + min(l, 47);
        const int ln = w == 0 ? (g & 3) * 3 + k : 12 + 3 * min(k, 1) + (fc >> 6);
        const int col = w == 0 ? l : (fc & 63);
        const int f0 = (16 * ln) / 9, f1 = (16 * ln + 15) / 9;
        use3[k] = f0 + 2 <= f1;
#pragma unroll
        for (int i = 0; i < 3; ++i) {
          const int pw = min(f0 + i, f1);
          ga[k * 3 + i] = WS_REC + (qd * 32 + pw) * REC + R_G1 + (9 * pw < 16 * ln ? 512 : 0) + col * 8;
        }
      }
      ga[9] = grec + j * REC + R_G1;
      if (w == 1) {
        ga[9] = WS_REC + ((l & 31) * NS + (red_b ? (s & 4) + hh : s)) * REC + R_G1;
        ga[8] = ga[9] + (red_b ? 2 * REC : 0);
      }
      lds_wait(cnt + C_PROG, 8);
      if (w != 2 && (w != 1 || is_red)) {
        u32x2 G[10];
        bool ok = false;
        u32 spins = 0;
        for (;;) {
          if (!ok) {
#pragma unroll
            for (int i = 0; i < 10; ++i) if (w == 0 || (w == 3 && i < 6) || (w == 1 && i >= 8)) G[i] = __builtin_amdgcn_raw_buffer_load_b64(wsr, ga[i], 0, SC1);
            u32 d_ = 0;
#pragma unroll
            for (int i = 0; i < 10; ++i) if (w == 0 || (w == 3 && i < 6) || (w == 1 && i >= 8)) d_ |= G[i][1] ^ tag;
            ok = (d_ == 0);
          }
          if (__all(ok) || ++spins > SPIN_CAP) break;
          RETRY(w == 0 ? 1 : w == 3 ? 1000 : 100000);
          __builtin_amdgcn_s_sleep(8);
        }
        float t[3];
#pragma unroll
        for (int k = 0; k < 3; ++k) t[k] = __uint_as_float(G[3 * k][0]) + __uint_as_float(G[3 * k + 1][0]) + (use3[k] ? __uint_as_float(G[3 * k + 2][0]) : 0.f);
        if (w == 0) {
#pragma unroll
          for (int k = 0; k < 3; ++k) ((u16*)q_s)[64 * k + l] = __builtin_bit_cast(u16, (__bf16)t[k]);
          if (l == 0) cnt[C_EPOCH] = tag;
          lds_arrive(cnt + C_Q, l);
        } else if (w == 3) {
          const float gate = rbf(t[0]), up = rbf(t[1]);
          const float hv = rbf(gate / (1.f + __expf(-gate))) * up;
          h_s[l] = l < 48 ? pkbf(hv) : 0u;
        }
      }
    }
    STAMP(2);
    __builtin_amdgcn_s_setprio(3);
    wg_barrier();
    STAMP(5);
    {
      const int col = min(tid, 191);
      float pv[16];
#pragma unroll
      for (int i = 0; i < 16; ++i) pv[i] = op_s[i * 192 + col];
      const float mj = m_s[j];
      const float M = max_lo3(mj);
      const float fj = __builtin_amdgcn_exp2f((mj - M) * LOG2E);
      float o = 0.f;
      if (w == 3) {
        const float L = sum_lo3(l_s[j] * fj);
        o = (l & 1) ? L : M;
      } else {
#pragma unroll
        for (int i = 0; i < 8; ++i) o = fmaf(__uint_as_float(__builtin_amdgcn_readlane(__float_as_uint(fj), i)), pv[2 * i] + pv[2 * i + 1], o);
      }
      __builtin_amdgcn_raw_buffer_store_b32(__float_as_uint(o) | 1u, wsr, tid < 194 ? myrec + R_G2 + tid * 4 : OOB, 0, SC1);
    }
    STAMP(6);
    wg_barrier();
    STAMP(7);
    if (w < 2) {
      float y = 0.f;
#pragma unroll
      for (int i = 0; i < 16; ++i) y += yp_s[i * 128 + tid];
      __builtin_amdgcn_raw_buffer_store_b32(__float_as_uint(y) | 1u, wsr, myrec + R_G3 + tid * 4, 0, SC1);
    }
    STAMP(8);
  }
  STAMP(9);
}

extern "C" void kernel_launch(void* const* d_in, const int* in_sizes, int n_in, void* d_out, int out_size, void* d_ws,
                              size_t ws_size, hipStream_t stream) {
  fused_decode_kernel<<<256, 768, 0, stream>>>((const u16*)d_in[0], (const char*)d_in[1], (const char*)d_in[2],
                                               (const char*)d_in[3], (const char*)d_in[4], (const char*)d_in[6],
                                               (const char*)d_in[7], (const char*)d_in[8], (u16*)d_out, (char*)d_ws);
}
```

```cpp
#define CLRM 1
#include <hip/hip_runtime.h>
#include <hip/hip_bf16.h>
#include <stdint.h>

typedef unsigned short u16;
typedef unsigned int u32;
typedef unsigned long long u64;
typedef u32 u32x4 __attribute__((ext_vector_type(4)));
typedef u32 u32x2 __attribute__((ext_vector_type(2)));
typedef float f32x2 __attribute__((ext_vector_type(2)));
typedef float f32x4 __attribute__((ext_vector_type(4)));
typedef __attribute__((ext_vector_type(2))) __bf16 bf16x2;
typedef __attribute__((ext_vector_type(8))) __bf16 bf16x8;
#define GAS __attribute__((address_space(1)))
#define LAS __attribute__((address_space(3)))
typedef GAS u64 gu64;
typedef GAS u32 gu32;
#define RLX_AGENT __ATOMIC_RELAXED, __HIP_MEMORY_SCOPE_AGENT

#define POLL_SLEEP 2
#define PD 4
#define ABL 0
#define G1S 16
#define G1L 16
#define CLRA 16
#define EPA 18
#define DIAG 0

constexpr int DM = 640, NH = 32, DH = 192, SK = 32768, WIN = 4096, FF = 1536;
constexpr int NS = 8, KPS = WIN / NS;
constexpr u32 WS_EPOCH = 0;
constexpr u32 WS_REC = 4096, REC = 4096;
constexpr u32 R_G1 = 0;
constexpr u32 R_G2 = 2560;
constexpr u32 R_ST = 3328;
constexpr u32 R_G3 = 3456;
constexpr u32 WS_END = WS_REC + NH * NS * REC;
constexpr int WS_STAMP = 1 << 19;
constexpr u32 SPIN_CAP = 1u << 22;
constexpr float LOG2E = 1.4426950408889634f;

__device__ __forceinline__ float bf_lo(u32 v) { return __uint_as_float(v << 16); }
__device__ __forceinline__ float bf_hi(u32 v) { return __uint_as_float(v & 0xffff0000u); }
__device__ __forceinline__ float rbf(float f) { return __bfloat162float(__float2bfloat16(f)); }
__device__ __forceinline__ u32 pkbf(float f) { const u32 h = __bfloat16_as_ushort(__float2bfloat16(f)); return h | (h << 16); }
__device__ __forceinline__ u32x4 ldnt(const char* p) { return __builtin_nontemporal_load(reinterpret_cast<const u32x4*>(p)); }
__device__ __forceinline__ float dot2(u32 a, u32 b, float c) {
  return __builtin_amdgcn_fdot2_f32_bf16(__builtin_bit_cast(bf16x2, a), __builtin_bit_cast(bf16x2, b), c, false);
}
__device__ __forceinline__ f32x4 mfma(u32x4 a, u32x4 b, f32x4 c) {
  return __builtin_amdgcn_mfma_f32_16x16x32_bf16(__builtin_bit_cast(bf16x8, a), __builtin_bit_cast(bf16x8, b), c, 0, 0, 0);
}
template <int CTRL> __device__ __forceinline__ float dpp(float x) {
  return __builtin_bit_cast(float, __builtin_amdgcn_mov_dpp(__builtin_bit_cast(int, x), CTRL, 0xf, 0xf, true));
}
constexpr int XOR1 = 0xB1, XOR2 = 0x4E, HMIR = 0x141, ROR8 = 0x128, ROR4 = 0x124;
__device__ __forceinline__ float swap16_add(float a, float b) {
  auto s = __builtin_amdgcn_permlane16_swap(__float_as_uint(a), __float_as_uint(b), false, false);
  return __uint_as_float(s[0]) + __uint_as_float(s[1]);
}
__device__ __forceinline__ float swap32_add(float a, float b) {
  auto s = __builtin_amdgcn_permlane32_swap(__float_as_uint(a), __float_as_uint(b), false, false);
  return __uint_as_float(s[0]) + __uint_as_float(s[1]);
}
__device__ __forceinline__ float swap16_max(float a) {
  auto s = __builtin_amdgcn_permlane16_swap(__float_as_uint(a), __float_as_uint(a), false, false);
  return fmaxf(__uint_as_float(s[0]), __uint_as_float(s[1]));
}
__device__ __forceinline__ float swap32_max(float a) {
  auto s = __builtin_amdgcn_permlane32_swap(__float_as_uint(a), __float_as_uint(a), false, false);
  return fmaxf(__uint_as_float(s[0]), __uint_as_float(s[1]));
}
__device__ __forceinline__ float sum_hi3(float x) { x += dpp<ROR8>(x); x = swap16_add(x, x); return swap32_add(x, x); }
__device__ __forceinline__ float max_hi3(float x) { x = fmaxf(x, dpp<ROR8>(x)); x = swap16_max(x); return swap32_max(x); }
__device__ __forceinline__ float sum_lo3(float x) { x += dpp<XOR1>(x); x += dpp<XOR2>(x); x += dpp<HMIR>(x); return x; }
__device__ __forceinline__ float max_lo3(float x) { x = fmaxf(x, dpp<XOR1>(x)); x = fmaxf(x, dpp<XOR2>(x)); x = fmaxf(x, dpp<HMIR>(x)); return x; }
__device__ __forceinline__ float treduce8(const float* a, bool b3) {
  float v[4];
#pragma unroll
  for (int k = 0; k < 4; ++k) {
    const float keep = b3 ? a[k + 4] : a[k], send = b3 ? a[k] : a[k + 4];
    v[k] = keep + dpp<ROR8>(send);
  }
  const float u0 = swap16_add(v[0], v[2]), u1 = swap16_add(v[1], v[3]);
  return swap32_add(u0, u1);
}
__device__ __forceinline__ float treduce4(float a0, float a1, float a2, float a3) {
  return swap32_add(swap16_add(a0, a2), swap16_add(a1, a3));
}
__device__ __forceinline__ void lds_arrive(u32* c, int l) {
  asm volatile("s_waitcnt lgkmcnt(0)" ::: "memory");
  if (l == 0) __hip_atomic_fetch_add((LAS u32*)c, 1u, __ATOMIC_RELAXED, __HIP_MEMORY_SCOPE_WORKGROUP);
  asm volatile("" ::: "memory");
}
__device__ __forceinline__ void lds_wait(u32* c, u32 n) {
  asm volatile("" ::: "memory");
  while (__builtin_amdgcn_readfirstlane(__hip_atomic_load((LAS u32*)c, __ATOMIC_RELAXED, __HIP_MEMORY_SCOPE_WORKGROUP)) < n) __builtin_amdgcn_s_sleep(1);
  asm volatile("" ::: "memory");
}
__device__ __forceinline__ u64 mktag(u64 epoch) { return ((epoch + 1) << 24) | (1ull << 63); }
__device__ __forceinline__ u64 mkgran(u64 tagsh, float v) { return tagsh | (u64)(__float_as_uint(v) >> 8); }
__device__ __forceinline__ float granval(u64 x) { return __uint_as_float(((u32)x) << 8); }
__device__ __forceinline__ bool granok(u64 x, u64 tagsh) { return ((x ^ tagsh) >> 24) == 0; }
__device__ __forceinline__ void st_gran(gu64* p, u64 v) { __hip_atomic_store(p, v, RLX_AGENT); }
__device__ __forceinline__ u64 ld_gran(gu64* p) { return __hip_atomic_load(p, RLX_AGENT); }
__device__ __forceinline__ void st_val(gu32* p, float v) { __hip_atomic_store(p, __float_as_uint(v) | 1u, RLX_AGENT); }
#define SCHED() __builtin_amdgcn_sched_barrier(0)
__device__ __forceinline__ void wg_barrier() {
  asm volatile("s_waitcnt lgkmcnt(0)" ::: "memory");
  __builtin_amdgcn_s_barrier();
  asm volatile("" ::: "memory");
}
#define STAMP(i)
#define RETRY(n)

__global__ void __launch_bounds__(512, 2)
fused_decode_kernel(const u16* __restrict__ x, const char* __restrict__ kc, const char* __restrict__ vc,
                    const char* __restrict__ wq, const char* __restrict__ wo, const char* __restrict__ w1,
                    const char* __restrict__ w3, const char* __restrict__ w2, u16* __restrict__ out, char* ws) {
  __shared__ __attribute__((aligned(16))) float lds[96 + 96 + 32 + 16 * 192 + 16 * 128 + 8 * 64 + 64];
  float* xn_s = lds;
  u32* q_s = (u32*)(lds + 96);
  float* m_s = lds + 192;
  float* l_s = lds + 200;
  u32* cnt_s = (u32*)(lds + 208);
  float* op_s = lds + 224;
  float* yp_s = op_s + 16 * 192;
  float* fin_s = yp_s + 16 * 128;
  u32* h_s = (u32*)(fin_s + 8 * 64);
  const u64 tstart = __builtin_amdgcn_s_memrealtime();
  const int b = blockIdx.x;
  const int g = (b & 7) * 4 + (b >> 6), s = (b >> 3) & 7;
  const int tid = threadIdx.x;
  const int w = __builtin_amdgcn_readfirstlane(tid >> 6), l = tid & 63;
  const int r = l >> 3, j = l & 7;
  const __amdgpu_buffer_rsrc_t wsr = __builtin_amdgcn_make_buffer_rsrc(ws, 0, WS_END, 0x00020000);
  const bool is_red = (s == (g >> 2)) && !(g & 1);
  const bool red_b = (g & 2) != 0;

  const u32 myrec = WS_REC + (g * NS + s) * REC;
  const size_t kvbase = ((size_t)g * SK + (SK - WIN) + (size_t)s * KPS) * (DH * 2);
  const u32 kvl = (u32)(w * 3072 + r * 384 + j * 16);
  const char* kp = kc + kvbase + kvl;
  const char* vp = vc + kvbase + kvl;
  u32x4 K[8][3], V[8][3];
#define STR2(x) #x
#define STR(x) STR2(x)
#define PACE() asm volatile("s_waitcnt vmcnt(" STR(PD) ")" ::: "memory")
#define LOADK0(u) do { _Pragma("unroll") for (int c = 0; c < 3; ++c) K[u][c] = ldnt(kp + (u) * 24576 + c * 128); SCHED(); } while (0)
#define LOADK(u) do { _Pragma("unroll") for (int c = 0; c < 3; ++c) { PACE(); K[u][c] = ldnt(kp + (u) * 24576 + c * 128); } SCHED(); } while (0)
#define LOADV(u) do { _Pragma("unroll") for (int c = 0; c < 3; ++c) { PACE(); V[u][c] = ldnt(vp + (u) * 24576 + c * 128); } SCHED(); } while (0)
  u32x4 S1[10];
  if (w == 0) {
    const u32x4 xa = ldnt((const char*)(x + l * 8));
    const u32 xb = __builtin_nontemporal_load(reinterpret_cast<const u32*>(x + 512 + l * 2));
    u32 xc = __builtin_nontemporal_load(reinterpret_cast<const u32*>(x + s * 80 + min(l, 39) * 2));
    SCHED();
    asm volatile("" : "+v"(xc));
    float xv[10];
#pragma unroll
    for (int k = 0; k < 4; ++k) { xv[2 * k] = bf_lo(xa[k]); xv[2 * k + 1] = bf_hi(xa[k]); }
    xv[8] = bf_lo(xb); xv[9] = bf_hi(xb);
    float sm = 0.f;
#pragma unroll
    for (int k = 0; k < 10; ++k) sm += xv[k];
    sm = sum_hi3(sum_lo3(sm));
    const float mean = sm * (1.f / DM);
    float sv = 0.f;
#pragma unroll
    for (int k = 0; k < 10; ++k) { const float d = xv[k] - mean; sv = fmaf(d, d, sv); }
    sv = sum_hi3(sum_lo3(sv));
    const float var = rbf(sv * (1.f / DM));
    const float mb = rbf(mean);
    const float sd = rbf(sqrtf(rbf(var + 1e-5f)));
    const float n0 = rbf(rbf(bf_lo(xc) - mb) / sd), n1 = rbf(rbf(bf_hi(xc) - mb) / sd);
    if (l < 40) *reinterpret_cast<float2*>(xn_s + l * 2) = make_float2(n0, n1);
    if (l < 4) cnt_s[l] = 0;
  } else if (w < 6) {
    const char* sp; u32 rstride;
    if (w < 4) { sp = wq + (size_t)(g * DH + (w - 1) * 64) * 2; rstride = NH * DH * 2; }
    else { sp = (w == 4 ? w1 : w3) + (size_t)(g * 48) * 2; rstride = FF * 2; }
    const int jj = (w < 4) ? j : min(j, 5);
    const char* p = sp + (size_t)(s * 80 + r) * rstride + jj * 16;
#pragma unroll
    for (int i = 0; i < 10; ++i) S1[i] = ldnt(p + (size_t)i * 8 * rstride);
  } else if (w == 7) {
    __builtin_amdgcn_s_setprio(3);
    const u32x4 z = {0u, 0u, 0u, 0u};
#pragma unroll
    for (int i = 0; i < 4; ++i) if (!(ABL & 1) && CLRM == 0) __builtin_amdgcn_raw_buffer_store_b128(z, wsr, myrec + i * 1024 + l * 16, 0, CLRA);
    if (CLRM == 1) {
      __builtin_amdgcn_raw_buffer_store_b128(z, wsr, l < 49 ? myrec + R_G2 + l * 16 : 0xfffffff0u, 0, CLRA);
      __builtin_amdgcn_raw_buffer_store_b128(z, wsr, l < 32 ? myrec + R_G3 + l * 16 : 0xfffffff0u, 0, CLRA);
    }
    asm volatile("s_waitcnt vmcnt(0)" ::: "memory");
    __builtin_amdgcn_s_setprio(0);
  }
  SCHED();
  const u32x2 ep2 = __builtin_amdgcn_raw_buffer_load_b64(wsr, WS_EPOCH, 0, EPA);
  SCHED();
  LOADK0(0);
  LOADK0(1);
  wg_barrier();
  if (w >= 1 && w < 6) {
    __builtin_amdgcn_s_setprio(3);
    float xr[10];
#pragma unroll
    for (int i = 0; i < 10; ++i) xr[i] = xn_s[i * 8 + r];
    SCHED();
    float a[8];
#pragma unroll
    for (int k = 0; k < 8; ++k) a[k] = 0.f;
#pragma unroll
    for (int i = 0; i < 10; ++i) {
#pragma unroll
      for (int k = 0; k < 4; ++k) { a[2 * k] = fmaf(xr[i], bf_lo(S1[i][k]), a[2 * k]); a[2 * k + 1] = fmaf(xr[i], bf_hi(S1[i][k]), a[2 * k + 1]); }
    }
    const float t = treduce8(a, (l & 8) != 0);
    const int col = 8 * j + 4 * ((l >> 3) & 1) + 2 * ((l >> 4) & 1) + (l >> 5);
    u32x2 e1 = ep2;
    asm volatile("" : "+v"(e1));
    const u64 gv = mkgran(mktag((u64)e1[0] | ((u64)e1[1] << 32)), t);
    const int gi = (w < 4) ? (w - 1) * 64 + col : 192 + (col >> 5) * 64 + (w - 4) * 32 + (col & 31);
    __builtin_amdgcn_raw_buffer_store_b64(u32x2{(u32)gv, (u32)(gv >> 32)}, wsr, (ABL & 2) ? 0xfffffff0u : myrec + R_G1 + gi * 8, 0, G1S);
    SCHED();
    __builtin_amdgcn_s_setprio(0);
  }
  SCHED();
  STAMP(1);
  LOADK(2); LOADK(3); LOADK(4); LOADK(5); LOADK(6); LOADK(7);
  const bool hello = w == 5;
  const bool gath = w < 5 || (hello && is_red);
  const u32 gvoff = hello ? WS_REC + (((l >> 2) & 3) * NS + (red_b ? (s & 4) + (l >> 4) : s)) * REC : WS_REC + (g * NS + r) * REC + R_G1 + (w * 64 + j) * 8;
  const u32 gstride = hello ? 32 * REC : 64;
  u32x2 G[8];
#define LOADG() do { _Pragma("unroll") for (int i = 0; i < 8; ++i) G[i] = __builtin_amdgcn_raw_buffer_load_b64(wsr, (ABL & 4) ? 0xffffff00u : gvoff, (ABL & 4) ? 0 : i * gstride, G1L); } while (0)
#define CHECKG(ok_) do { u32 d_ = 0; _Pragma("unroll") for (int i = 0; i < 8; ++i) d_ |= (G[i][1] ^ taghi) | ((G[i][0] ^ taglo) & 0xff000000u); ok_ = (d_ == 0); } while (0)
  if (gath) LOADG();
  SCHED();
  _Pragma("unroll") for (int c = 0; c < 3; ++c) V[0][c] = ldnt(vp + c * 128);
  SCHED();
  LOADV(1);
  STAMP(2);
  const u64 epoch = (u64)ep2[0] | ((u64)ep2[1] << 32);
  if (gath) {
    const u64 tagsh = mktag(epoch);
    const u32 taglo = (u32)tagsh, taghi = (u32)(tagsh >> 32);
    bool ok;
    CHECKG(ok);
    if (ABL & 4) ok = true;
    if (!__all(ok)) {
      u32 spins = 0;
      do {
        __builtin_amdgcn_s_sleep(8);
        if (!ok) { LOADG(); CHECKG(ok); }
        RETRY(hello ? 1000000 : w < 3 ? 1 : 1000);
      } while (!__all(ok) && ++spins < SPIN_CAP);
    }
    float t0 = treduce4(__uint_as_float(G[0][0] << 8), __uint_as_float(G[2][0] << 8), __uint_as_float(G[1][0] << 8), __uint_as_float(G[3][0] << 8));
    float t1 = treduce4(__uint_as_float(G[4][0] << 8), __uint_as_float(G[6][0] << 8), __uint_as_float(G[5][0] << 8), __uint_as_float(G[7][0] << 8));
    t0 = rbf(t0 + dpp<ROR8>(t0));
    t1 = rbf(t1 + dpp<ROR8>(t1));
    if (w < 3) {
      ((u16*)q_s)[64 * w + 32 * (r & 1) + 8 * (r >> 1) + j] = (u16)(__float_as_uint((r & 1) ? t1 : t0) >> 16);
      lds_arrive(cnt_s, l);
    } else if (w < 5) {
      const float hv = rbf(t0 / (1.f + __expf(-t0))) * t1;
      const int row = (w - 3) * 32 + 8 * (r >> 1) + j;
      h_s[row] = row < 48 ? pkbf(hv) : 0u;
    }
  }
  SCHED();
  LOADV(2);
  lds_wait(cnt_s, 3);
  STAMP(3);
  u32 ppk[8];
  {
    u32x4 q[3];
    float p[8];
#pragma unroll
    for (int c = 0; c < 3; ++c) q[c] = *reinterpret_cast<const u32x4*>(q_s + c * 32 + j * 4);
#define QK(u) do { float d = 0.f; _Pragma("unroll") for (int c = 0; c < 3; ++c) _Pragma("unroll") for (int k = 0; k < 4; ++k) d = dot2(K[u][c][k], q[c][k], d); \
      d = sum_lo3(d); p[u] = rbf(rbf(d) * 0.07216878364870322f); } while (0)
#define QK2(u) do { QK(u); QK(u + 1); asm volatile("" : "+v"(p[u]), "+v"(p[u + 1])); SCHED(); } while (0)
    QK2(0); LOADV(3);
    QK2(2); LOADV(4);
    QK2(4); LOADV(5);
    QK2(6);
    const float mx = fmaxf(fmaxf(fmaxf(p[0], p[1]), fmaxf(p[2], p[3])), fmaxf(fmaxf(p[4], p[5]), fmaxf(p[6], p[7])));
    const float mw = max_hi3(mx);
    float ls = 0.f;
#pragma unroll
    for (int u = 0; u < 8; ++u) { const float e = __builtin_amdgcn_exp2f((p[u] - mw) * LOG2E); ls += e; ppk[u] = pkbf(e); }
    ls = sum_hi3(ls);
    if (l == 0) { m_s[w] = mw; l_s[w] = ls; }
    lds_arrive(cnt_s + 1, l);
  }
  STAMP(4);
  u32x4 msk;
#pragma unroll
  for (int d = 0; d < 4; ++d) msk[d] = (j >> 1) == d ? ((j & 1) ? 0xffff0000u : 0x0000ffffu) : 0u;
  f32x4 acc[3];
#pragma unroll
  for (int c = 0; c < 3; ++c) acc[c] = f32x4{0.f, 0.f, 0.f, 0.f};
#define PV(u) do { const u32x4 a_ = msk & ppk[u]; _Pragma("unroll") for (int c = 0; c < 3; ++c) acc[c] = mfma(a_, V[u][c], acc[c]); \
    asm volatile("" : "+v"(acc[0]), "+v"(acc[1]), "+v"(acc[2])); SCHED(); } while (0)
  LOADV(6);
  PV(0); PV(1); PV(2); PV(3);
  LOADV(7);
  PV(4);
  float fw[8];
  u32x2 stv;
  if (w >= 6) {
    lds_wait(cnt_s + 1, 8);
    const float mj = m_s[j];
    const float M = max_lo3(mj);
    const float fj = __builtin_amdgcn_exp2f((mj - M) * LOG2E);
    const float L = sum_lo3(l_s[j] * fj);
#pragma unroll
    for (int i = 0; i < 8; ++i) fw[i] = __uint_as_float(__builtin_amdgcn_readlane(__float_as_uint(fj), i));
    stv = u32x2{__float_as_uint(M) | 1u, __float_as_uint(L) | 1u};
  }
  __builtin_amdgcn_raw_buffer_store_b64(stv, wsr, (tid == 384 && !(ABL & 8)) ? myrec + R_ST : 0xfffffff0u, 0, 16);
  SCHED();
  u32x4 WO[5];
  u32 xraw;
  u32 opk[4];
  {
    const char* wrow = (w < 6) ? wo + (size_t)(g * DH + w * 32) * (DM * 2) : w2 + (size_t)(g * 48 + (w - 6) * 32) * (DM * 2);
    const int rr = (w == 7) ? (r & 3) : r;
    const char* p = wrow + rr * (4 * DM * 2) + j * 16;
    WO[0] = ldnt(p + s * 128);
    WO[1] = ldnt(p + (DM * 2) + s * 128);
    SCHED();
    PV(5);
    PV(6);
    WO[2] = ldnt(p + 2 * (DM * 2) + s * 128);
    WO[3] = ldnt(p + 3 * (DM * 2) + s * 128);
    WO[4] = ldnt(p + (s & 3) * (DM * 2) + (8 + (s >> 2)) * 128);
    xraw = __builtin_nontemporal_load(x + (red_b ? 512 + s * 16 + (tid & 15) : s * 64 + (tid & 63)));
    SCHED();
  }
  PV(7);
  STAMP(5);
  const int hh = l >> 5;
  const bool useful = ((l >> 3) & 1) == hh;
  const int dcol = 8 * j + 4 * ((l >> 4) & 1);
  if (useful) {
#pragma unroll
    for (int c = 0; c < 3; ++c) *reinterpret_cast<f32x4*>(op_s + (w * 2 + hh) * 192 + c * 64 + dcol) = acc[c];
  }
  __builtin_amdgcn_s_setprio(3);
  wg_barrier();
  if (tid >= 384 && tid < 480) {
    const int c2 = (tid - 384) * 2;
    f32x2 o = {0.f, 0.f};
#pragma unroll
    for (int i = 0; i < 8; ++i)
      o += fw[i] * (*reinterpret_cast<const f32x2*>(op_s + (2 * i) * 192 + c2) + *reinterpret_cast<const f32x2*>(op_s + (2 * i + 1) * 192 + c2));
    st_gran((gu64*)(ws + myrec + R_G2 + c2 * 4), (u64)(__float_as_uint(o[0]) | 1u) | ((u64)(__float_as_uint(o[1]) | 1u) << 32));
  }
  STAMP(6);
  if (w < 6) {
    const u32 goff = WS_REC + (g * NS + r) * REC + R_G2 + w * 128 + j * 16;
    u64 SS = ld_gran((gu64*)(ws + WS_REC + (g * NS + r) * REC + R_ST));
    u32x4 O = __builtin_amdgcn_raw_buffer_load_b128(wsr, goff, 0, 16);
    SCHED();
    {
      bool ok = (SS & (SS >> 32) & 1) != 0;
      u32 spins = 0;
      while (!__all(ok) && ++spins < SPIN_CAP) {
        __builtin_amdgcn_s_sleep(POLL_SLEEP);
        if (!ok) SS = ld_gran((gu64*)(ws + WS_REC + (g * NS + r) * REC + R_ST));
        ok = (SS & (SS >> 32) & 1) != 0;
      }
    }
    const float ms = __uint_as_float((u32)SS & ~1u), lsum = __uint_as_float((u32)(SS >> 32));
    const float f = __builtin_amdgcn_exp2f((ms - max_hi3(ms)) * LOG2E);
    float sc = f / sum_hi3(f * lsum);
    asm volatile("" : "+v"(sc));
    SCHED();
    bool ok = (O[0] & O[1] & O[2] & O[3] & 1) != 0;
    u32 spins = 0;
    while (!__all(ok) && ++spins < SPIN_CAP) {
      __builtin_amdgcn_s_sleep(POLL_SLEEP);
      if (!ok) {
        O = __builtin_amdgcn_raw_buffer_load_b128(wsr, goff, 0, 16);
        ok = (O[0] & O[1] & O[2] & O[3] & 1) != 0;
      }
    }
    STAMP(7);
    float t = treduce4(sc * __uint_as_float(O[0]), sc * __uint_as_float(O[1]), sc * __uint_as_float(O[2]), sc * __uint_as_float(O[3]));
    t += dpp<ROR8>(t);
    const u32 tp = pkbf(t);
#pragma unroll
    for (int i = 0; i < 4; ++i) opk[i] = (u32)__builtin_amdgcn_ds_bpermute((r + 16 * (i >> 1) + 32 * (i & 1)) * 4, (int)tp);
  } else {
    STAMP(7);
    const u32x4 hq = *reinterpret_cast<const u32x4*>(h_s + (w - 6) * 32 + 4 * r);
#pragma unroll
    for (int i = 0; i < 4; ++i) opk[i] = hq[i];
  }
  {
    f32x4 y1 = {0.f, 0.f, 0.f, 0.f}, y2 = y1;
    const int s3 = s & 3;
    const u32 o2 = s3 == 0 ? opk[0] : s3 == 1 ? opk[1] : s3 == 2 ? opk[2] : opk[3];
    y2 = mfma(msk & o2, WO[4], y2);
#pragma unroll
    for (int i = 0; i < 4; ++i) y1 = mfma(msk & opk[i], WO[i], y1);
    if (useful) {
      *reinterpret_cast<f32x4*>(yp_s + (w * 2 + hh) * 128 + dcol) = y1;
      *reinterpret_cast<f32x4*>(yp_s + (w * 2 + hh) * 128 + 64 + dcol) = y2;
    }
  }
  wg_barrier();
  if (w < 2) {
    float y = 0.f;
#pragma unroll
    for (int i = 0; i < 16; ++i) y += yp_s[i * 128 + tid];
    st_val((gu32*)(ws + myrec + R_G3) + tid, y);
  }
  STAMP(8);
  if (is_red) {
    const u32 goff = red_b ? WS_REC + ((4 * w + ((l >> 2) & 3)) * NS + (s & 4) + (l >> 4)) * REC + R_G3 + 256 + (s & 3) * 64 + (l & 3) * 16
                           : WS_REC + ((4 * w + (l >> 4)) * NS + s) * REC + R_G3 + (l & 15) * 16;
    u32x4 Y;
    bool ok = false;
    u32 spins = 0;
    for (;;) {
      if (!ok) {
        Y = __builtin_amdgcn_raw_buffer_load_b128(wsr, goff, 0, 16);
        ok = (Y[0] & Y[1] & Y[2] & Y[3] & 1) != 0;
      }
      if (__all(ok) || ++spins > SPIN_CAP) break;
      __builtin_amdgcn_s_sleep(1);
    }
    float t = treduce4(__uint_as_float(Y[0]), __uint_as_float(Y[1]), __uint_as_float(Y[2]), __uint_as_float(Y[3]));
    const int k2 = 2 * ((l >> 4) & 1) + (l >> 5);
    if (red_b) {
      t += dpp<ROR4>(t); t += dpp<ROR8>(t);
      if ((l & 12) == 0) fin_s[w * 64 + 4 * (l & 3) + k2] = t;
    } else {
      fin_s[w * 64 + 4 * (l & 15) + k2] = t;
    }
    wg_barrier();
    if (tid < (red_b ? 16 : 64)) {
      float y = 0.f;
#pragma unroll
      for (int i = 0; i < 8; ++i) y += fin_s[i * 64 + tid];
      out[red_b ? 512 + s * 16 + tid : s * 64 + tid] = __bfloat16_as_ushort(__float2bfloat16(__uint_as_float(xraw << 16) + y));
    }
    if (b == 0 && tid == 0) st_gran((gu64*)(ws + WS_EPOCH), epoch + 1);
    if (b == 0 && w == 0) { const u64 tend = tstart + 4000; while (__builtin_amdgcn_s_memrealtime() < tend) __builtin_amdgcn_s_sleep(1); }
  }
  STAMP(9);
}

extern "C" void kernel_launch(void* const* d_in, const int* in_sizes, int n_in, void* d_out, int out_size, void* d_ws,
                              size_t ws_size, hipStream_t stream) {
  fused_decode_kernel<<<256, 512, 0, stream>>>((const u16*)d_in[0], (const char*)d_in[1], (const char*)d_in[2],
                                               (const char*)d_in[3], (const char*)d_in[4], (const char*)d_in[6],
                                               (const char*)d_in[7], (const char*)d_in[8], (u16*)d_out, (char*)d_ws);
}
```

```cpp
#define ROT 3
#define TU 100
#define TB -60
#include <hip/hip_runtime.h>
#include <hip/hip_bf16.h>
#include <stdint.h>

typedef unsigned short u16;
typedef unsigned int u32;
typedef unsigned long long u64;
typedef u32 u32x2 __attribute__((ext_vector_type(2)));
typedef u32 u32x4 __attribute__((ext_vector_type(4)));
typedef float f32x4 __attribute__((ext_vector_type(4)));
typedef __attribute__((ext_vector_type(2))) __bf16 bf16x2;
typedef __attribute__((ext_vector_type(8))) __bf16 bf16x8;
#define LAS __attribute__((address_space(3)))

#define POLL_SLEEP 2
#define PD 7
#define GPOS 5
#define PRE 0
#define D1 24
#define D2 12
#define WG16 16
#define FSLEEP 4
#define BLK 0
#define PH 0
#define WOPACE 1
#define DIAG 0

constexpr int DM = 640, NH = 32, DH = 192, SK = 32768, WIN = 4096, FF = 1536;
constexpr int NS = 8, KPS = WIN / NS;
constexpr u32 WS_EPOCH = 0;
constexpr u32 WS_REC = 4096, REC = 2560;
constexpr u32 R_G1 = 0;
constexpr u32 R_G2 = 1024;
constexpr u32 R_ST = R_G2 + 768;
constexpr u32 R_G3 = 1920;
constexpr u32 WS_END = WS_REC + NH * NS * REC;
constexpr int WS_STAMP = 1 << 19;
constexpr int NWAVE = 12, NHELP = 4;
constexpr u32 SPIN_CAP = 1u << 22;
constexpr float LOG2E = 1.4426950408889634f;
constexpr u32 OOB = 0xfffffff0u;
constexpr int NT = 2, SC1 = 16;

__device__ __forceinline__ float bf_lo(u32 v) { return __uint_as_float(v << 16); }
__device__ __forceinline__ float bf_hi(u32 v) { return __uint_as_float(v & 0xffff0000u); }
__device__ __forceinline__ float rbf(float f) { return (float)(__bf16)f; }
__device__ __forceinline__ u32 pkbf(float f) { const bf16x2 h = {(__bf16)f, (__bf16)f}; return __builtin_bit_cast(u32, h); }
__device__ __forceinline__ float dot2(u32 a, u32 b, float c) {
  return __builtin_amdgcn_fdot2_f32_bf16(__builtin_bit_cast(bf16x2, a), __builtin_bit_cast(bf16x2, b), c, false);
}
__device__ __forceinline__ f32x4 mfma(u32x4 a, u32x4 b, f32x4 c) {
  return __builtin_amdgcn_mfma_f32_16x16x32_bf16(__builtin_bit_cast(bf16x8, a), __builtin_bit_cast(bf16x8, b), c, 0, 0, 0);
}
__device__ __forceinline__ __amdgpu_buffer_rsrc_t mkrsrc(const void* p, u32 bytes) {
  return __builtin_amdgcn_make_buffer_rsrc(const_cast<void*>(p), 0, bytes, 0x00020000);
}
template <int CTRL> __device__ __forceinline__ float dpp(float x) {
  return __builtin_bit_cast(float, __builtin_amdgcn_mov_dpp(__builtin_bit_cast(int, x), CTRL, 0xf, 0xf, true));
}
constexpr int XOR1 = 0xB1, XOR2 = 0x4E, HMIR = 0x141, ROR8 = 0x128, ROR4 = 0x124;
__device__ __forceinline__ float swap16_add(float a, float b) {
  auto s = __builtin_amdgcn_permlane16_swap(__float_as_uint(a), __float_as_uint(b), false, false);
  return __uint_as_float(s[0]) + __uint_as_float(s[1]);
}
__device__ __forceinline__ float swap32_add(float a, float b) {
  auto s = __builtin_amdgcn_permlane32_swap(__float_as_uint(a), __float_as_uint(b), false, false);
  return __uint_as_float(s[0]) + __uint_as_float(s[1]);
}
__device__ __forceinline__ float swap16_max(float a) {
  auto s = __builtin_amdgcn_permlane16_swap(__float_as_uint(a), __float_as_uint(a), false, false);
  return fmaxf(__uint_as_float(s[0]), __uint_as_float(s[1]));
}
__device__ __forceinline__ float swap32_max(float a) {
  auto s = __builtin_amdgcn_permlane32_swap(__float_as_uint(a), __float_as_uint(a), false, false);
  return fmaxf(__uint_as_float(s[0]), __uint_as_float(s[1]));
}
__device__ __forceinline__ float sum_hi3(float x) { x += dpp<ROR8>(x); x = swap16_add(x, x); return swap32_add(x, x); }
__device__ __forceinline__ float max_hi3(float x) { x = fmaxf(x, dpp<ROR8>(x)); x = swap16_max(x); return swap32_max(x); }
__device__ __forceinline__ float sum_lo3(float x) { x += dpp<XOR1>(x); x += dpp<XOR2>(x); x += dpp<HMIR>(x); return x; }
__device__ __forceinline__ float max_lo3(float x) { x = fmaxf(x, dpp<XOR1>(x)); x = fmaxf(x, dpp<XOR2>(x)); x = fmaxf(x, dpp<HMIR>(x)); return x; }
__device__ __forceinline__ float treduce4(float a0, float a1, float a2, float a3) {
  return swap32_add(swap16_add(a0, a2), swap16_add(a1, a3));
}
__device__ __forceinline__ void lds_arrive(u32* c, int l) {
  asm volatile("s_waitcnt lgkmcnt(0)" ::: "memory");
  if (l == 0) __hip_atomic_fetch_add((LAS u32*)c, 1u, __ATOMIC_RELAXED, __HIP_MEMORY_SCOPE_WORKGROUP);
  asm volatile("" ::: "memory");
}
__device__ __forceinline__ u32 lds_ticket(u32* c, int l) {
  asm volatile("s_waitcnt lgkmcnt(0)" ::: "memory");
  u32 t = 0;
  if (l == 0) t = __hip_atomic_fetch_add((LAS u32*)c, 1u, __ATOMIC_RELAXED, __HIP_MEMORY_SCOPE_WORKGROUP);
  t = __builtin_amdgcn_readfirstlane(t);
  asm volatile("" ::: "memory");
  return t;
}
__device__ __forceinline__ void lds_wait(u32* c, u32 n) {
  asm volatile("" ::: "memory");
  while (__builtin_amdgcn_readfirstlane(__hip_atomic_load((LAS u32*)c, __ATOMIC_RELAXED, __HIP_MEMORY_SCOPE_WORKGROUP)) < n) __builtin_amdgcn_s_sleep(1);
  asm volatile("" ::: "memory");
}
#define SCHED() __builtin_amdgcn_sched_barrier(0)
__device__ __forceinline__ void wg_barrier() {
  asm volatile("s_waitcnt lgkmcnt(0)" ::: "memory");
  __builtin_amdgcn_s_barrier();
  asm volatile("" ::: "memory");
}
#define STAMP(i)
#define RETRY(n)
#define STR2(x) #x
#define STR(x) STR2(x)
#define PACE() asm volatile("s_waitcnt vmcnt(" STR(PD) ")" ::: "memory")

enum { C_S1 = 0, C_PROG, C_Q, C_FIN, C_EPOCH };

__global__ void __launch_bounds__(768, 3)
fused_decode_kernel(const u16* __restrict__ x, const char* __restrict__ kc, const char* __restrict__ vc,
                    const char* __restrict__ wq, const char* __restrict__ wo, const char* __restrict__ w1,
                    const char* __restrict__ w3, const char* __restrict__ w2, u16* __restrict__ out, char* ws) {
  __shared__ __attribute__((aligned(16))) float lds[640 + 96 + 32 + 64 + 24 * 64 + 16 * 192 + 16 * 128];
  u32* xn_s = (u32*)lds;
  u32* q_s = (u32*)(lds + 640);
  float* m_s = lds + 736;
  float* l_s = lds + 744;
  u32* cnt = (u32*)(lds + 752);
  u32* h_s = (u32*)(lds + 768);
  float* s1_s = lds + 832;
  float* op_s = s1_s + 24 * 64;
  float* yp_s = op_s + 16 * 192;
  STAMP(0);
  const int b = blockIdx.x;
  const int g = (b & 7) * 4 + (b >> 6), s = (b >> 3) & 7;
  const int tid = threadIdx.x;
  const int w = __builtin_amdgcn_readfirstlane(tid >> 6), l = tid & 63;
  const int r = l >> 3, j = l & 7;
  const __amdgpu_buffer_rsrc_t wsr = mkrsrc(ws, WS_END);
  const u32 grec = WS_REC + g * NS * REC, myrec = grec + s * REC;
  const bool is_red = (s == (g >> 2)) && !(g & 1);
  const bool red_b = (g & 2) != 0;
  const int hh = l >> 5;
  const bool useful = ((l >> 3) & 1) == hh;
  const int dcol = 8 * j + 4 * ((l >> 4) & 1);
#define MAKE_MSK() u32x4 msk; _Pragma("unroll") for (int d = 0; d < 4; ++d) msk[d] = (j >> 1) == d ? ((j & 1) ? 0xffff0000u : 0x0000ffffu) : 0u

  if (w >= 4) {
    const int sw = w - 4;
    const size_t kvbase = ((size_t)g * SK + (SK - WIN) + (size_t)s * KPS) * (DH * 2);
    const __amdgpu_buffer_rsrc_t rk = mkrsrc(kc + kvbase, KPS * DH * 2), rv = mkrsrc(vc + kvbase, KPS * DH * 2);
    const u32 ll = (u32)(sw * (BLK ? 24576 : 3072) + r * 384 + j * 16);
    const int rot = ROT == 0 ? 0 : ROT == 1 ? s : ROT == 2 ? (g & 7) : ROT == 3 ? ((g + s) & 7) : ((g + s + sw) & 7);
#define UOFF(u) ((((u) + rot) & 7) * (BLK ? 3072 : 24576))
    u32x4 K[8][3], V[8][3];
    const u32 tz = TU ? (u32)__builtin_amdgcn_s_memrealtime() + TB + sw * TU / 8 + (PH ? ((b >> 3) & 3) * TU / 32 : 0) : 0u;
#define GATE(n) do { if (TU && (n) >= 1) { while ((int)((u32)__builtin_amdgcn_s_memrealtime() - (tz + (n) * TU)) < 0) __builtin_amdgcn_s_sleep(1); asm volatile("" ::: "memory"); } } while (0)
#define LOAD1(dst, rs, u, c) do { PACE(); dst[u][c] = __builtin_amdgcn_raw_buffer_load_b128(rs, ll + (c) * 128, UOFF(u), NT); } while (0)
#define LOAD3(dst, rs, u) do { GATE(8 + (u)); LOAD1(dst, rs, u, 0); LOAD1(dst, rs, u, 1); LOAD1(dst, rs, u, 2); SCHED(); } while (0)
#pragma unroll
    for (int i = 0; i < PRE; ++i) LOAD1(K, rk, i / 3, i % 3);
    SCHED();
    wg_barrier();
#pragma unroll
    for (int i = PRE; i < 24; ++i) { if (i % 3 == 0) GATE(i / 3); LOAD1(K, rk, i / 3, i % 3); if (i == GPOS * 3 + 2) lds_arrive(cnt + C_PROG, l); }
    SCHED();
    STAMP(1);
    LOAD3(V, rv, 0); LOAD3(V, rv, 1); LOAD3(V, rv, 2);
    STAMP(2);
    lds_wait(cnt + C_Q, 1);
    STAMP(3);
    MAKE_MSK();
    u32 ppk[8];
    {
      u32x4 q[3];
      float p[8];
#pragma unroll
      for (int c = 0; c < 3; ++c) q[c] = *reinterpret_cast<const u32x4*>(q_s + c * 32 + j * 4);
#define QK(u) do { float d = 0.f; _Pragma("unroll") for (int c = 0; c < 3; ++c) _Pragma("unroll") for (int k = 0; k < 4; ++k) d = dot2(K[u][c][k], q[c][k], d); \
      p[u] = rbf(rbf(sum_lo3(d)) * 0.07216878364870322f); } while (0)
#define QK2(u) do { QK(u); QK(u + 1); asm volatile("" : "+v"(p[u]), "+v"(p[u + 1])); SCHED(); } while (0)
      QK2(0); LOAD3(V, rv, 3);
      QK2(2); LOAD3(V, rv, 4);
      QK2(4); LOAD3(V, rv, 5);
      QK2(6);
      const float mw = max_hi3(fmaxf(fmaxf(fmaxf(p[0], p[1]), fmaxf(p[2], p[3])), fmaxf(fmaxf(p[4], p[5]), fmaxf(p[6], p[7]))));
      float ls = 0.f;
#pragma unroll
      for (int u = 0; u < 8; ++u) { const float e = __builtin_amdgcn_exp2f((p[u] - mw) * LOG2E); ls += e; ppk[u] = pkbf(e); }
      ls = sum_hi3(ls);
      if (l == 0) { m_s[sw] = mw; l_s[sw] = ls; }
    }
    STAMP(4);
    f32x4 acc[3];
#pragma unroll
    for (int c = 0; c < 3; ++c) acc[c] = f32x4{0.f, 0.f, 0.f, 0.f};
#define PV(u) do { const u32x4 a_ = msk & ppk[u]; _Pragma("unroll") for (int c = 0; c < 3; ++c) acc[c] = mfma(a_, V[u][c], acc[c]); \
      asm volatile("" : "+v"(acc[0]), "+v"(acc[1]), "+v"(acc[2])); SCHED(); } while (0)
    LOAD3(V, rv, 6);
    PV(0); PV(1); PV(2); PV(3);
    LOAD3(V, rv, 7);
    u32x4 WO[5];
    const int ocol = red_b ? 512 + s * 16 + 4 * (sw & 3) : s * 64 + 8 * sw + 4 * (l & 1);
    const int ok2 = 2 * ((l >> 4) & 1) + hh;
    u32 xraw;
    {
      const __amdgpu_buffer_rsrc_t rw = mkrsrc((sw < 6) ? wo + (size_t)(g * DH + sw * 32) * (DM * 2) : w2 + (size_t)(g * 48 + (sw - 6) * 32) * (DM * 2), 32 * DM * 2);
      const u32 wl = ((sw == 7) ? (r & 3) : r) * (4 * DM * 2) + j * 16;
      PV(4);
      if (WG16) GATE(WG16);
      WO[0] = __builtin_amdgcn_raw_buffer_load_b128(rw, wl, s * 128, NT);
      WO[1] = __builtin_amdgcn_raw_buffer_load_b128(rw, wl + DM * 2, s * 128, NT);
      SCHED();
      PV(5); PV(6);
      WO[2] = __builtin_amdgcn_raw_buffer_load_b128(rw, wl + 2 * DM * 2, s * 128, NT);
      WO[3] = __builtin_amdgcn_raw_buffer_load_b128(rw, wl + 3 * DM * 2, s * 128, NT);
      WO[4] = __builtin_amdgcn_raw_buffer_load_b128(rw, wl, (s & 3) * (DM * 2) + (8 + (s >> 2)) * 128, NT);
      xraw = x[ocol + ok2];
      SCHED();
    }
    PV(7);
    STAMP(5);
    if (useful) {
#pragma unroll
      for (int c = 0; c < 3; ++c) *reinterpret_cast<f32x4*>(op_s + (sw * 2 + hh) * 192 + c * 64 + dcol) = acc[c];
    }
    wg_barrier();
    STAMP(6);
    u32 opk[4];
    if (sw < 6) {
      const u32 goff = grec + j * REC + R_G2 + sw * 128 + r * 16;
      const u32 soff = grec + j * REC + R_ST;
      u32x2 SS;
      u32x4 O;
      if (D1) __builtin_amdgcn_s_sleep(D1);
      bool ok = false;
      u32 spins = 0;
      for (;;) {
        if (!ok) {
          SS = __builtin_amdgcn_raw_buffer_load_b64(wsr, soff, 0, SC1);
          O = __builtin_amdgcn_raw_buffer_load_b128(wsr, goff, 0, SC1);
          ok = (O[0] & O[1] & O[2] & O[3] & SS[0] & SS[1] & 1) != 0;
        }
        if (__all(ok) || ++spins > SPIN_CAP) break;
        __builtin_amdgcn_s_sleep(POLL_SLEEP);
      }
      STAMP(7);
      const float ms = __uint_as_float(SS[0] & ~1u);
      const float f = __builtin_amdgcn_exp2f((ms - max_lo3(ms)) * LOG2E);
      const float sc = f * __builtin_amdgcn_rcpf(sum_lo3(f * __uint_as_float(SS[1])));
#pragma unroll
      for (int i = 0; i < 4; ++i) opk[i] = pkbf(sum_lo3(sc * __uint_as_float(O[i])));
    } else {
      const u32x4 hq = *reinterpret_cast<const u32x4*>(h_s + (sw - 6) * 32 + 4 * r);
#pragma unroll
      for (int i = 0; i < 4; ++i) opk[i] = hq[i];
    }
    {
      f32x4 y1 = {0.f, 0.f, 0.f, 0.f}, y2 = y1;
      const u32 o2 = (s & 2) ? ((s & 1) ? opk[3] : opk[2]) : ((s & 1) ? opk[1] : opk[0]);
      y2 = mfma(msk & o2, WO[4], y2);
#pragma unroll
      for (int i = 0; i < 4; ++i) y1 = mfma(msk & opk[i], WO[i], y1);
      if (useful) {
        *reinterpret_cast<f32x4*>(yp_s + (sw * 2 + hh) * 128 + dcol) = y1;
        *reinterpret_cast<f32x4*>(yp_s + (sw * 2 + hh) * 128 + 64 + dcol) = y2;
      }
    }
    wg_barrier();
    STAMP(8);
    if (is_red && !(red_b && sw >= 4)) {
      const u32 goff = red_b ? WS_REC + ((l & 31) * NS + (s & 4) + hh) * REC + R_G3 + 256 + (s & 3) * 64 + sw * 16
                             : WS_REC + ((l >> 1) * NS + s) * REC + R_G3 + sw * 32 + (l & 1) * 16;
      u32x4 Y, Z;
      if (D2) __builtin_amdgcn_s_sleep(D2);
      bool ok = false;
      u32 spins = 0;
      for (;;) {
        if (!ok) {
          Y = __builtin_amdgcn_raw_buffer_load_b128(wsr, goff, 0, SC1);
          Z = __builtin_amdgcn_raw_buffer_load_b128(wsr, red_b ? goff + 2 * REC : OOB, 0, SC1);
          ok = (Y[0] & Y[1] & Y[2] & Y[3] & (red_b ? Z[0] & Z[1] & Z[2] & Z[3] : 1u) & 1) != 0;
        }
        if (__all(ok) || ++spins > SPIN_CAP) break;
        __builtin_amdgcn_s_sleep(FSLEEP);
      }
      const f32x4 yz = __builtin_bit_cast(f32x4, Y) + __builtin_bit_cast(f32x4, Z);
      float t = treduce4(yz[0], yz[1], yz[2], yz[3]);
      t += dpp<ROR8>(t); t += dpp<ROR4>(t); t += dpp<XOR2>(t);
      const float t1 = t + dpp<XOR1>(t);
      if ((l & (red_b ? 15 : 14)) == 0) out[ocol + ok2] = __builtin_bit_cast(u16, (__bf16)(__uint_as_float(xraw << 16) + (red_b ? t1 : t)));
      if (b == 0 && sw == 0) __builtin_amdgcn_raw_buffer_store_b32(cnt[C_EPOCH], wsr, l == 0 ? WS_EPOCH : OOB, 0, SC1);
    }
  } else {
    const __amdgpu_buffer_rsrc_t rx = mkrsrc(x, DM * 2);
    const u32x4 xa = __builtin_amdgcn_raw_buffer_load_b128(rx, l * 16, 0, 0);
    const u32 xb = __builtin_amdgcn_raw_buffer_load_b32(rx, l * 4, 1024, 0);
    const int qd = g >> 2, wi = (g & 3) * 8 + s;
    u32x4 S1[12];
    int row0[3];
#pragma unroll
    for (int k = 0; k < 3; ++k) {
      const int z = 9 * wi + (k < 2 ? w + 4 * k : 8), ln = z >> 4;
      row0[k] = (z & 15) * 40 + (k == 2 ? w * 8 : 0);
      const bool isq = ln < 12;
      const u32 rs = isq ? NH * DH * 2 : FF * 2;
      const char* p = isq ? wq + qd * 1536 + ln * 128 : (ln < 15 ? w1 + (ln - 12) * 128 : w3 + (ln - 15) * 128) + qd * 384;
      const __amdgpu_buffer_rsrc_t rr = mkrsrc(p, (DM - 1) * rs + 128);
      const u32 vo = r * rs + j * 16;
      if (k < 2) {
#pragma unroll
        for (int i = 0; i < 5; ++i) S1[5 * k + i] = __builtin_amdgcn_raw_buffer_load_b128(rr, vo, (row0[k] + 8 * i) * rs, NT);
      } else {
        S1[10] = __builtin_amdgcn_raw_buffer_load_b128(rr, vo, row0[2] * rs, NT);
        S1[11] = __builtin_amdgcn_raw_buffer_load_b128(rr, w == 0 ? vo + (row0[2] + 32) * rs : OOB, 0, NT);
      }
    }
    const u32 epoch = __builtin_amdgcn_raw_buffer_load_b32(wsr, WS_EPOCH, 0, SC1);
    {
      const u32x4 z = {0u, 0u, 0u, 0u};
      __builtin_amdgcn_raw_buffer_store_b128(z, wsr, (w == 3 && l < 49) ? myrec + R_G2 + l * 16 : (w == 2 && l < 32) ? myrec + R_G3 + l * 16 : OOB, 0, SC1);
    }
    if (tid < 8) cnt[tid] = 0;
    SCHED();
    wg_barrier();
    SCHED();
    {
      float xv[10];
#pragma unroll
      for (int k = 0; k < 4; ++k) { xv[2 * k] = bf_lo(xa[k]); xv[2 * k + 1] = bf_hi(xa[k]); }
      xv[8] = bf_lo(xb); xv[9] = bf_hi(xb);
      float sm = 0.f;
#pragma unroll
      for (int k = 0; k < 10; ++k) sm += xv[k];
      sm = sum_hi3(sum_lo3(sm));
      const float mean = sm * (1.f / DM);
      float sv = 0.f;
#pragma unroll
      for (int k = 0; k < 10; ++k) { const float d = xv[k] - mean; sv = fmaf(d, d, sv); }
      sv = sum_hi3(sum_lo3(sv));
      const float var = rbf(sv * (1.f / DM));
      const float mb = rbf(mean);
      const float sd = rbf(sqrtf(rbf(var + 1e-5f)));
      u32 xp[10];
#pragma unroll
      for (int k = 0; k < 10; ++k) xp[k] = pkbf(rbf(xv[k] - mb) / sd);
      *reinterpret_cast<u32x4*>(xn_s + l * 8) = u32x4{xp[0], xp[1], xp[2], xp[3]};
      *reinterpret_cast<u32x4*>(xn_s + l * 8 + 4) = u32x4{xp[4], xp[5], xp[6], xp[7]};
      *reinterpret_cast<u32x2*>(xn_s + 512 + l * 2) = u32x2{xp[8], xp[9]};
      asm volatile("s_waitcnt lgkmcnt(0)" ::: "memory");
    }
    MAKE_MSK();
    const u32 tag = epoch + 1;
    {
      u32 xr[12];
#pragma unroll
      for (int i = 0; i < 10; ++i) xr[i] = xn_s[row0[i / 5] + 8 * (i % 5) + r];
      xr[10] = xn_s[row0[2] + r];
      xr[11] = w == 0 ? xn_s[row0[2] + 32 + r] : 0u;
      f32x4 a[3];
#pragma unroll
      for (int k = 0; k < 3; ++k) a[k] = f32x4{0.f, 0.f, 0.f, 0.f};
#pragma unroll
      for (int i = 0; i < 12; ++i) a[i / 5] = mfma(msk & xr[i], S1[i], a[i / 5]);
      if (useful) {
#pragma unroll
        for (int k = 0; k < 3; ++k) *reinterpret_cast<f32x4*>(s1_s + (k * 8 + w * 2 + hh) * 64 + dcol) = a[k];
      }
      asm volatile("s_waitcnt vmcnt(0)" ::: "memory");
      if (lds_ticket(cnt + C_S1, l) == 3) {
        const int nA = 16 - ((9 * wi) & 15);
        float va = 0.f, vb = 0.f;
#pragma unroll
        for (int c = 0; c < 8; ++c) {
          const float v = s1_s[(2 * c) * 64 + l] + s1_s[(2 * c + 1) * 64 + l];
          if (c < nA) va += v; else vb += v;
        }
        float v8 = 0.f;
#pragma unroll
        for (int t = 16; t < 24; ++t) v8 += s1_s[t * 64 + l];
        if (8 < nA) va += v8; else vb += v8;
        __builtin_amdgcn_raw_buffer_store_b64(u32x2{__float_as_uint(va), tag}, wsr, myrec + R_G1 + l * 8, 0, SC1);
        __builtin_amdgcn_raw_buffer_store_b64(u32x2{__float_as_uint(vb), tag}, wsr, nA <= 8 ? myrec + R_G1 + 512 + l * 8 : OOB, 0, SC1);
      }
    }
    STAMP(1);
    {
      u32 ga[10];
      bool use3[3];
#pragma unroll
      for (int k = 0; k < 3; ++k) {
        const int fc = (g & 3) * 48 + min(l, 47);
        const int ln = w == 0 ? (g & 3) * 3 + k : 12 + 3 * min(k, 1) + (fc >> 6);
        const int col = w == 0 ? l : (fc & 63);
        const int f0 = (16 * ln) / 9, f1 = (16 * ln + 15) / 9;
        use3[k] = f0 + 2 <= f1;
#pragma unroll
        for (int i = 0; i < 3; ++i) {
          const int pw = min(f0 + i, f1);
          ga[k * 3 + i] = WS_REC + (qd * 32 + pw) * REC + R_G1 + (9 * pw < 16 * ln ? 512 : 0) + col * 8;
        }
      }
      ga[9] = grec + j * REC + R_G1;
      if (w == 1) {
        ga[9] = WS_REC + ((l & 31) * NS + (red_b ? (s & 4) + hh : s)) * REC + R_G1;
        ga[8] = ga[9] + (red_b ? 2 * REC : 0);
      }
      lds_wait(cnt + C_PROG, 8);
      if (w != 2 && (w != 1 || is_red)) {
        u32x2 G[10];
        bool ok = false;
        u32 spins = 0;
        for (;;) {
          if (!ok) {
#pragma unroll
            for (int i = 0; i < 10; ++i) if (w == 0 || (w == 3 && i < 6) || (w == 1 && i >= 8)) G[i] = __builtin_amdgcn_raw_buffer_load_b64(wsr, ga[i], 0, SC1);
            u32 d_ = 0;
#pragma unroll
            for (int i = 0; i < 10; ++i) if (w == 0 || (w == 3 && i < 6) || (w == 1 && i >= 8)) d_ |= G[i][1] ^ tag;
            ok = (d_ == 0);
          }
          if (__all(ok) || ++spins > SPIN_CAP) break;
          RETRY(w == 0 ? 1 : w == 3 ? 1000 : 100000);
          __builtin_amdgcn_s_sleep(8);
        }
        float t[3];
#pragma unroll
        for (int k = 0; k < 3; ++k) t[k] = __uint_as_float(G[3 * k][0]) + __uint_as_float(G[3 * k + 1][0]) + (use3[k] ? __uint_as_float(G[3 * k + 2][0]) : 0.f);
        if (w == 0) {
#pragma unroll
          for (int k = 0; k < 3; ++k) ((u16*)q_s)[64 * k + l] = __builtin_bit_cast(u16, (__bf16)t[k]);
          if (l == 0) cnt[C_EPOCH] = tag;
          lds_arrive(cnt + C_Q, l);
        } else if (w == 3) {
          const float gate = rbf(t[0]), up = rbf(t[1]);
          const float hv = rbf(gate / (1.f + __expf(-gate))) * up;
          h_s[l] = l < 48 ? pkbf(hv) : 0u;
        }
      }
    }
    STAMP(2);
    __builtin_amdgcn_s_setprio(3);
    wg_barrier();
    STAMP(5);
    {
      const int col = min(tid, 191);
      float pv[16];
#pragma unroll
      for (int i = 0; i < 16; ++i) pv[i] = op_s[i * 192 + col];
      const float mj = m_s[j];
      const float M = max_lo3(mj);
      const float fj = __builtin_amdgcn_exp2f((mj - M) * LOG2E);
      float o = 0.f;
      if (w == 3) {
        const float L = sum_lo3(l_s[j] * fj);
        o = (l & 1) ? L : M;
      } else {
#pragma unroll
        for (int i = 0; i < 8; ++i) o = fmaf(__uint_as_float(__builtin_amdgcn_readlane(__float_as_uint(fj), i)), pv[2 * i] + pv[2 * i + 1], o);
      }
      __builtin_amdgcn_raw_buffer_store_b32(__float_as_uint(o) | 1u, wsr, tid < 194 ? myrec + R_G2 + tid * 4 : OOB, 0, SC1);
    }
    STAMP(6);
    wg_barrier();
    STAMP(7);
    if (w < 2) {
      float y = 0.f;
#pragma unroll
      for (int i = 0; i < 16; ++i) y += yp_s[i * 128 + tid];
      __builtin_amdgcn_raw_buffer_store_b32(__float_as_uint(y) | 1u, wsr, myrec + R_G3 + tid * 4, 0, SC1);
    }
    STAMP(8);
  }
  STAMP(9);
}

extern "C" void kernel_launch(void* const* d_in, const int* in_sizes, int n_in, void* d_out, int out_size, void* d_ws,
                              size_t ws_size, hipStream_t stream) {
  fused_decode_kernel<<<256, 768, 0, stream>>>((const u16*)d_in[0], (const char*)d_in[1], (const char*)d_in[2],
                                               (const char*)d_in[3], (const char*)d_in[4], (const char*)d_in[6],
                                               (const char*)d_in[7], (const char*)d_in[8], (u16*)d_out, (char*)d_ws);
}
```

```cpp
#define WOPACE 1
#define TB -100
#include <hip/hip_runtime.h>
#include <hip/hip_bf16.h>
#include <stdint.h>

typedef unsigned short u16;
typedef unsigned int u32;
typedef unsigned long long u64;
typedef u32 u32x2 __attribute__((ext_vector_type(2)));
typedef u32 u32x4 __attribute__((ext_vector_type(4)));
typedef float f32x4 __attribute__((ext_vector_type(4)));
typedef __attribute__((ext_vector_type(2))) __bf16 bf16x2;
typedef __attribute__((ext_vector_type(8))) __bf16 bf16x8;
#define LAS __attribute__((address_space(3)))

#define POLL_SLEEP 2
#define PD 7
#define GPOS 5
#define PRE 0
#define TU 100
#define D1 24
#define D2 12
#define WG16 16
#define FSLEEP 4
#define ROT 0
#define WGA TU
#define DIAG 0

constexpr int DM = 640, NH = 32, DH = 192, SK = 32768, WIN = 4096, FF = 1536;
constexpr int NS = 8, KPS = WIN / NS;
constexpr u32 WS_EPOCH = 0;
constexpr u32 WS_REC = 4096, REC = 2560;
constexpr u32 R_G1 = 0;
constexpr u32 R_G2 = 1024;
constexpr u32 R_ST = R_G2 + 768;
constexpr u32 R_G3 = 1920;
constexpr u32 WS_END = WS_REC + NH * NS * REC;
constexpr int WS_STAMP = 1 << 19;
constexpr int NWAVE = 12, NHELP = 4;
constexpr u32 SPIN_CAP = 1u << 22;
constexpr float LOG2E = 1.4426950408889634f;
constexpr u32 OOB = 0xfffffff0u;
constexpr int NT = 2, SC1 = 16;

__device__ __forceinline__ float bf_lo(u32 v) { return __uint_as_float(v << 16); }
__device__ __forceinline__ float bf_hi(u32 v) { return __uint_as_float(v & 0xffff0000u); }
__device__ __forceinline__ float rbf(float f) { return (float)(__bf16)f; }
__device__ __forceinline__ u32 pkbf(float f) { const bf16x2 h = {(__bf16)f, (__bf16)f}; return __builtin_bit_cast(u32, h); }
__device__ __forceinline__ float dot2(u32 a, u32 b, float c) {
  return __builtin_amdgcn_fdot2_f32_bf16(__builtin_bit_cast(bf16x2, a), __builtin_bit_cast(bf16x2, b), c, false);
}
__device__ __forceinline__ f32x4 mfma(u32x4 a, u32x4 b, f32x4 c) {
  return __builtin_amdgcn_mfma_f32_16x16x32_bf16(__builtin_bit_cast(bf16x8, a), __builtin_bit_cast(bf16x8, b), c, 0, 0, 0);
}
__device__ __forceinline__ __amdgpu_buffer_rsrc_t mkrsrc(const void* p, u32 bytes) {
  return __builtin_amdgcn_make_buffer_rsrc(const_cast<void*>(p), 0, bytes, 0x00020000);
}
template <int CTRL> __device__ __forceinline__ float dpp(float x) {
  return __builtin_bit_cast(float, __builtin_amdgcn_mov_dpp(__builtin_bit_cast(int, x), CTRL, 0xf, 0xf, true));
}
constexpr int XOR1 = 0xB1, XOR2 = 0x4E, HMIR = 0x141, ROR8 = 0x128, ROR4 = 0x124;
__device__ __forceinline__ float swap16_add(float a, float b) {
  auto s = __builtin_amdgcn_permlane16_swap(__float_as_uint(a), __float_as_uint(b), false, false);
  return __uint_as_float(s[0]) + __uint_as_float(s[1]);
}
__device__ __forceinline__ float swap32_add(float a, float b) {
  auto s = __builtin_amdgcn_permlane32_swap(__float_as_uint(a), __float_as_uint(b), false, false);
  return __uint_as_float(s[0]) + __uint_as_float(s[1]);
}
__device__ __forceinline__ float swap16_max(float a) {
  auto s = __builtin_amdgcn_permlane16_swap(__float_as_uint(a), __float_as_uint(a), false, false);
  return fmaxf(__uint_as_float(s[0]), __uint_as_float(s[1]));
}
__device__ __forceinline__ float swap32_max(float a) {
  auto s = __builtin_amdgcn_permlane32_swap(__float_as_uint(a), __float_as_uint(a), false, false);
  return fmaxf(__uint_as_float(s[0]), __uint_as_float(s[1]));
}
__device__ __forceinline__ float sum_hi3(float x) { x += dpp<ROR8>(x); x = swap16_add(x, x); return swap32_add(x, x); }
__device__ __forceinline__ float max_hi3(float x) { x = fmaxf(x, dpp<ROR8>(x)); x = swap16_max(x); return swap32_max(x); }
__device__ __forceinline__ float sum_lo3(float x) { x += dpp<XOR1>(x); x += dpp<XOR2>(x); x += dpp<HMIR>(x); return x; }
__device__ __forceinline__ float max_lo3(float x) { x = fmaxf(x, dpp<XOR1>(x)); x = fmaxf(x, dpp<XOR2>(x)); x = fmaxf(x, dpp<HMIR>(x)); return x; }
__device__ __forceinline__ float treduce4(float a0, float a1, float a2, float a3) {
  return swap32_add(swap16_add(a0, a2), swap16_add(a1, a3));
}
__device__ __forceinline__ void lds_arrive(u32* c, int l) {
  asm volatile("s_waitcnt lgkmcnt(0)" ::: "memory");
  if (l == 0) __hip_atomic_fetch_add((LAS u32*)c, 1u, __ATOMIC_RELAXED, __HIP_MEMORY_SCOPE_WORKGROUP);
  asm volatile("" ::: "memory");
}
__device__ __forceinline__ u32 lds_ticket(u32* c, int l) {
  asm volatile("s_waitcnt lgkmcnt(0)" ::: "memory");
  u32 t = 0;
  if (l == 0) t = __hip_atomic_fetch_add((LAS u32*)c, 1u, __ATOMIC_RELAXED, __HIP_MEMORY_SCOPE_WORKGROUP);
  t = __builtin_amdgcn_readfirstlane(t);
  asm volatile("" ::: "memory");
  return t;
}
__device__ __forceinline__ void lds_wait(u32* c, u32 n) {
  asm volatile("" ::: "memory");
  while (__builtin_amdgcn_readfirstlane(__hip_atomic_load((LAS u32*)c, __ATOMIC_RELAXED, __HIP_MEMORY_SCOPE_WORKGROUP)) < n) __builtin_amdgcn_s_sleep(1);
  asm volatile("" ::: "memory");
}
#define SCHED() __builtin_amdgcn_sched_barrier(0)
__device__ __forceinline__ void wg_barrier() {
  asm volatile("s_waitcnt lgkmcnt(0)" ::: "memory");
  __builtin_amdgcn_s_barrier();
  asm volatile("" ::: "memory");
}
#define STAMP(i)
#define RETRY(n)
#define STR2(x) #x
#define STR(x) STR2(x)
#define PACE() asm volatile("s_waitcnt vmcnt(" STR(PD) ")" ::: "memory")

enum { C_S1 = 0, C_PROG, C_Q, C_FIN, C_EPOCH };

__global__ void __launch_bounds__(768, 3)
fused_decode_kernel(const u16* __restrict__ x, const char* __restrict__ kc, const char* __restrict__ vc,
                    const char* __restrict__ wq, const char* __restrict__ wo, const char* __restrict__ w1,
                    const char* __restrict__ w3, const char* __restrict__ w2, u16* __restrict__ out, char* ws) {
  __shared__ __attribute__((aligned(16))) float lds[640 + 96 + 32 + 64 + 24 * 64 + 16 * 192 + 16 * 128];
  u32* xn_s = (u32*)lds;
  u32* q_s = (u32*)(lds + 640);
  float* m_s = lds + 736;
  float* l_s = lds + 744;
  u32* cnt = (u32*)(lds + 752);
  u32* h_s = (u32*)(lds + 768);
  float* s1_s = lds + 832;
  float* op_s = s1_s + 24 * 64;
  float* yp_s = op_s + 16 * 192;
  STAMP(0);
  const int b = blockIdx.x;
  const int g = (b & 7) * 4 + (b >> 6), s = (b >> 3) & 7;
  const int tid = threadIdx.x;
  const int w = __builtin_amdgcn_readfirstlane(tid >> 6), l = tid & 63;
  const int r = l >> 3, j = l & 7;
  const __amdgpu_buffer_rsrc_t wsr = mkrsrc(ws, WS_END);
  const u32 grec = WS_REC + g * NS * REC, myrec = grec + s * REC;
  const bool is_red = (s == (g >> 2)) && !(g & 1);
  const bool red_b = (g & 2) != 0;
  const int hh = l >> 5;
  const bool useful = ((l >> 3) & 1) == hh;
  const int dcol = 8 * j + 4 * ((l >> 4) & 1);
#define MAKE_MSK() u32x4 msk; _Pragma("unroll") for (int d = 0; d < 4; ++d) msk[d] = (j >> 1) == d ? ((j & 1) ? 0xffff0000u : 0x0000ffffu) : 0u

  if (w >= 4) {
    const int sw = w - 4;
    const size_t kvbase = ((size_t)g * SK + (SK - WIN)) * (DH * 2);
    const __amdgpu_buffer_rsrc_t rk = mkrsrc(kc + kvbase, WIN * DH * 2), rv = mkrsrc(vc + kvbase, WIN * DH * 2);
    const u32 ll = (u32)(sw * 3072 + r * 384 + j * 16);
    const int rot = s + (g & 7) * 8 + (ROT == 1 ? (g >> 3) * 2 : 0);
#define UOFF(u) ((((u) * 8 + rot) & 63) * 24576)
    u32x4 K[8][3], V[8][3];
    const u32 tz = TU ? (u32)__builtin_amdgcn_s_memrealtime() + TB + sw * TU / 8 : 0u;
#define GATE(n) do { if (TU && (n) >= 1) { while ((int)((u32)__builtin_amdgcn_s_memrealtime() - (tz + (n) * TU)) < 0) __builtin_amdgcn_s_sleep(1); asm volatile("" ::: "memory"); } } while (0)
#define LOAD1(dst, rs, u, c) do { PACE(); dst[u][c] = __builtin_amdgcn_raw_buffer_load_b128(rs, ll + (c) * 128, UOFF(u), NT); } while (0)
#define LOAD3(dst, rs, u) do { GATE(8 + (u)); LOAD1(dst, rs, u, 0); LOAD1(dst, rs, u, 1); LOAD1(dst, rs, u, 2); SCHED(); } while (0)
#pragma unroll
    for (int i = 0; i < PRE; ++i) LOAD1(K, rk, i / 3, i % 3);
    SCHED();
    wg_barrier();
#pragma unroll
    for (int i = PRE; i < 24; ++i) { if (i % 3 == 0) GATE(i / 3); LOAD1(K, rk, i / 3, i % 3); if (i == GPOS * 3 + 2) lds_arrive(cnt + C_PROG, l); }
    SCHED();
    STAMP(1);
    LOAD3(V, rv, 0); LOAD3(V, rv, 1); LOAD3(V, rv, 2);
    STAMP(2);
    lds_wait(cnt + C_Q, 1);
    STAMP(3);
    MAKE_MSK();
    u32 ppk[8];
    {
      u32x4 q[3];
      float p[8];
#pragma unroll
      for (int c = 0; c < 3; ++c) q[c] = *reinterpret_cast<const u32x4*>(q_s + c * 32 + j * 4);
#define QK(u) do { float d = 0.f; _Pragma("unroll") for (int c = 0; c < 3; ++c) _Pragma("unroll") for (int k = 0; k < 4; ++k) d = dot2(K[u][c][k], q[c][k], d); \
      p[u] = rbf(rbf(sum_lo3(d)) * 0.07216878364870322f); } while (0)
#define QK2(u) do { QK(u); QK(u + 1); asm volatile("" : "+v"(p[u]), "+v"(p[u + 1])); SCHED(); } while (0)
      QK2(0); LOAD3(V, rv, 3);
      QK2(2); LOAD3(V, rv, 4);
      QK2(4); LOAD3(V, rv, 5);
      QK2(6);
      const float mw = max_hi3(fmaxf(fmaxf(fmaxf(p[0], p[1]), fmaxf(p[2], p[3])), fmaxf(fmaxf(p[4], p[5]), fmaxf(p[6], p[7]))));
      float ls = 0.f;
#pragma unroll
      for (int u = 0; u < 8; ++u) { const float e = __builtin_amdgcn_exp2f((p[u] - mw) * LOG2E); ls += e; ppk[u] = pkbf(e); }
      ls = sum_hi3(ls);
      if (l == 0) { m_s[sw] = mw; l_s[sw] = ls; }
    }
    STAMP(4);
    f32x4 acc[3];
#pragma unroll
    for (int c = 0; c < 3; ++c) acc[c] = f32x4{0.f, 0.f, 0.f, 0.f};
#define PV(u) do { const u32x4 a_ = msk & ppk[u]; _Pragma("unroll") for (int c = 0; c < 3; ++c) acc[c] = mfma(a_, V[u][c], acc[c]); \
      asm volatile("" : "+v"(acc[0]), "+v"(acc[1]), "+v"(acc[2])); SCHED(); } while (0)
    LOAD3(V, rv, 6);
    PV(0); PV(1); PV(2); PV(3);
    LOAD3(V, rv, 7);
    u32x4 WO[5];
    const int ocol = red_b ? 512 + s * 16 + 4 * (sw & 3) : s * 64 + 8 * sw + 4 * (l & 1);
    const int ok2 = 2 * ((l >> 4) & 1) + hh;
    u32 xraw;
    {
      const __amdgpu_buffer_rsrc_t rw = mkrsrc((sw < 6) ? wo + (size_t)(g * DH + sw * 32) * (DM * 2) : w2 + (size_t)(g * 48 + (sw - 6) * 32) * (DM * 2), 32 * DM * 2);
      const u32 wl = ((sw == 7) ? (r & 3) : r) * (4 * DM * 2) + j * 16;
#define WLD(i) WO[i] = __builtin_amdgcn_raw_buffer_load_b128(rw, wl + (i) * (DM * 2), s * 128, NT)
#define WLD4() do { WO[4] = __builtin_amdgcn_raw_buffer_load_b128(rw, wl, (s & 3) * (DM * 2) + (8 + (s >> 2)) * 128, NT); xraw = x[ocol + ok2]; } while (0)
#define GATET(t) do { while ((int)((u32)__builtin_amdgcn_s_memrealtime() - (tz + (t))) < 0) __builtin_amdgcn_s_sleep(1); asm volatile("" ::: "memory"); } while (0)
      PV(4); PV(5); PV(6);
      GATET(15 * TU + WGA);
      WLD(0); WLD(1); WLD(2); WLD(3); WLD4();
      SCHED();
    PV(7);
    STAMP(5);
    if (useful) {
#pragma unroll
      for (int c = 0; c < 3; ++c) *reinterpret_cast<f32x4*>(op_s + (sw * 2 + hh) * 192 + c * 64 + dcol) = acc[c];
    }
    wg_barrier();
    }
    STAMP(6);
    u32 opk[4];
    if (sw < 6) {
      const u32 goff = grec + j * REC + R_G2 + sw * 128 + r * 16;
      const u32 soff = grec + j * REC + R_ST;
      u32x2 SS;
      u32x4 O;
      if (D1) __builtin_amdgcn_s_sleep(D1);
      bool ok = false;
      u32 spins = 0;
      for (;;) {
        if (!ok) {
          SS = __builtin_amdgcn_raw_buffer_load_b64(wsr, soff, 0, SC1);
          O = __builtin_amdgcn_raw_buffer_load_b128(wsr, goff, 0, SC1);
          ok = (O[0] & O[1] & O[2] & O[3] & SS[0] & SS[1] & 1) != 0;
        }
        if (__all(ok) || ++spins > SPIN_CAP) break;
        __builtin_amdgcn_s_sleep(POLL_SLEEP);
      }
      STAMP(7);
      const float ms = __uint_as_float(SS[0] & ~1u);
      const float f = __builtin_amdgcn_exp2f((ms - max_lo3(ms)) * LOG2E);
      const float sc = f * __builtin_amdgcn_rcpf(sum_lo3(f * __uint_as_float(SS[1])));
#pragma unroll
      for (int i = 0; i < 4; ++i) opk[i] = pkbf(sum_lo3(sc * __uint_as_float(O[i])));
    } else {
      const u32x4 hq = *reinterpret_cast<const u32x4*>(h_s + (sw - 6) * 32 + 4 * r);
#pragma unroll
      for (int i = 0; i < 4; ++i) opk[i] = hq[i];
    }
    {
      f32x4 y1 = {0.f, 0.f, 0.f, 0.f}, y2 = y1;
      const u32 o2 = (s & 2) ? ((s & 1) ? opk[3] : opk[2]) : ((s & 1) ? opk[1] : opk[0]);
      y2 = mfma(msk & o2, WO[4], y2);
#pragma unroll
      for (int i = 0; i < 4; ++i) y1 = mfma(msk & opk[i], WO[i], y1);
      if (useful) {
        *reinterpret_cast<f32x4*>(yp_s + (sw * 2 + hh) * 128 + dcol) = y1;
        *reinterpret_cast<f32x4*>(yp_s + (sw * 2 + hh) * 128 + 64 + dcol) = y2;
      }
    }
    wg_barrier();
    STAMP(8);
    if (is_red && !(red_b && sw >= 4)) {
      const u32 goff = red_b ? WS_REC + ((l & 31) * NS + (s & 4) + hh) * REC + R_G3 + 256 + (s & 3) * 64 + sw * 16
                             : WS_REC + ((l >> 1) * NS + s) * REC + R_G3 + sw * 32 + (l & 1) * 16;
      u32x4 Y, Z;
      if (D2) __builtin_amdgcn_s_sleep(D2);
      bool ok = false;
      u32 spins = 0;
      for (;;) {
        if (!ok) {
          Y = __builtin_amdgcn_raw_buffer_load_b128(wsr, goff, 0, SC1);
          Z = __builtin_amdgcn_raw_buffer_load_b128(wsr, red_b ? goff + 2 * REC : OOB, 0, SC1);
          ok = (Y[0] & Y[1] & Y[2] & Y[3] & (red_b ? Z[0] & Z[1] & Z[2] & Z[3] : 1u) & 1) != 0;
        }
        if (__all(ok) || ++spins > SPIN_CAP) break;
        __builtin_amdgcn_s_sleep(FSLEEP);
      }
      const f32x4 yz = __builtin_bit_cast(f32x4, Y) + __builtin_bit_cast(f32x4, Z);
      float t = treduce4(yz[0], yz[1], yz[2], yz[3]);
      t += dpp<ROR8>(t); t += dpp<ROR4>(t); t += dpp<XOR2>(t);
      const float t1 = t + dpp<XOR1>(t);
      if ((l & (red_b ? 15 : 14)) == 0) out[ocol + ok2] = __builtin_bit_cast(u16, (__bf16)(__uint_as_float(xraw << 16) + (red_b ? t1 : t)));
      if (b == 0 && sw == 0) __builtin_amdgcn_raw_buffer_store_b32(cnt[C_EPOCH], wsr, l == 0 ? WS_EPOCH : OOB, 0, SC1);
    }
  } else {
    const __amdgpu_buffer_rsrc_t rx = mkrsrc(x, DM * 2);
    const u32x4 xa = __builtin_amdgcn_raw_buffer_load_b128(rx, l * 16, 0, 0);
    const u32 xb = __builtin_amdgcn_raw_buffer_load_b32(rx, l * 4, 1024, 0);
    const int qd = g >> 2, wi = (g & 3) * 8 + s;
    u32x4 S1[12];
    int row0[3];
#pragma unroll
    for (int k = 0; k < 3; ++k) {
      const int z = 9 * wi + (k < 2 ? w + 4 * k : 8), ln = z >> 4;
      row0[k] = (z & 15) * 40 + (k == 2 ? w * 8 : 0);
      const bool isq = ln < 12;
      const u32 rs = isq ? NH * DH * 2 : FF * 2;
      const char* p = isq ? wq + qd * 1536 + ln * 128 : (ln < 15 ? w1 + (ln - 12) * 128 : w3 + (ln - 15) * 128) + qd * 384;
      const __amdgpu_buffer_rsrc_t rr = mkrsrc(p, (DM - 1) * rs + 128);
      const u32 vo = r * rs + j * 16;
      if (k < 2) {
#pragma unroll
        for (int i = 0; i < 5; ++i) S1[5 * k + i] = __builtin_amdgcn_raw_buffer_load_b128(rr, vo, (row0[k] + 8 * i) * rs, NT);
      } else {
        S1[10] = __builtin_amdgcn_raw_buffer_load_b128(rr, vo, row0[2] * rs, NT);
        S1[11] = __builtin_amdgcn_raw_buffer_load_b128(rr, w == 0 ? vo + (row0[2] + 32) * rs : OOB, 0, NT);
      }
    }
    const u32 epoch = __builtin_amdgcn_raw_buffer_load_b32(wsr, WS_EPOCH, 0, SC1);
    {
      const u32x4 z = {0u, 0u, 0u, 0u};
      __builtin_amdgcn_raw_buffer_store_b128(z, wsr, (w == 3 && l < 49) ? myrec + R_G2 + l * 16 : (w == 2 && l < 32) ? myrec + R_G3 + l * 16 : OOB, 0, SC1);
    }
    if (tid < 8) cnt[tid] = 0;
    SCHED();
    wg_barrier();
    SCHED();
    {
      float xv[10];
#pragma unroll
      for (int k = 0; k < 4; ++k) { xv[2 * k] = bf_lo(xa[k]); xv[2 * k + 1] = bf_hi(xa[k]); }
      xv[8] = bf_lo(xb); xv[9] = bf_hi(xb);
      float sm = 0.f;
#pragma unroll
      for (int k = 0; k < 10; ++k) sm += xv[k];
      sm = sum_hi3(sum_lo3(sm));
      const float mean = sm * (1.f / DM);
      float sv = 0.f;
#pragma unroll
      for (int k = 0; k < 10; ++k) { const float d = xv[k] - mean; sv = fmaf(d, d, sv); }
      sv = sum_hi3(sum_lo3(sv));
      const float var = rbf(sv * (1.f / DM));
      const float mb = rbf(mean);
      const float sd = rbf(sqrtf(rbf(var + 1e-5f)));
      u32 xp[10];
#pragma unroll
      for (int k = 0; k < 10; ++k) xp[k] = pkbf(rbf(xv[k] - mb) / sd);
      *reinterpret_cast<u32x4*>(xn_s + l * 8) = u32x4{xp[0], xp[1], xp[2], xp[3]};
      *reinterpret_cast<u32x4*>(xn_s + l * 8 + 4) = u32x4{xp[4], xp[5], xp[6], xp[7]};
      *reinterpret_cast<u32x2*>(xn_s + 512 + l * 2) = u32x2{xp[8], xp[9]};
      asm volatile("s_waitcnt lgkmcnt(0)" ::: "memory");
    }
    MAKE_MSK();
    const u32 tag = epoch + 1;
    {
      u32 xr[12];
#pragma unroll
      for (int i = 0; i < 10; ++i) xr[i] = xn_s[row0[i / 5] + 8 * (i % 5) + r];
      xr[10] = xn_s[row0[2] + r];
      xr[11] = w == 0 ? xn_s[row0[2] + 32 + r] : 0u;
      f32x4 a[3];
#pragma unroll
      for (int k = 0; k < 3; ++k) a[k] = f32x4{0.f, 0.f, 0.f, 0.f};
#pragma unroll
      for (int i = 0; i < 12; ++i) a[i / 5] = mfma(msk & xr[i], S1[i], a[i / 5]);
      if (useful) {
#pragma unroll
        for (int k = 0; k < 3; ++k) *reinterpret_cast<f32x4*>(s1_s + (k * 8 + w * 2 + hh) * 64 + dcol) = a[k];
      }
      asm volatile("s_waitcnt vmcnt(0)" ::: "memory");
      if (lds_ticket(cnt + C_S1, l) == 3) {
        const int nA = 16 - ((9 * wi) & 15);
        float va = 0.f, vb = 0.f;
#pragma unroll
        for (int c = 0; c < 8; ++c) {
          const float v = s1_s[(2 * c) * 64 + l] + s1_s[(2 * c + 1) * 64 + l];
          if (c < nA) va += v; else vb += v;
        }
        float v8 = 0.f;
#pragma unroll
        for (int t = 16; t < 24; ++t) v8 += s1_s[t * 64 + l];
        if (8 < nA) va += v8; else vb += v8;
        __builtin_amdgcn_raw_buffer_store_b64(u32x2{__float_as_uint(va), tag}, wsr, myrec + R_G1 + l * 8, 0, SC1);
        __builtin_amdgcn_raw_buffer_store_b64(u32x2{__float_as_uint(vb), tag}, wsr, nA <= 8 ? myrec + R_G1 + 512 + l * 8 : OOB, 0, SC1);
      }
    }
    STAMP(1);
    {
      u32 ga[10];
      bool use3[3];
#pragma unroll
      for (int k = 0; k < 3; ++k) {
        const int fc = (g & 3) * 48 + min(l, 47);
        const int ln = w == 0 ? (g & 3) * 3 + k : 12 + 3 * min(k, 1) + (fc >> 6);
        const int col = w == 0 ? l : (fc & 63);
        const int f0 = (16 * ln) / 9, f1 = (16 * ln + 15) / 9;
        use3[k] = f0 + 2 <= f1;
#pragma unroll
        for (int i = 0; i < 3; ++i) {
          const int pw = min(f0 + i, f1);
          ga[k * 3 + i] = WS_REC + (qd * 32 + pw) * REC + R_G1 + (9 * pw < 16 * ln ? 512 : 0) + col * 8;
        }
      }
      ga[9] = grec + j * REC + R_G1;
      if (w == 1) {
        ga[9] = WS_REC + ((l & 31) * NS + (red_b ? (s & 4) + hh : s)) * REC + R_G1;
        ga[8] = ga[9] + (red_b ? 2 * REC : 0);
      }
      lds_wait(cnt + C_PROG, 8);
      if (w != 2 && (w != 1 || is_red)) {
        u32x2 G[10];
        bool ok = false;
        u32 spins = 0;
        for (;;) {
          if (!ok) {
#pragma unroll
            for (int i = 0; i < 10; ++i) if (w == 0 || (w == 3 && i < 6) || (w == 1 && i >= 8)) G[i] = __builtin_amdgcn_raw_buffer_load_b64(wsr, ga[i], 0, SC1);
            u32 d_ = 0;
#pragma unroll
            for (int i = 0; i < 10; ++i) if (w == 0 || (w == 3 && i < 6) || (w == 1 && i >= 8)) d_ |= G[i][1] ^ tag;
            ok = (d_ == 0);
          }
          if (__all(ok) || ++spins > SPIN_CAP) break;
          RETRY(w == 0 ? 1 : w == 3 ? 1000 : 100000);
          __builtin_amdgcn_s_sleep(8);
        }
        float t[3];
#pragma unroll
        for (int k = 0; k < 3; ++k) t[k] = __uint_as_float(G[3 * k][0]) + __uint_as_float(G[3 * k + 1][0]) + (use3[k] ? __uint_as_float(G[3 * k + 2][0]) : 0.f);
        if (w == 0) {
#pragma unroll
          for (int k = 0; k < 3; ++k) ((u16*)q_s)[64 * k + l] = __builtin_bit_cast(u16, (__bf16)t[k]);
          if (l == 0) cnt[C_EPOCH] = tag;
          lds_arrive(cnt + C_Q, l);
        } else if (w == 3) {
          const float gate = rbf(t[0]), up = rbf(t[1]);
          const float hv = rbf(gate / (1.f + __expf(-gate))) * up;
          h_s[l] = l < 48 ? pkbf(hv) : 0u;
        }
      }
    }
    STAMP(2);
    __builtin_amdgcn_s_setprio(3);
    wg_barrier();
    STAMP(5);
    {
      const int col = min(tid, 191);
      float pv[16];
#pragma unroll
      for (int i = 0; i < 16; ++i) pv[i] = op_s[i * 192 + col];
      const float mj = m_s[j];
      const float M = max_lo3(mj);
      const float fj = __builtin_amdgcn_exp2f((mj - M) * LOG2E);
      float o = 0.f;
      if (w == 3) {
        const float L = sum_lo3(l_s[j] * fj);
        o = (l & 1) ? L : M;
      } else {
#pragma unroll
        for (int i = 0; i < 8; ++i) o = fmaf(__uint_as_float(__builtin_amdgcn_readlane(__float_as_uint(fj), i)), pv[2 * i] + pv[2 * i + 1], o);
      }
      __builtin_amdgcn_raw_buffer_store_b32(__float_as_uint(o) | 1u, wsr, tid < 194 ? myrec + R_G2 + tid * 4 : OOB, 0, SC1);
    }
    STAMP(6);
    wg_barrier();
    STAMP(7);
    if (w < 2) {
      float y = 0.f;
#pragma unroll
      for (int i = 0; i < 16; ++i) y += yp_s[i * 128 + tid];
      __builtin_amdgcn_raw_buffer_store_b32(__float_as_uint(y) | 1u, wsr, myrec + R_G3 + tid * 4, 0, SC1);
    }
    STAMP(8);
  }
  STAMP(9);
}

extern "C" void kernel_launch(void* const* d_in, const int* in_sizes, int n_in, void* d_out, int out_size, void* d_ws,
                              size_t ws_size, hipStream_t stream) {
  fused_decode_kernel<<<256, 768, 0, stream>>>((const u16*)d_in[0], (const char*)d_in[1], (const char*)d_in[2],
                                               (const char*)d_in[3], (const char*)d_in[4], (const char*)d_in[6],
                                               (const char*)d_in[7], (const char*)d_in[8], (u16*)d_out, (char*)d_ws);
}
```

```cpp
#define GT 0
#define SYNC0 0
#define TB -15
#include <hip/hip_runtime.h>
#include <hip/hip_bf16.h>
#include <stdint.h>

typedef unsigned short u16;
typedef unsigned int u32;
typedef unsigned long long u64;
typedef u32 u32x2 __attribute__((ext_vector_type(2)));
typedef u32 u32x4 __attribute__((ext_vector_type(4)));
typedef float f32x4 __attribute__((ext_vector_type(4)));
typedef __attribute__((ext_vector_type(2))) __bf16 bf16x2;
typedef __attribute__((ext_vector_type(8))) __bf16 bf16x8;
#define LAS __attribute__((address_space(3)))

#define POLL_SLEEP 2
#define PD 7
#define GPOS 5
#define PRE 0
#define TU 100
#define D1 24
#define D2 12
#define WG16 16
#define FSLEEP 4
#define ROT 0
#define WGA TU
#define WOPACE 2
#define DIAG 0

constexpr int DM = 640, NH = 32, DH = 192, SK = 32768, WIN = 4096, FF = 1536;
constexpr int NS = 8, KPS = WIN / NS;
constexpr u32 WS_EPOCH = 0;
constexpr u32 WS_REC = 4096, REC = 2560;
constexpr u32 R_G1 = 0;
constexpr u32 R_G2 = 1024;
constexpr u32 R_ST = R_G2 + 768;
constexpr u32 R_G3 = 1920;
constexpr u32 R_T0 = 2432;
constexpr u32 WS_END = WS_REC + NH * NS * REC;
constexpr int WS_STAMP = 1 << 19;
constexpr int NWAVE = 12, NHELP = 4;
constexpr u32 SPIN_CAP = 1u << 22;
constexpr float LOG2E = 1.4426950408889634f;
constexpr u32 OOB = 0xfffffff0u;
constexpr int NT = 2, SC1 = 16;

__device__ __forceinline__ float bf_lo(u32 v) { return __uint_as_float(v << 16); }
__device__ __forceinline__ float bf_hi(u32 v) { return __uint_as_float(v & 0xffff0000u); }
__device__ __forceinline__ float rbf(float f) { return (float)(__bf16)f; }
__device__ __forceinline__ u32 pkbf(float f) { const bf16x2 h = {(__bf16)f, (__bf16)f}; return __builtin_bit_cast(u32, h); }
__device__ __forceinline__ float dot2(u32 a, u32 b, float c) {
  return __builtin_amdgcn_fdot2_f32_bf16(__builtin_bit_cast(bf16x2, a), __builtin_bit_cast(bf16x2, b), c, false);
}
__device__ __forceinline__ f32x4 mfma(u32x4 a, u32x4 b, f32x4 c) {
  return __builtin_amdgcn_mfma_f32_16x16x32_bf16(__builtin_bit_cast(bf16x8, a), __builtin_bit_cast(bf16x8, b), c, 0, 0, 0);
}
__device__ __forceinline__ __amdgpu_buffer_rsrc_t mkrsrc(const void* p, u32 bytes) {
  return __builtin_amdgcn_make_buffer_rsrc(const_cast<void*>(p), 0, bytes, 0x00020000);
}
template <int CTRL> __device__ __forceinline__ float dpp(float x) {
  return __builtin_bit_cast(float, __builtin_amdgcn_mov_dpp(__builtin_bit_cast(int, x), CTRL, 0xf, 0xf, true));
}
constexpr int XOR1 = 0xB1, XOR2 = 0x4E, HMIR = 0x141, ROR8 = 0x128, ROR4 = 0x124;
__device__ __forceinline__ float swap16_add(float a, float b) {
  auto s = __builtin_amdgcn_permlane16_swap(__float_as_uint(a), __float_as_uint(b), false, false);
  return __uint_as_float(s[0]) + __uint_as_float(s[1]);
}
__device__ __forceinline__ float swap32_add(float a, float b) {
  auto s = __builtin_amdgcn_permlane32_swap(__float_as_uint(a), __float_as_uint(b), false, false);
  return __uint_as_float(s[0]) + __uint_as_float(s[1]);
}
__device__ __forceinline__ float swap16_max(float a) {
  auto s = __builtin_amdgcn_permlane16_swap(__float_as_uint(a), __float_as_uint(a), false, false);
  return fmaxf(__uint_as_float(s[0]), __uint_as_float(s[1]));
}
__device__ __forceinline__ float swap32_max(float a) {
  auto s = __builtin_amdgcn_permlane32_swap(__float_as_uint(a), __float_as_uint(a), false, false);
  return fmaxf(__uint_as_float(s[0]), __uint_as_float(s[1]));
}
__device__ __forceinline__ float sum_hi3(float x) { x += dpp<ROR8>(x); x = swap16_add(x, x); return swap32_add(x, x); }
__device__ __forceinline__ float max_hi3(float x) { x = fmaxf(x, dpp<ROR8>(x)); x = swap16_max(x); return swap32_max(x); }
__device__ __forceinline__ float sum_lo3(float x) { x += dpp<XOR1>(x); x += dpp<XOR2>(x); x += dpp<HMIR>(x); return x; }
__device__ __forceinline__ float max_lo3(float x) { x = fmaxf(x, dpp<XOR1>(x)); x = fmaxf(x, dpp<XOR2>(x)); x = fmaxf(x, dpp<HMIR>(x)); return x; }
__device__ __forceinline__ float treduce4(float a0, float a1, float a2, float a3) {
  return swap32_add(swap16_add(a0, a2), swap16_add(a1, a3));
}
__device__ __forceinline__ void lds_arrive(u32* c, int l) {
  asm volatile("s_waitcnt lgkmcnt(0)" ::: "memory");
  if (l == 0) __hip_atomic_fetch_add((LAS u32*)c, 1u, __ATOMIC_RELAXED, __HIP_MEMORY_SCOPE_WORKGROUP);
  asm volatile("" ::: "memory");
}
__device__ __forceinline__ u32 lds_ticket(u32* c, int l) {
  asm volatile("s_waitcnt lgkmcnt(0)" ::: "memory");
  u32 t = 0;
  if (l == 0) t = __hip_atomic_fetch_add((LAS u32*)c, 1u, __ATOMIC_RELAXED, __HIP_MEMORY_SCOPE_WORKGROUP);
  t = __builtin_amdgcn_readfirstlane(t);
  asm volatile("" ::: "memory");
  return t;
}
__device__ __forceinline__ void lds_wait(u32* c, u32 n) {
  asm volatile("" ::: "memory");
  while (__builtin_amdgcn_readfirstlane(__hip_atomic_load((LAS u32*)c, __ATOMIC_RELAXED, __HIP_MEMORY_SCOPE_WORKGROUP)) < n) __builtin_amdgcn_s_sleep(1);
  asm volatile("" ::: "memory");
}
#define SCHED() __builtin_amdgcn_sched_barrier(0)
__device__ __forceinline__ void wg_barrier() {
  asm volatile("s_waitcnt lgkmcnt(0)" ::: "memory");
  __builtin_amdgcn_s_barrier();
  asm volatile("" ::: "memory");
}
#define STAMP(i)
#define RETRY(n)
#define STR2(x) #x
#define STR(x) STR2(x)
#define PACE() asm volatile("s_waitcnt vmcnt(" STR(PD) ")" ::: "memory")

enum { C_S1 = 0, C_PROG, C_Q, C_TZ, C_EPOCH };

__global__ void __launch_bounds__(768, 3)
fused_decode_kernel(const u16* __restrict__ x, const char* __restrict__ kc, const char* __restrict__ vc,
                    const char* __restrict__ wq, const char* __restrict__ wo, const char* __restrict__ w1,
                    const char* __restrict__ w3, const char* __restrict__ w2, u16* __restrict__ out, char* ws) {
  __shared__ __attribute__((aligned(16))) float lds[640 + 96 + 32 + 64 + 24 * 64 + 16 * 192 + 16 * 128];
  u32* xn_s = (u32*)lds;
  u32* q_s = (u32*)(lds + 640);
  float* m_s = lds + 736;
  float* l_s = lds + 744;
  u32* cnt = (u32*)(lds + 752);
  u32* h_s = (u32*)(lds + 768);
  float* s1_s = lds + 832;
  float* op_s = s1_s + 24 * 64;
  float* yp_s = op_s + 16 * 192;
  STAMP(0);
  u32 t0 = (u32)__builtin_amdgcn_s_memrealtime();
  asm volatile("" : "+s"(t0));
  const int b = blockIdx.x;
  const int g = (b & 7) * 4 + (b >> 6), s = (b >> 3) & 7;
  const int tid = threadIdx.x;
  const int w = __builtin_amdgcn_readfirstlane(tid >> 6), l = tid & 63;
  const int r = l >> 3, j = l & 7;
  const __amdgpu_buffer_rsrc_t wsr = mkrsrc(ws, WS_END);
  const u32 grec = WS_REC + g * NS * REC, myrec = grec + s * REC;
  const bool is_red = (s == (g >> 2)) && !(g & 1);
  const bool red_b = (g & 2) != 0;
  const int hh = l >> 5;
  const bool useful = ((l >> 3) & 1) == hh;
  const int dcol = 8 * j + 4 * ((l >> 4) & 1);
#define MAKE_MSK() u32x4 msk; _Pragma("unroll") for (int d = 0; d < 4; ++d) msk[d] = (j >> 1) == d ? ((j & 1) ? 0xffff0000u : 0x0000ffffu) : 0u

  if (w >= 4) {
    const int sw = w - 4;
    const size_t kvbase = ((size_t)g * SK + (SK - WIN)) * (DH * 2);
    const __amdgpu_buffer_rsrc_t rk = mkrsrc(kc + kvbase, WIN * DH * 2), rv = mkrsrc(vc + kvbase, WIN * DH * 2);
    const u32 ll = (u32)(sw * 3072 + r * 384 + j * 16);
    const int rot = s + (g & 7) * 8 + (ROT == 1 ? (g >> 3) * 2 : 0);
#define UOFF(u) ((((u) * 8 + rot) & 63) * 24576)
    u32x4 K[8][3], V[8][3];
    u32 tz = t0 + TB + sw * TU / 8;
#define GATE(n) do { if (TU && (n) >= 1) { while ((int)((u32)__builtin_amdgcn_s_memrealtime() - (tz + (n) * TU)) < 0) __builtin_amdgcn_s_sleep(1); asm volatile("" ::: "memory"); } } while (0)
#define LOAD1(dst, rs, u, c) do { PACE(); dst[u][c] = __builtin_amdgcn_raw_buffer_load_b128(rs, ll + (c) * 128, UOFF(u), NT); } while (0)
#define LOAD3(dst, rs, u) do { GATE(8 + (u)); LOAD1(dst, rs, u, 0); LOAD1(dst, rs, u, 1); LOAD1(dst, rs, u, 2); SCHED(); } while (0)
#pragma unroll
    for (int i = 0; i < PRE; ++i) LOAD1(K, rk, i / 3, i % 3);
    SCHED();
    wg_barrier();
#pragma unroll
    for (int i = PRE; i < 24; ++i) { if (i % 3 == 0) GATE(i / 3); LOAD1(K, rk, i / 3, i % 3); if (!GT && i == GPOS * 3 + 2) lds_arrive(cnt + C_PROG, l); }
    SCHED();
    STAMP(1);
    LOAD3(V, rv, 0); LOAD3(V, rv, 1); LOAD3(V, rv, 2);
    STAMP(2);
    lds_wait(cnt + C_Q, 1);
    if (SYNC0) tz -= __builtin_amdgcn_readfirstlane(cnt[C_TZ]);
    STAMP(3);
    MAKE_MSK();
    u32 ppk[8];
    {
      u32x4 q[3];
      float p[8];
#pragma unroll
      for (int c = 0; c < 3; ++c) q[c] = *reinterpret_cast<const u32x4*>(q_s + c * 32 + j * 4);
#define QK(u) do { float d = 0.f; _Pragma("unroll") for (int c = 0; c < 3; ++c) _Pragma("unroll") for (int k = 0; k < 4; ++k) d = dot2(K[u][c][k], q[c][k], d); \
      p[u] = rbf(rbf(sum_lo3(d)) * 0.07216878364870322f); } while (0)
#define QK2(u) do { QK(u); QK(u + 1); asm volatile("" : "+v"(p[u]), "+v"(p[u + 1])); SCHED(); } while (0)
      QK2(0); LOAD3(V, rv, 3);
      QK2(2); LOAD3(V, rv, 4);
      QK2(4); LOAD3(V, rv, 5);
      QK2(6);
      const float mw = max_hi3(fmaxf(fmaxf(fmaxf(p[0], p[1]), fmaxf(p[2], p[3])), fmaxf(fmaxf(p[4], p[5]), fmaxf(p[6], p[7]))));
      float ls = 0.f;
#pragma unroll
      for (int u = 0; u < 8; ++u) { const float e = __builtin_amdgcn_exp2f((p[u] - mw) * LOG2E); ls += e; ppk[u] = pkbf(e); }
      ls = sum_hi3(ls);
      if (l == 0) { m_s[sw] = mw; l_s[sw] = ls; }
    }
    STAMP(4);
    f32x4 acc[3];
#pragma unroll
    for (int c = 0; c < 3; ++c) acc[c] = f32x4{0.f, 0.f, 0.f, 0.f};
#define PV(u) do { const u32x4 a_ = msk & ppk[u]; _Pragma("unroll") for (int c = 0; c < 3; ++c) acc[c] = mfma(a_, V[u][c], acc[c]); \
      asm volatile("" : "+v"(acc[0]), "+v"(acc[1]), "+v"(acc[2])); SCHED(); } while (0)
    LOAD3(V, rv, 6);
    PV(0); PV(1); PV(2); PV(3);
    LOAD3(V, rv, 7);
    u32x4 WO[5];
    const int ocol = red_b ? 512 + s * 16 + 4 * (sw & 3) : s * 64 + 8 * sw + 4 * (l & 1);
    const int ok2 = 2 * ((l >> 4) & 1) + hh;
    u32 xraw;
    {
      const __amdgpu_buffer_rsrc_t rw = mkrsrc((sw < 6) ? wo + (size_t)(g * DH + sw * 32) * (DM * 2) : w2 + (size_t)(g * 48 + (sw - 6) * 32) * (DM * 2), 32 * DM * 2);
      const u32 wl = ((sw == 7) ? (r & 3) : r) * (4 * DM * 2) + j * 16;
#define WLD(i) WO[i] = __builtin_amdgcn_raw_buffer_load_b128(rw, wl + (i) * (DM * 2), s * 128, NT)
#define WLD4() do { WO[4] = __builtin_amdgcn_raw_buffer_load_b128(rw, wl, (s & 3) * (DM * 2) + (8 + (s >> 2)) * 128, NT); xraw = x[ocol + ok2]; } while (0)
#define GATET(t) do { while ((int)((u32)__builtin_amdgcn_s_memrealtime() - (tz + (t))) < 0) __builtin_amdgcn_s_sleep(1); asm volatile("" ::: "memory"); } while (0)
      PV(4); PV(5); PV(6);
      GATET(15 * TU + WGA);
      WLD(0); WLD(1); WLD(2);
      SCHED();
    PV(7);
    STAMP(5);
    if (useful) {
#pragma unroll
      for (int c = 0; c < 3; ++c) *reinterpret_cast<f32x4*>(op_s + (sw * 2 + hh) * 192 + c * 64 + dcol) = acc[c];
    }
    wg_barrier();
    WLD(3); WLD4();
    SCHED();
    }
    STAMP(6);
    u32 opk[4];
    if (sw < 6) {
      const u32 goff = grec + j * REC + R_G2 + sw * 128 + r * 16;
      const u32 soff = grec + j * REC + R_ST;
      u32x2 SS;
      u32x4 O;
      if (D1) __builtin_amdgcn_s_sleep(D1);
      bool ok = false;
      u32 spins = 0;
      for (;;) {
        if (!ok) {
          SS = __builtin_amdgcn_raw_buffer_load_b64(wsr, soff, 0, SC1);
          O = __builtin_amdgcn_raw_buffer_load_b128(wsr, goff, 0, SC1);
          ok = (O[0] & O[1] & O[2] & O[3] & SS[0] & SS[1] & 1) != 0;
        }
        if (__all(ok) || ++spins > SPIN_CAP) break;
        __builtin_amdgcn_s_sleep(POLL_SLEEP);
      }
      STAMP(7);
      const float ms = __uint_as_float(SS[0] & ~1u);
      const float f = __builtin_amdgcn_exp2f((ms - max_lo3(ms)) * LOG2E);
      const float sc = f * __builtin_amdgcn_rcpf(sum_lo3(f * __uint_as_float(SS[1])));
#pragma unroll
      for (int i = 0; i < 4; ++i) opk[i] = pkbf(sum_lo3(sc * __uint_as_float(O[i])));
    } else {
      const u32x4 hq = *reinterpret_cast<const u32x4*>(h_s + (sw - 6) * 32 + 4 * r);
#pragma unroll
      for (int i = 0; i < 4; ++i) opk[i] = hq[i];
    }
    {
      f32x4 y1 = {0.f, 0.f, 0.f, 0.f}, y2 = y1;
      const u32 o2 = (s & 2) ? ((s & 1) ? opk[3] : opk[2]) : ((s & 1) ? opk[1] : opk[0]);
      y2 = mfma(msk & o2, WO[4], y2);
#pragma unroll
      for (int i = 0; i < 4; ++i) y1 = mfma(msk & opk[i], WO[i], y1);
      if (useful) {
        *reinterpret_cast<f32x4*>(yp_s + (sw * 2 + hh) * 128 + dcol) = y1;
        *reinterpret_cast<f32x4*>(yp_s + (sw * 2 + hh) * 128 + 64 + dcol) = y2;
      }
    }
    wg_barrier();
    STAMP(8);
    if (is_red && !(red_b && sw >= 4)) {
      const u32 goff = red_b ? WS_REC + ((l & 31) * NS + (s & 4) + hh) * REC + R_G3 + 256 + (s & 3) * 64 + sw * 16
                             : WS_REC + ((l >> 1) * NS + s) * REC + R_G3 + sw * 32 + (l & 1) * 16;
      u32x4 Y, Z;
      if (D2) __builtin_amdgcn_s_sleep(D2);
      bool ok = false;
      u32 spins = 0;
      for (;;) {
        if (!ok) {
          Y = __builtin_amdgcn_raw_buffer_load_b128(wsr, goff, 0, SC1);
          Z = __builtin_amdgcn_raw_buffer_load_b128(wsr, red_b ? goff + 2 * REC : OOB, 0, SC1);
          ok = (Y[0] & Y[1] & Y[2] & Y[3] & (red_b ? Z[0] & Z[1] & Z[2] & Z[3] : 1u) & 1) != 0;
        }
        if (__all(ok) || ++spins > SPIN_CAP) break;
        __builtin_amdgcn_s_sleep(FSLEEP);
      }
      const f32x4 yz = __builtin_bit_cast(f32x4, Y) + __builtin_bit_cast(f32x4, Z);
      float t = treduce4(yz[0], yz[1], yz[2], yz[3]);
      t += dpp<ROR8>(t); t += dpp<ROR4>(t); t += dpp<XOR2>(t);
      const float t1 = t + dpp<XOR1>(t);
      if ((l & (red_b ? 15 : 14)) == 0) out[ocol + ok2] = __builtin_bit_cast(u16, (__bf16)(__uint_as_float(xraw << 16) + (red_b ? t1 : t)));
      if (b == 0 && sw == 0) __builtin_amdgcn_raw_buffer_store_b32(cnt[C_EPOCH], wsr, l == 0 ? WS_EPOCH : OOB, 0, SC1);
    }
  } else {
    const __amdgpu_buffer_rsrc_t rx = mkrsrc(x, DM * 2);
    const u32x4 xa = __builtin_amdgcn_raw_buffer_load_b128(rx, l * 16, 0, 0);
    const u32 xb = __builtin_amdgcn_raw_buffer_load_b32(rx, l * 4, 1024, 0);
    const int qd = g >> 2, wi = (g & 3) * 8 + s;
    u32x4 S1[12];
    int row0[3];
#pragma unroll
    for (int k = 0; k < 3; ++k) {
      const int z = 9 * wi + (k < 2 ? w + 4 * k : 8), ln = z >> 4;
      row0[k] = (z & 15) * 40 + (k == 2 ? w * 8 : 0);
      const bool isq = ln < 12;
      const u32 rs = isq ? NH * DH * 2 : FF * 2;
      const char* p = isq ? wq + qd * 1536 + ln * 128 : (ln < 15 ? w1 + (ln - 12) * 128 : w3 + (ln - 15) * 128) + qd * 384;
      const __amdgpu_buffer_rsrc_t rr = mkrsrc(p, (DM - 1) * rs + 128);
      const u32 vo = r * rs + j * 16;
      if (k < 2) {
#pragma unroll
        for (int i = 0; i < 5; ++i) S1[5 * k + i] = __builtin_amdgcn_raw_buffer_load_b128(rr, vo, (row0[k] + 8 * i) * rs, NT);
      } else {
        S1[10] = __builtin_amdgcn_raw_buffer_load_b128(rr, vo, row0[2] * rs, NT);
        S1[11] = __builtin_amdgcn_raw_buffer_load_b128(rr, w == 0 ? vo + (row0[2] + 32) * rs : OOB, 0, NT);
      }
    }
    const u32 epoch = __builtin_amdgcn_raw_buffer_load_b32(wsr, WS_EPOCH, 0, SC1);
    {
      const u32x4 z = {0u, 0u, 0u, 0u};
      __builtin_amdgcn_raw_buffer_store_b128(z, wsr, (w == 3 && l < 49) ? myrec + R_G2 + l * 16 : (w == 2 && l < 32) ? myrec + R_G3 + l * 16 : OOB, 0, SC1);
    }
    if (tid < 8) cnt[tid] = 0;
    SCHED();
    wg_barrier();
    SCHED();
    {
      float xv[10];
#pragma unroll
      for (int k = 0; k < 4; ++k) { xv[2 * k] = bf_lo(xa[k]); xv[2 * k + 1] = bf_hi(xa[k]); }
      xv[8] = bf_lo(xb); xv[9] = bf_hi(xb);
      float sm = 0.f;
#pragma unroll
      for (int k = 0; k < 10; ++k) sm += xv[k];
      sm = sum_hi3(sum_lo3(sm));
      const float mean = sm * (1.f / DM);
      float sv = 0.f;
#pragma unroll
      for (int k = 0; k < 10; ++k) { const float d = xv[k] - mean; sv = fmaf(d, d, sv); }
      sv = sum_hi3(sum_lo3(sv));
      const float var = rbf(sv * (1.f / DM));
      const float mb = rbf(mean);
      const float sd = rbf(sqrtf(rbf(var + 1e-5f)));
      u32 xp[10];
#pragma unroll
      for (int k = 0; k < 10; ++k) xp[k] = pkbf(rbf(xv[k] - mb) / sd);
      *reinterpret_cast<u32x4*>(xn_s + l * 8) = u32x4{xp[0], xp[1], xp[2], xp[3]};
      *reinterpret_cast<u32x4*>(xn_s + l * 8 + 4) = u32x4{xp[4], xp[5], xp[6], xp[7]};
      *reinterpret_cast<u32x2*>(xn_s + 512 + l * 2) = u32x2{xp[8], xp[9]};
      asm volatile("s_waitcnt lgkmcnt(0)" ::: "memory");
    }
    MAKE_MSK();
    const u32 tag = epoch + 1;
    {
      u32 xr[12];
#pragma unroll
      for (int i = 0; i < 10; ++i) xr[i] = xn_s[row0[i / 5] + 8 * (i % 5) + r];
      xr[10] = xn_s[row0[2] + r];
      xr[11] = w == 0 ? xn_s[row0[2] + 32 + r] : 0u;
      f32x4 a[3];
#pragma unroll
      for (int k = 0; k < 3; ++k) a[k] = f32x4{0.f, 0.f, 0.f, 0.f};
#pragma unroll
      for (int i = 0; i < 12; ++i) a[i / 5] = mfma(msk & xr[i], S1[i], a[i / 5]);
      if (useful) {
#pragma unroll
        for (int k = 0; k < 3; ++k) *reinterpret_cast<f32x4*>(s1_s + (k * 8 + w * 2 + hh) * 64 + dcol) = a[k];
      }
      asm volatile("s_waitcnt vmcnt(0)" ::: "memory");
      if (lds_ticket(cnt + C_S1, l) == 3) {
        const int nA = 16 - ((9 * wi) & 15);
        float va = 0.f, vb = 0.f;
#pragma unroll
        for (int c = 0; c < 8; ++c) {
          const float v = s1_s[(2 * c) * 64 + l] + s1_s[(2 * c + 1) * 64 + l];
          if (c < nA) va += v; else vb += v;
        }
        float v8 = 0.f;
#pragma unroll
        for (int t = 16; t < 24; ++t) v8 += s1_s[t * 64 + l];
        if (8 < nA) va += v8; else vb += v8;
        __builtin_amdgcn_raw_buffer_store_b64(u32x2{__float_as_uint(va), tag}, wsr, myrec + R_G1 + l * 8, 0, SC1);
        __builtin_amdgcn_raw_buffer_store_b64(u32x2{__float_as_uint(vb), tag}, wsr, nA <= 8 ? myrec + R_G1 + 512 + l * 8 : OOB, 0, SC1);
        if (SYNC0) __builtin_amdgcn_raw_buffer_store_b64(u32x2{t0, tag}, wsr, (b < 8 && l == 0) ? myrec + R_T0 : OOB, 0, SC1);
      }
    }
    STAMP(1);
    {
      u32 ga[10];
      bool use3[3];
#pragma unroll
      for (int k = 0; k < 3; ++k) {
        const int fc = (g & 3) * 48 + min(l, 47);
        const int ln = w == 0 ? (g & 3) * 3 + k : 12 + 3 * min(k, 1) + (fc >> 6);
        const int col = w == 0 ? l : (fc & 63);
        const int f0 = (16 * ln) / 9, f1 = (16 * ln + 15) / 9;
        use3[k] = f0 + 2 <= f1;
#pragma unroll
        for (int i = 0; i < 3; ++i) {
          const int pw = min(f0 + i, f1);
          ga[k * 3 + i] = WS_REC + (qd * 32 + pw) * REC + R_G1 + (9 * pw < 16 * ln ? 512 : 0) + col * 8;
        }
      }
      ga[9] = grec + j * REC + R_G1;
      if (w == 1) {
        ga[9] = WS_REC + ((l & 31) * NS + (red_b ? (s & 4) + hh : s)) * REC + R_G1;
        ga[8] = ga[9] + (red_b ? 2 * REC : 0);
      }
      if (GT) { while ((int)((u32)__builtin_amdgcn_s_memrealtime() - (t0 + GT)) < 0) __builtin_amdgcn_s_sleep(4); asm volatile("" ::: "memory"); }
      else lds_wait(cnt + C_PROG, 8);
      if (w != 2 && (w != 1 || is_red)) {
        u32x2 G[10];
        u32x2 T0 = {0u, 0u};
        if (SYNC0 && w == 0) T0 = __builtin_amdgcn_raw_buffer_load_b64(wsr, WS_REC + j * 4 * NS * REC + R_T0, 0, SC1);
        bool ok = false;
        u32 spins = 0;
        for (;;) {
          if (!ok) {
#pragma unroll
            for (int i = 0; i < 10; ++i) if (w == 0 || (w == 3 && i < 6) || (w == 1 && i >= 8)) G[i] = __builtin_amdgcn_raw_buffer_load_b64(wsr, ga[i], 0, SC1);
            u32 d_ = 0;
#pragma unroll
            for (int i = 0; i < 10; ++i) if (w == 0 || (w == 3 && i < 6) || (w == 1 && i >= 8)) d_ |= G[i][1] ^ tag;
            ok = (d_ == 0);
          }
          if (__all(ok) || ++spins > SPIN_CAP) break;
          RETRY(w == 0 ? 1 : w == 3 ? 1000 : 100000);
          __builtin_amdgcn_s_sleep(8);
        }
        float t[3];
#pragma unroll
        for (int k = 0; k < 3; ++k) t[k] = __uint_as_float(G[3 * k][0]) + __uint_as_float(G[3 * k + 1][0]) + (use3[k] ? __uint_as_float(G[3 * k + 2][0]) : 0.f);
        if (w == 0) {
#pragma unroll
          for (int k = 0; k < 3; ++k) ((u16*)q_s)[64 * k + l] = __builtin_bit_cast(u16, (__bf16)t[k]);
          if (SYNC0) {
            const int late = T0[1] == tag ? min(max((int)(t0 - T0[0]), -100), 100) : 0;
            const float lm = sum_lo3((float)late) * 0.125f;
            if (l == 0) cnt[C_TZ] = (u32)(int)lm;
          }
          if (l == 0) cnt[C_EPOCH] = tag;
          lds_arrive(cnt + C_Q, l);
        } else if (w == 3) {
          const float gate = rbf(t[0]), up = rbf(t[1]);
          const float hv = rbf(gate / (1.f + __expf(-gate))) * up;
          h_s[l] = l < 48 ? pkbf(hv) : 0u;
        }
      }
    }
    STAMP(2);
    __builtin_amdgcn_s_setprio(3);
    wg_barrier();
    STAMP(5);
    {
      const int col = min(tid, 191);
      float pv[16];
#pragma unroll
      for (int i = 0; i < 16; ++i) pv[i] = op_s[i * 192 + col];
      const float mj = m_s[j];
      const float M = max_lo3(mj);
      const float fj = __builtin_amdgcn_exp2f((mj - M) * LOG2E);
      float o = 0.f;
      if (w == 3) {
        const float L = sum_lo3(l_s[j] * fj);
        o = (l & 1) ? L : M;
      } else {
#pragma unroll
        for (int i = 0; i < 8; ++i) o = fmaf(__uint_as_float(__builtin_amdgcn_readlane(__float_as_uint(fj), i)), pv[2 * i] + pv[2 * i + 1], o);
      }
      __builtin_amdgcn_raw_buffer_store_b32(__float_as_uint(o) | 1u, wsr, tid < 194 ? myrec + R_G2 + tid * 4 : OOB, 0, SC1);
    }
    STAMP(6);
    wg_barrier();
    STAMP(7);
    if (w < 2) {
      float y = 0.f;
#pragma unroll
      for (int i = 0; i < 16; ++i) y += yp_s[i * 128 + tid];
      __builtin_amdgcn_raw_buffer_store_b32(__float_as_uint(y) | 1u, wsr, myrec + R_G3 + tid * 4, 0, SC1);
    }
    STAMP(8);
  }
  STAMP(9);
}

extern "C" void kernel_launch(void* const* d_in, const int* in_sizes, int n_in, void* d_out, int out_size, void* d_ws,
                              size_t ws_size, hipStream_t stream) {
  fused_decode_kernel<<<256, 768, 0, stream>>>((const u16*)d_in[0], (const char*)d_in[1], (const char*)d_in[2],
                                               (const char*)d_in[3], (const char*)d_in[4], (const char*)d_in[6],
                                               (const char*)d_in[7], (const char*)d_in[8], (u16*)d_out, (char*)d_ws);
}
```

```cpp
#define TB 180
#include <hip/hip_runtime.h>
#include <hip/hip_bf16.h>
#include <stdint.h>

typedef unsigned short u16;
typedef unsigned int u32;
typedef unsigned long long u64;
typedef u32 u32x2 __attribute__((ext_vector_type(2)));
typedef u32 u32x4 __attribute__((ext_vector_type(4)));
typedef float f32x4 __attribute__((ext_vector_type(4)));
typedef __attribute__((ext_vector_type(2))) __bf16 bf16x2;
typedef __attribute__((ext_vector_type(8))) __bf16 bf16x8;
#define LAS __attribute__((address_space(3)))

#define TU 87
#define D1 24
#define D2 12
#define PD 7
#define GPOS 5
#define DIAG 0

constexpr int DM = 640, NH = 32, DH = 192, SK = 32768, WIN = 4096, FF = 1536;
constexpr int NS = 8;
constexpr u32 WS_EPOCH = 0;
constexpr u32 WS_REC = 4096, REC = 2560;
constexpr u32 R_G1 = 0;
constexpr u32 R_G2 = 1024;
constexpr u32 R_ST = R_G2 + 768;
constexpr u32 R_G3 = 1920;
constexpr u32 WS_END = WS_REC + NH * NS * REC;
constexpr int WS_STAMP = 1 << 19;
constexpr int NWAVE = 12, NHELP = 4;
constexpr u32 SPIN_CAP = 1u << 19;
constexpr float LOG2E = 1.4426950408889634f;
constexpr u32 OOB = 0xfffffff0u;
constexpr int NT = 2, SC1 = 16;

__device__ __forceinline__ float bf_lo(u32 v) { return __uint_as_float(v << 16); }
__device__ __forceinline__ float bf_hi(u32 v) { return __uint_as_float(v & 0xffff0000u); }
__device__ __forceinline__ float rbf(float f) { return (float)(__bf16)f; }
__device__ __forceinline__ u32 pkbf(float f) { const bf16x2 h = {(__bf16)f, (__bf16)f}; return __builtin_bit_cast(u32, h); }
__device__ __forceinline__ float dot2(u32 a, u32 b, float c) {
  return __builtin_amdgcn_fdot2_f32_bf16(__builtin_bit_cast(bf16x2, a), __builtin_bit_cast(bf16x2, b), c, false);
}
__device__ __forceinline__ f32x4 mfma(u32x4 a, u32x4 b, f32x4 c) {
  return __builtin_amdgcn_mfma_f32_16x16x32_bf16(__builtin_bit_cast(bf16x8, a), __builtin_bit_cast(bf16x8, b), c, 0, 0, 0);
}
__device__ __forceinline__ __amdgpu_buffer_rsrc_t mkrsrc(const void* p, u32 bytes) {
  return __builtin_amdgcn_make_buffer_rsrc(const_cast<void*>(p), 0, bytes, 0x00020000);
}
template <int CTRL> __device__ __forceinline__ float dpp(float x) {
  return __builtin_bit_cast(float, __builtin_amdgcn_mov_dpp(__builtin_bit_cast(int, x), CTRL, 0xf, 0xf, true));
}
constexpr int XOR1 = 0xB1, XOR2 = 0x4E, HMIR = 0x141, ROR8 = 0x128, ROR4 = 0x124;
__device__ __forceinline__ float swap16_add(float a, float b) {
  auto s = __builtin_amdgcn_permlane16_swap(__float_as_uint(a), __float_as_uint(b), false, false);
  return __uint_as_float(s[0]) + __uint_as_float(s[1]);
}
__device__ __forceinline__ float swap32_add(float a, float b) {
  auto s = __builtin_amdgcn_permlane32_swap(__float_as_uint(a), __float_as_uint(b), false, false);
  return __uint_as_float(s[0]) + __uint_as_float(s[1]);
}
__device__ __forceinline__ float swap16_max(float a) {
  auto s = __builtin_amdgcn_permlane16_swap(__float_as_uint(a), __float_as_uint(a), false, false);
  return fmaxf(__uint_as_float(s[0]), __uint_as_float(s[1]));
}
__device__ __forceinline__ float swap32_max(float a) {
  auto s = __builtin_amdgcn_permlane32_swap(__float_as_uint(a), __float_as_uint(a), false, false);
  return fmaxf(__uint_as_float(s[0]), __uint_as_float(s[1]));
}
__device__ __forceinline__ float sum_hi3(float x) { x += dpp<ROR8>(x); x = swap16_add(x, x); return swap32_add(x, x); }
__device__ __forceinline__ float max_hi3(float x) { x = fmaxf(x, dpp<ROR8>(x)); x = swap16_max(x); return swap32_max(x); }
__device__ __forceinline__ float sum_lo3(float x) { x += dpp<XOR1>(x); x += dpp<XOR2>(x); x += dpp<HMIR>(x); return x; }
__device__ __forceinline__ float max_lo3(float x) { x = fmaxf(x, dpp<XOR1>(x)); x = fmaxf(x, dpp<XOR2>(x)); x = fmaxf(x, dpp<HMIR>(x)); return x; }
__device__ __forceinline__ float treduce4(float a0, float a1, float a2, float a3) {
  return swap32_add(swap16_add(a0, a2), swap16_add(a1, a3));
}
__device__ __forceinline__ void lds_arrive(u32* c, int l) {
  asm volatile("s_waitcnt lgkmcnt(0)" ::: "memory");
  if (l == 0) __hip_atomic_fetch_add((LAS u32*)c, 1u, __ATOMIC_RELAXED, __HIP_MEMORY_SCOPE_WORKGROUP);
  asm volatile("" ::: "memory");
}
__device__ __forceinline__ u32 lds_ticket(u32* c, int l) {
  asm volatile("s_waitcnt lgkmcnt(0)" ::: "memory");
  u32 t = 0;
  if (l == 0) t = __hip_atomic_fetch_add((LAS u32*)c, 1u, __ATOMIC_RELAXED, __HIP_MEMORY_SCOPE_WORKGROUP);
  t = __builtin_amdgcn_readfirstlane(t);
  asm volatile("" ::: "memory");
  return t;
}
__device__ __forceinline__ void lds_wait(u32* c, u32 n) {
  asm volatile("" ::: "memory");
  while (__builtin_amdgcn_readfirstlane(__hip_atomic_load((LAS u32*)c, __ATOMIC_RELAXED, __HIP_MEMORY_SCOPE_WORKGROUP)) < n) __builtin_amdgcn_s_sleep(1);
  asm volatile("" ::: "memory");
}
#define SCHED() __builtin_amdgcn_sched_barrier(0)
__device__ __forceinline__ void wg_barrier() {
  asm volatile("s_waitcnt lgkmcnt(0)" ::: "memory");
  __builtin_amdgcn_s_barrier();
  asm volatile("" ::: "memory");
}
#define STAMP(i)
#define RETRY(n)
#define STR2(x) #x
#define STR(x) STR2(x)
#define PACE() asm volatile("s_waitcnt vmcnt(" STR(PD) ")" ::: "memory")

enum { C_S1 = 0, C_PROG, C_Q, C_EPOCH };

__global__ void __launch_bounds__(768, 3)
fused_decode_kernel(const u16* __restrict__ x, const char* __restrict__ kc, const char* __restrict__ vc,
                    const char* __restrict__ wq, const char* __restrict__ wo, const char* __restrict__ w1,
                    const char* __restrict__ w3, const char* __restrict__ w2, u16* __restrict__ out, char* ws) {
  __shared__ __attribute__((aligned(16))) float lds[640 + 96 + 32 + 64 + 24 * 64 + 16 * 192 + 16 * 128];
  u32* xn_s = (u32*)lds;
  u32* q_s = (u32*)(lds + 640);
  float* m_s = lds + 736;
  float* l_s = lds + 744;
  u32* cnt = (u32*)(lds + 752);
  u32* h_s = (u32*)(lds + 768);
  float* s1_s = lds + 832;
  float* op_s = s1_s + 24 * 64;
  float* yp_s = op_s + 16 * 192;
  STAMP(0);
  u32 t0 = (u32)__builtin_amdgcn_s_memrealtime();
  asm volatile("" : "+s"(t0));
  const int b = blockIdx.x;
  const int g = (b & 7) * 4 + (b >> 6), s = (b >> 3) & 7;
  const int tid = threadIdx.x;
  const int w = __builtin_amdgcn_readfirstlane(tid >> 6), l = tid & 63;
  const int r = l >> 3, j = l & 7;
  const __amdgpu_buffer_rsrc_t wsr = mkrsrc(ws, WS_END);
  const u32 grec = WS_REC + g * NS * REC, myrec = grec + s * REC;
  const bool is_red = (s == (g >> 2)) && !(g & 1);
  const bool red_b = (g & 2) != 0;
  const int hh = l >> 5;
  const bool useful = ((l >> 3) & 1) == hh;
  const int dcol = 8 * j + 4 * ((l >> 4) & 1);
#define MAKE_MSK() u32x4 msk; _Pragma("unroll") for (int d = 0; d < 4; ++d) msk[d] = (j >> 1) == d ? ((j & 1) ? 0xffff0000u : 0x0000ffffu) : 0u

  if (w >= 4) {
    const int sw = w - 4;
    const size_t kvbase = ((size_t)g * SK + (SK - WIN)) * (DH * 2);
    const __amdgpu_buffer_rsrc_t rk = mkrsrc(kc + kvbase, WIN * DH * 2), rv = mkrsrc(vc + kvbase, WIN * DH * 2);
    const u32 ll = (u32)(sw * 3072 + r * 384 + j * 16);
    const int rot = s + (g & 7) * 8;
#define UOFF(u) ((((u) * 8 + rot) & 63) * 24576)
    u32x4 K[8][3], V[8][3];
    const u32 tz = t0 + TB + sw * TU / 8;
#define GATE(n) do { while ((int)((u32)__builtin_amdgcn_s_memrealtime() - (tz + (n) * TU)) < 0) __builtin_amdgcn_s_sleep(1); asm volatile("" ::: "memory"); } while (0)
#define LOAD1(dst, rs, u, c) do { PACE(); dst[u][c] = __builtin_amdgcn_raw_buffer_load_b128(rs, ll + (c) * 128, UOFF(u), NT); } while (0)
#define LOAD3(dst, rs, u) do { GATE(8 + (u)); LOAD1(dst, rs, u, 0); LOAD1(dst, rs, u, 1); LOAD1(dst, rs, u, 2); SCHED(); } while (0)
    wg_barrier();
#pragma unroll
    for (int i = 0; i < 24; ++i) { if (i % 3 == 0 && i) GATE(i / 3); LOAD1(K, rk, i / 3, i % 3); if (i == GPOS * 3 + 2) lds_arrive(cnt + C_PROG, l); }
    SCHED();
    STAMP(1);
    LOAD3(V, rv, 0); LOAD3(V, rv, 1); LOAD3(V, rv, 2);
    STAMP(2);
    lds_wait(cnt + C_Q, 1);
    STAMP(3);
    MAKE_MSK();
    u32 ppk[8];
    {
      u32x4 q[3];
      float p[8];
#pragma unroll
      for (int c = 0; c < 3; ++c) q[c] = *reinterpret_cast<const u32x4*>(q_s + c * 32 + j * 4);
#define QK(u) do { float d = 0.f; _Pragma("unroll") for (int c = 0; c < 3; ++c) _Pragma("unroll") for (int k = 0; k < 4; ++k) d = dot2(K[u][c][k], q[c][k], d); \
      p[u] = rbf(rbf(sum_lo3(d)) * 0.07216878364870322f); } while (0)
#define QK2(u) do { QK(u); QK(u + 1); asm volatile("" : "+v"(p[u]), "+v"(p[u + 1])); SCHED(); } while (0)
      QK2(0); LOAD3(V, rv, 3);
      QK2(2); LOAD3(V, rv, 4);
      QK2(4); LOAD3(V, rv, 5);
      QK2(6);
      const float mw = max_hi3(fmaxf(fmaxf(fmaxf(p[0], p[1]), fmaxf(p[2], p[3])), fmaxf(fmaxf(p[4], p[5]), fmaxf(p[6], p[7]))));
      float ls = 0.f;
#pragma unroll
      for (int u = 0; u < 8; ++u) { const float e = __builtin_amdgcn_exp2f((p[u] - mw) * LOG2E); ls += e; ppk[u] = pkbf(e); }
      ls = sum_hi3(ls);
      if (l == 0) { m_s[sw] = mw; l_s[sw] = ls; }
    }
    STAMP(4);
    f32x4 acc[3];
#pragma unroll
    for (int c = 0; c < 3; ++c) acc[c] = f32x4{0.f, 0.f, 0.f, 0.f};
#define PV(u) do { const u32x4 a_ = msk & ppk[u]; _Pragma("unroll") for (int c = 0; c < 3; ++c) acc[c] = mfma(a_, V[u][c], acc[c]); \
      asm volatile("" : "+v"(acc[0]), "+v"(acc[1]), "+v"(acc[2])); SCHED(); } while (0)
    LOAD3(V, rv, 6);
    PV(0); PV(1); PV(2); PV(3);
    LOAD3(V, rv, 7);
    u32x4 WO[5];
    const int ocol = red_b ? 512 + s * 16 + 4 * (sw & 3) : s * 64 + 8 * sw + 4 * (l & 1);
    const int ok2 = 2 * ((l >> 4) & 1) + hh;
    u32 xraw;
    {
      const __amdgpu_buffer_rsrc_t rw = mkrsrc((sw < 6) ? wo + (size_t)(g * DH + sw * 32) * (DM * 2) : w2 + (size_t)(g * 48 + (sw - 6) * 32) * (DM * 2), 32 * DM * 2);
      const u32 wl = ((sw == 7) ? (r & 3) : r) * (4 * DM * 2) + j * 16;
#define WLD(i) WO[i] = __builtin_amdgcn_raw_buffer_load_b128(rw, wl + (i) * (DM * 2), s * 128, NT)
#define WLD4() do { WO[4] = __builtin_amdgcn_raw_buffer_load_b128(rw, wl, (s & 3) * (DM * 2) + (8 + (s >> 2)) * 128, NT); xraw = x[ocol + ok2]; } while (0)
      PV(4); PV(5); PV(6);
      GATE(16);
      WLD(0); WLD(1); WLD(2);
      SCHED();
    PV(7);
    STAMP(5);
    if (useful) {
#pragma unroll
      for (int c = 0; c < 3; ++c) *reinterpret_cast<f32x4*>(op_s + (sw * 2 + hh) * 192 + c * 64 + dcol) = acc[c];
    }
    wg_barrier();
    WLD(3); WLD4();
    SCHED();
    }
    STAMP(6);
    u32 opk[4];
    if (sw < 6) {
      const u32 goff = grec + j * REC + R_G2 + sw * 128 + r * 16;
      const u32 soff = grec + j * REC + R_ST;
      u32x2 SS;
      u32x4 O;
      __builtin_amdgcn_s_sleep(D1);
      bool ok = false;
      u32 spins = 0;
      for (;;) {
        if (!ok) {
          SS = __builtin_amdgcn_raw_buffer_load_b64(wsr, soff, 0, SC1);
          O = __builtin_amdgcn_raw_buffer_load_b128(wsr, goff, 0, SC1);
          ok = (O[0] & O[1] & O[2] & O[3] & SS[0] & SS[1] & 1) != 0;
        }
        if (__all(ok) || ++spins > SPIN_CAP) break;
        __builtin_amdgcn_s_sleep(2);
      }
      STAMP(7);
      const float ms = __uint_as_float(SS[0] & ~1u);
      const float f = __builtin_amdgcn_exp2f((ms - max_lo3(ms)) * LOG2E);
      const float sc = f * __builtin_amdgcn_rcpf(sum_lo3(f * __uint_as_float(SS[1])));
#pragma unroll
      for (int i = 0; i < 4; ++i) opk[i] = pkbf(sum_lo3(sc * __uint_as_float(O[i])));
    } else {
      const u32x4 hq = *reinterpret_cast<const u32x4*>(h_s + (sw - 6) * 32 + 4 * r);
#pragma unroll
      for (int i = 0; i < 4; ++i) opk[i] = hq[i];
    }
    {
      f32x4 y1 = {0.f, 0.f, 0.f, 0.f}, y2 = y1;
      const u32 o2 = (s & 2) ? ((s & 1) ? opk[3] : opk[2]) : ((s & 1) ? opk[1] : opk[0]);
      y2 = mfma(msk & o2, WO[4], y2);
#pragma unroll
      for (int i = 0; i < 4; ++i) y1 = mfma(msk & opk[i], WO[i], y1);
      if (useful) {
        *reinterpret_cast<f32x4*>(yp_s + (sw * 2 + hh) * 128 + dcol) = y1;
        *reinterpret_cast<f32x4*>(yp_s + (sw * 2 + hh) * 128 + 64 + dcol) = y2;
      }
    }
    wg_barrier();
    STAMP(8);
    if (is_red && !(red_b && sw >= 4)) {
      const u32 goff = red_b ? WS_REC + ((l & 31) * NS + (s & 4) + hh) * REC + R_G3 + 256 + (s & 3) * 64 + sw * 16
                             : WS_REC + ((l >> 1) * NS + s) * REC + R_G3 + sw * 32 + (l & 1) * 16;
      u32x4 Y, Z;
      __builtin_amdgcn_s_sleep(D2);
      bool ok = false;
      u32 spins = 0;
      for (;;) {
        if (!ok) {
          Y = __builtin_amdgcn_raw_buffer_load_b128(wsr, goff, 0, SC1);
          Z = __builtin_amdgcn_raw_buffer_load_b128(wsr, red_b ? goff + 2 * REC : OOB, 0, SC1);
          ok = (Y[0] & Y[1] & Y[2] & Y[3] & (red_b ? Z[0] & Z[1] & Z[2] & Z[3] : 1u) & 1) != 0;
        }
        if (__all(ok) || ++spins > SPIN_CAP) break;
        __builtin_amdgcn_s_sleep(4);
      }
      const f32x4 yz = __builtin_bit_cast(f32x4, Y) + __builtin_bit_cast(f32x4, Z);
      float t = treduce4(yz[0], yz[1], yz[2], yz[3]);
      t += dpp<ROR8>(t); t += dpp<ROR4>(t); t += dpp<XOR2>(t);
      const float t1 = t + dpp<XOR1>(t);
      if ((l & (red_b ? 15 : 14)) == 0) out[ocol + ok2] = __builtin_bit_cast(u16, (__bf16)(__uint_as_float(xraw << 16) + (red_b ? t1 : t)));
      if (b == 0 && sw == 0) __builtin_amdgcn_raw_buffer_store_b32(cnt[C_EPOCH], wsr, l == 0 ? WS_EPOCH : OOB, 0, SC1);
    }
  } else {
    const int qd = g >> 2, wi = (g & 3) * 8 + s;
    u32x4 S1[12], xa;
    u32 xb;
    int row0[3];
#pragma unroll
    for (int k = 0; k < 3; ++k) {
      const int z = 9 * wi + (k < 2 ? w + 4 * k : 8), ln = z >> 4;
      row0[k] = (z & 15) * 40 + (k == 2 ? w * 8 : 0);
      const bool isq = ln < 12;
      const u32 rs = isq ? NH * DH * 2 : FF * 2;
      const u32 so = row0[k] * rs + (isq ? qd * 1536 + ln * 128 : qd * 384 + (ln - (ln < 15 ? 12 : 15)) * 128);
      const __amdgpu_buffer_rsrc_t rr = mkrsrc(isq ? wq : ln < 15 ? w1 : w3, DM * rs);
      const u32 vo = r * rs + j * 16;
      if (k < 2) {
#pragma unroll
        for (int i = 0; i < 5; ++i) S1[5 * k + i] = __builtin_amdgcn_raw_buffer_load_b128(rr, vo, so + 8 * i * rs, NT);
      } else {
        S1[10] = __builtin_amdgcn_raw_buffer_load_b128(rr, vo, so, NT);
        S1[11] = __builtin_amdgcn_raw_buffer_load_b128(rr, w == 0 ? vo + so + 32 * rs : OOB, 0, NT);
      }
      if (k == 0) {
        const __amdgpu_buffer_rsrc_t rx = mkrsrc(x, DM * 2);
        xa = __builtin_amdgcn_raw_buffer_load_b128(rx, l * 16, 0, 0);
        xb = __builtin_amdgcn_raw_buffer_load_b32(rx, l * 4, 1024, 0);
      }
      SCHED();
    }
    const u32 epoch = __builtin_amdgcn_raw_buffer_load_b32(wsr, WS_EPOCH, 0, SC1);
    {
      const u32x4 z = {0u, 0u, 0u, 0u};
      __builtin_amdgcn_raw_buffer_store_b128(z, wsr, (w == 3 && l < 49) ? myrec + R_G2 + l * 16 : (w == 2 && l < 32) ? myrec + R_G3 + l * 16 : OOB, 0, SC1);
    }
    if (tid < 8) cnt[tid] = 0;
    SCHED();
    wg_barrier();
    SCHED();
    {
      float xv[10];
#pragma unroll
      for (int k = 0; k < 4; ++k) { xv[2 * k] = bf_lo(xa[k]); xv[2 * k + 1] = bf_hi(xa[k]); }
      xv[8] = bf_lo(xb); xv[9] = bf_hi(xb);
      float sm = 0.f;
#pragma unroll
      for (int k = 0; k < 10; ++k) sm += xv[k];
      sm = sum_hi3(sum_lo3(sm));
      const float mean = sm * (1.f / DM);
      float sv = 0.f;
#pragma unroll
      for (int k = 0; k < 10; ++k) { const float d = xv[k] - mean; sv = fmaf(d, d, sv); }
      sv = sum_hi3(sum_lo3(sv));
      const float var = rbf(sv * (1.f / DM));
      const float mb = rbf(mean);
      const float sd = rbf(sqrtf(rbf(var + 1e-5f)));
      u32 xp[10];
#pragma unroll
      for (int k = 0; k < 10; ++k) xp[k] = pkbf(rbf(xv[k] - mb) / sd);
      *reinterpret_cast<u32x4*>(xn_s + l * 8) = u32x4{xp[0], xp[1], xp[2], xp[3]};
      *reinterpret_cast<u32x4*>(xn_s + l * 8 + 4) = u32x4{xp[4], xp[5], xp[6], xp[7]};
      *reinterpret_cast<u32x2*>(xn_s + 512 + l * 2) = u32x2{xp[8], xp[9]};
      asm volatile("s_waitcnt lgkmcnt(0)" ::: "memory");
    }
    MAKE_MSK();
    const u32 tag = epoch + 1;
    {
      u32 xr[12];
#pragma unroll
      for (int i = 0; i < 10; ++i) xr[i] = xn_s[row0[i / 5] + 8 * (i % 5) + r];
      xr[10] = xn_s[row0[2] + r];
      xr[11] = w == 0 ? xn_s[row0[2] + 32 + r] : 0u;
      f32x4 a[3];
#pragma unroll
      for (int k = 0; k < 3; ++k) a[k] = f32x4{0.f, 0.f, 0.f, 0.f};
#pragma unroll
      for (int i = 0; i < 12; ++i) a[i / 5] = mfma(msk & xr[i], S1[i], a[i / 5]);
      if (useful) {
#pragma unroll
        for (int k = 0; k < 3; ++k) *reinterpret_cast<f32x4*>(s1_s + (k * 8 + w * 2 + hh) * 64 + dcol) = a[k];
      }
      asm volatile("s_waitcnt vmcnt(0)" ::: "memory");
      if (lds_ticket(cnt + C_S1, l) == 3) {
        const int nA = 16 - ((9 * wi) & 15);
        float va = 0.f, vb = 0.f;
#pragma unroll
        for (int c = 0; c < 8; ++c) {
          const float v = s1_s[(2 * c) * 64 + l] + s1_s[(2 * c + 1) * 64 + l];
          if (c < nA) va += v; else vb += v;
        }
        float v8 = 0.f;
#pragma unroll
        for (int t = 16; t < 24; ++t) v8 += s1_s[t * 64 + l];
        if (8 < nA) va += v8; else vb += v8;
        __builtin_amdgcn_raw_buffer_store_b64(u32x2{__float_as_uint(va), tag}, wsr, myrec + R_G1 + l * 8, 0, SC1);
        __builtin_amdgcn_raw_buffer_store_b64(u32x2{__float_as_uint(vb), tag}, wsr, nA <= 8 ? myrec + R_G1 + 512 + l * 8 : OOB, 0, SC1);
      }
    }
    STAMP(1);
    {
      u32 ga[10];
      bool use3[3];
#pragma unroll
      for (int k = 0; k < 3; ++k) {
        const int fc = (g & 3) * 48 + min(l, 47);
        const int ln = w == 0 ? (g & 3) * 3 + k : 12 + 3 * min(k, 1) + (fc >> 6);
        const int col = w == 0 ? l : (fc & 63);
        const int f0 = (16 * ln) / 9, f1 = (16 * ln + 15) / 9;
        use3[k] = f0 + 2 <= f1;
#pragma unroll
        for (int i = 0; i < 3; ++i) {
          const int pw = min(f0 + i, f1);
          ga[k * 3 + i] = WS_REC + (qd * 32 + pw) * REC + R_G1 + (9 * pw < 16 * ln ? 512 : 0) + col * 8;
        }
      }
      ga[9] = grec + j * REC + R_G1;
      if (w == 1) {
        ga[9] = WS_REC + ((l & 31) * NS + (red_b ? (s & 4) + hh : s)) * REC + R_G1;
        ga[8] = ga[9] + (red_b ? 2 * REC : 0);
      }
      lds_wait(cnt + C_PROG, 8);
      if (w != 2 && (w != 1 || is_red)) {
        u32x2 G[10];
        bool ok = false;
        u32 spins = 0;
        for (;;) {
          if (!ok) {
#pragma unroll
            for (int i = 0; i < 10; ++i) if (w == 0 || (w == 3 && i < 6) || (w == 1 && i >= 8)) G[i] = __builtin_amdgcn_raw_buffer_load_b64(wsr, ga[i], 0, SC1);
            u32 d_ = 0;
#pragma unroll
            for (int i = 0; i < 10; ++i) if (w == 0 || (w == 3 && i < 6) || (w == 1 && i >= 8)) d_ |= G[i][1] ^ tag;
            ok = (d_ == 0);
          }
          if (__all(ok) || ++spins > SPIN_CAP) break;
          RETRY(w == 0 ? 1 : w == 3 ? 1000 : 100000);
          __builtin_amdgcn_s_sleep(8);
        }
        float t[3];
#pragma unroll
        for (int k = 0; k < 3; ++k) t[k] = __uint_as_float(G[3 * k][0]) + __uint_as_float(G[3 * k + 1][0]) + (use3[k] ? __uint_as_float(G[3 * k + 2][0]) : 0.f);
        if (w == 0) {
#pragma unroll
          for (int k = 0; k < 3; ++k) ((u16*)q_s)[64 * k + l] = __builtin_bit_cast(u16, (__bf16)t[k]);
          if (l == 0) cnt[C_EPOCH] = tag;
          lds_arrive(cnt + C_Q, l);
        } else if (w == 3) {
          const float gate = rbf(t[0]), up = rbf(t[1]);
          const float hv = rbf(gate / (1.f + __expf(-gate))) * up;
          h_s[l] = l < 48 ? pkbf(hv) : 0u;
        }
      }
    }
    STAMP(2);
    __builtin_amdgcn_s_setprio(3);
    wg_barrier();
    STAMP(5);
    {
      const int col = min(tid, 191);
      float pv[16];
#pragma unroll
      for (int i = 0; i < 16; ++i) pv[i] = op_s[i * 192 + col];
      const float mj = m_s[j];
      const float M = max_lo3(mj);
      const float fj = __builtin_amdgcn_exp2f((mj - M) * LOG2E);
      float o = 0.f;
      if (w == 3) {
        const float L = sum_lo3(l_s[j] * fj);
        o = (l & 1) ? L : M;
      } else {
#pragma unroll
        for (int i = 0; i < 8; ++i) o = fmaf(__uint_as_float(__builtin_amdgcn_readlane(__float_as_uint(fj), i)), pv[2 * i] + pv[2 * i + 1], o);
      }
      __builtin_amdgcn_raw_buffer_store_b32(__float_as_uint(o) | 1u, wsr, tid < 194 ? myrec + R_G2 + tid * 4 : OOB, 0, SC1);
    }
    STAMP(6);
    wg_barrier();
    STAMP(7);
    if (w < 2) {
      float y = 0.f;
#pragma unroll
      for (int i = 0; i < 16; ++i) y += yp_s[i * 128 + tid];
      __builtin_amdgcn_raw_buffer_store_b32(__float_as_uint(y) | 1u, wsr, myrec + R_G3 + tid * 4, 0, SC1);
    }
    STAMP(8);
  }
  STAMP(9);
}

extern "C" void kernel_launch(void* const* d_in, const int* in_sizes, int n_in, void* d_out, int out_size, void* d_ws,
                              size_t ws_size, hipStream_t stream) {
  fused_decode_kernel<<<256, 768, 0, stream>>>((const u16*)d_in[0], (const char*)d_in[1], (const char*)d_in[2],
                                               (const char*)d_in[3], (const char*)d_in[4], (const char*)d_in[6],
                                               (const char*)d_in[7], (const char*)d_in[8], (u16*)d_out, (char*)d_ws);
}
```

```cpp
#include <hip/hip_runtime.h>
#include <hip/hip_bf16.h>
#include <stdint.h>

typedef unsigned short u16;
typedef unsigned int u32;
typedef unsigned long long u64;
typedef u32 u32x2 __attribute__((ext_vector_type(2)));
typedef u32 u32x4 __attribute__((ext_vector_type(4)));
typedef float f32x4 __attribute__((ext_vector_type(4)));
typedef __attribute__((ext_vector_type(2))) __bf16 bf16x2;
typedef __attribute__((ext_vector_type(8))) __bf16 bf16x8;
#define LAS __attribute__((address_space(3)))

#define TU 87
#define TB 180
#define D1 24
#define D2 12
#define PD 7
#define GPOS 5

constexpr int DM = 640, NH = 32, DH = 192, SK = 32768, WIN = 4096, FF = 1536;
constexpr int NS = 8;
constexpr u32 WS_EPOCH = 0;
constexpr u32 WS_REC = 4096, REC = 2560;
constexpr u32 R_G1 = 0;
constexpr u32 R_G2 = 1024;
constexpr u32 R_ST = R_G2 + 768;
constexpr u32 R_G3 = 1920;
constexpr u32 WS_END = WS_REC + NH * NS * REC;
constexpr u32 SPIN_CAP = 1u << 19;
constexpr float LOG2E = 1.4426950408889634f;
constexpr u32 OOB = 0xfffffff0u;
constexpr int NT = 2, SC1 = 16;

__device__ __forceinline__ float bf_lo(u32 v) { return __uint_as_float(v << 16); }
__device__ __forceinline__ float bf_hi(u32 v) { return __uint_as_float(v & 0xffff0000u); }
__device__ __forceinline__ float rbf(float f) { return (float)(__bf16)f; }
__device__ __forceinline__ u32 pkbf(float f) { const bf16x2 h = {(__bf16)f, (__bf16)f}; return __builtin_bit_cast(u32, h); }
__device__ __forceinline__ float dot2(u32 a, u32 b, float c) {
  return __builtin_amdgcn_fdot2_f32_bf16(__builtin_bit_cast(bf16x2, a), __builtin_bit_cast(bf16x2, b), c, false);
}
__device__ __forceinline__ f32x4 mfma(u32x4 a, u32x4 b, f32x4 c) {
  return __builtin_amdgcn_mfma_f32_16x16x32_bf16(__builtin_bit_cast(bf16x8, a), __builtin_bit_cast(bf16x8, b), c, 0, 0, 0);
}
__device__ __forceinline__ __amdgpu_buffer_rsrc_t mkrsrc(const void* p, u32 bytes) {
  return __builtin_amdgcn_make_buffer_rsrc(const_cast<void*>(p), 0, bytes, 0x00020000);
}
template <int CTRL> __device__ __forceinline__ float dpp(float x) {
  return __builtin_bit_cast(float, __builtin_amdgcn_mov_dpp(__builtin_bit_cast(int, x), CTRL, 0xf, 0xf, true));
}
constexpr int XOR1 = 0xB1, XOR2 = 0x4E, HMIR = 0x141, ROR8 = 0x128, ROR4 = 0x124;
__device__ __forceinline__ float swap16_add(float a, float b) {
  auto s = __builtin_amdgcn_permlane16_swap(__float_as_uint(a), __float_as_uint(b), false, false);
  return __uint_as_float(s[0]) + __uint_as_float(s[1]);
}
__device__ __forceinline__ float swap32_add(float a, float b) {
  auto s = __builtin_amdgcn_permlane32_swap(__float_as_uint(a), __float_as_uint(b), false, false);
  return __uint_as_float(s[0]) + __uint_as_float(s[1]);
}
__device__ __forceinline__ float swap16_max(float a) {
  auto s = __builtin_amdgcn_permlane16_swap(__float_as_uint(a), __float_as_uint(a), false, false);
  return fmaxf(__uint_as_float(s[0]), __uint_as_float(s[1]));
}
__device__ __forceinline__ float swap32_max(float a) {
  auto s = __builtin_amdgcn_permlane32_swap(__float_as_uint(a), __float_as_uint(a), false, false);
  return fmaxf(__uint_as_float(s[0]), __uint_as_float(s[1]));
}
__device__ __forceinline__ float sum_hi3(float x) { x += dpp<ROR8>(x); x = swap16_add(x, x); return swap32_add(x, x); }
__device__ __forceinline__ float max_hi3(float x) { x = fmaxf(x, dpp<ROR8>(x)); x = swap16_max(x); return swap32_max(x); }
__device__ __forceinline__ float sum_lo3(float x) { x += dpp<XOR1>(x); x += dpp<XOR2>(x); x += dpp<HMIR>(x); return x; }
__device__ __forceinline__ float max_lo3(float x) { x = fmaxf(x, dpp<XOR1>(x)); x = fmaxf(x, dpp<XOR2>(x)); x = fmaxf(x, dpp<HMIR>(x)); return x; }
__device__ __forceinline__ float treduce4(float a0, float a1, float a2, float a3) {
  return swap32_add(swap16_add(a0, a2), swap16_add(a1, a3));
}
__device__ __forceinline__ void lds_arrive(u32* c, int l) {
  asm volatile("s_waitcnt lgkmcnt(0)" ::: "memory");
  if (l == 0) __hip_atomic_fetch_add((LAS u32*)c, 1u, __ATOMIC_RELAXED, __HIP_MEMORY_SCOPE_WORKGROUP);
  asm volatile("" ::: "memory");
}
__device__ __forceinline__ u32 lds_ticket(u32* c, int l) {
  asm volatile("s_waitcnt lgkmcnt(0)" ::: "memory");
  u32 t = 0;
  if (l == 0) t = __hip_atomic_fetch_add((LAS u32*)c, 1u, __ATOMIC_RELAXED, __HIP_MEMORY_SCOPE_WORKGROUP);
  t = __builtin_amdgcn_readfirstlane(t);
  asm volatile("" ::: "memory");
  return t;
}
__device__ __forceinline__ void lds_wait(u32* c, u32 n) {
  asm volatile("" ::: "memory");
  while (__builtin_amdgcn_readfirstlane(__hip_atomic_load((LAS u32*)c, __ATOMIC_RELAXED, __HIP_MEMORY_SCOPE_WORKGROUP)) < n) __builtin_amdgcn_s_sleep(1);
  asm volatile("" ::: "memory");
}
#define SCHED() __builtin_amdgcn_sched_barrier(0)
__device__ __forceinline__ void wg_barrier() {
  asm volatile("s_waitcnt lgkmcnt(0)" ::: "memory");
  __builtin_amdgcn_s_barrier();
  asm volatile("" ::: "memory");
}
#define STR2(x) #x
#define STR(x) STR2(x)
#define PACE() asm volatile("s_waitcnt vmcnt(" STR(PD) ")" ::: "memory")

enum { C_S1 = 0, C_PROG, C_Q, C_EPOCH };

__global__ void __launch_bounds__(768, 3)
fused_decode_kernel(const u16* __restrict__ x, const char* __restrict__ kc, const char* __restrict__ vc,
                    const char* __restrict__ wq, const char* __restrict__ wo, const char* __restrict__ w1,
                    const char* __restrict__ w3, const char* __restrict__ w2, u16* __restrict__ out, char* ws) {
  __shared__ __attribute__((aligned(16))) float lds[640 + 96 + 32 + 64 + 24 * 64 + 16 * 192 + 16 * 128];
  u32* xn_s = (u32*)lds;
  u32* q_s = (u32*)(lds + 640);
  float* m_s = lds + 736;
  float* l_s = lds + 744;
  u32* cnt = (u32*)(lds + 752);
  u32* h_s = (u32*)(lds + 768);
  float* s1_s = lds + 832;
  float* op_s = s1_s + 24 * 64;
  float* yp_s = op_s + 16 * 192;
  u32 t0 = (u32)__builtin_amdgcn_s_memrealtime();
  asm volatile("" : "+s"(t0));
  const int b = blockIdx.x;
  const int g = (b & 7) * 4 + (b >> 6), s = (b >> 3) & 7;
  const int tid = threadIdx.x;
  const int w = __builtin_amdgcn_readfirstlane(tid >> 6), l = tid & 63;
  const int r = l >> 3, j = l & 7;
  const __amdgpu_buffer_rsrc_t wsr = mkrsrc(ws, WS_END);
  const u32 grec = WS_REC + g * NS * REC, myrec = grec + s * REC;
  const bool is_red = (s == (g >> 2)) && !(g & 1);
  const bool red_b = (g & 2) != 0;
  const int hh = l >> 5;
  const bool useful = ((l >> 3) & 1) == hh;
  const int dcol = 8 * j + 4 * ((l >> 4) & 1);
#define MAKE_MSK() u32x4 msk; _Pragma("unroll") for (int d = 0; d < 4; ++d) msk[d] = (j >> 1) == d ? ((j & 1) ? 0xffff0000u : 0x0000ffffu) : 0u

  if (w >= 4) {
    const int sw = w - 4;
    const size_t kvbase = ((size_t)g * SK + (SK - WIN)) * (DH * 2);
    const __amdgpu_buffer_rsrc_t rk = mkrsrc(kc + kvbase, WIN * DH * 2), rv = mkrsrc(vc + kvbase, WIN * DH * 2);
    const u32 ll = (u32)(sw * 3072 + r * 384 + j * 16);
    const int rot = s + (g & 7) * 8;
#define UOFF(u) ((((u) * 8 + rot) & 63) * 24576)
    u32x4 K[8][3], V[8][3];
    const u32 tz = t0 + TB + sw * TU / 8;
#define GATE(n) do { while ((int)((u32)__builtin_amdgcn_s_memrealtime() - (tz + (n) * TU)) < 0) __builtin_amdgcn_s_sleep(1); asm volatile("" ::: "memory"); } while (0)
#define LOAD1(dst, rs, u, c) do { PACE(); dst[u][c] = __builtin_amdgcn_raw_buffer_load_b128(rs, ll + (c) * 128, UOFF(u), NT); } while (0)
#define LOAD3(dst, rs, u) do { GATE(8 + (u)); LOAD1(dst, rs, u, 0); LOAD1(dst, rs, u, 1); LOAD1(dst, rs, u, 2); SCHED(); } while (0)
    wg_barrier();
#pragma unroll
    for (int i = 0; i < 24; ++i) { if (i % 3 == 0 && i) GATE(i / 3); LOAD1(K, rk, i / 3, i % 3); if (i == GPOS * 3 + 2) lds_arrive(cnt + C_PROG, l); }
    SCHED();
    LOAD3(V, rv, 0); LOAD3(V, rv, 1); LOAD3(V, rv, 2);
    lds_wait(cnt + C_Q, 1);
    MAKE_MSK();
    u32 ppk[8];
    {
      u32x4 q[3];
      float p[8];
#pragma unroll
      for (int c = 0; c < 3; ++c) q[c] = *reinterpret_cast<const u32x4*>(q_s + c * 32 + j * 4);
#define QK(u) do { float d = 0.f; _Pragma("unroll") for (int c = 0; c < 3; ++c) _Pragma("unroll") for (int k = 0; k < 4; ++k) d = dot2(K[u][c][k], q[c][k], d); \
      p[u] = rbf(rbf(sum_lo3(d)) * 0.07216878364870322f); } while (0)
#define QK2(u) do { QK(u); QK(u + 1); asm volatile("" : "+v"(p[u]), "+v"(p[u + 1])); SCHED(); } while (0)
      QK2(0); LOAD3(V, rv, 3);
      QK2(2); LOAD3(V, rv, 4);
      QK2(4); LOAD3(V, rv, 5);
      QK2(6);
      const float mw = max_hi3(fmaxf(fmaxf(fmaxf(p[0], p[1]), fmaxf(p[2], p[3])), fmaxf(fmaxf(p[4], p[5]), fmaxf(p[6], p[7]))));
      float ls = 0.f;
#pragma unroll
      for (int u = 0; u < 8; ++u) { const float e = __builtin_amdgcn_exp2f((p[u] - mw) * LOG2E); ls += e; ppk[u] = pkbf(e); }
      ls = sum_hi3(ls);
      if (l == 0) { m_s[sw] = mw; l_s[sw] = ls; }
    }
    f32x4 acc[3];
#pragma unroll
    for (int c = 0; c < 3; ++c) acc[c] = f32x4{0.f, 0.f, 0.f, 0.f};
#define PV(u) do { const u32x4 a_ = msk & ppk[u]; _Pragma("unroll") for (int c = 0; c < 3; ++c) acc[c] = mfma(a_, V[u][c], acc[c]); \
      asm volatile("" : "+v"(acc[0]), "+v"(acc[1]), "+v"(acc[2])); SCHED(); } while (0)
    LOAD3(V, rv, 6);
    PV(0); PV(1); PV(2); PV(3);
    LOAD3(V, rv, 7);
    u32x4 WO[5];
    const int ocol = red_b ? 512 + s * 16 + 4 * (sw & 3) : s * 64 + 8 * sw + 4 * (l & 1);
    const int ok2 = 2 * ((l >> 4) & 1) + hh;
    u32 xraw;
    {
      const __amdgpu_buffer_rsrc_t rw = mkrsrc((sw < 6) ? wo + (size_t)(g * DH + sw * 32) * (DM * 2) : w2 + (size_t)(g * 48 + (sw - 6) * 32) * (DM * 2), 32 * DM * 2);
      const u32 wl = ((sw == 7) ? (r & 3) : r) * (4 * DM * 2) + j * 16;
#define WLD(i) WO[i] = __builtin_amdgcn_raw_buffer_load_b128(rw, wl + (i) * (DM * 2), s * 128, NT)
#define WLD4() do { WO[4] = __builtin_amdgcn_raw_buffer_load_b128(rw, wl, (s & 3) * (DM * 2) + (8 + (s >> 2)) * 128, NT); xraw = x[ocol + ok2]; } while (0)
      PV(4); PV(5); PV(6);
      GATE(16);
      WLD(0); WLD(1); WLD(2);
      SCHED();
      PV(7);
      if (useful) {
#pragma unroll
        for (int c = 0; c < 3; ++c) *reinterpret_cast<f32x4*>(op_s + (sw * 2 + hh) * 192 + c * 64 + dcol) = acc[c];
      }
      wg_barrier();
      WLD(3); WLD4();
      SCHED();
    }
    u32 opk[4];
    if (sw < 6) {
      const u32 goff = grec + j * REC + R_G2 + sw * 128 + r * 16;
      const u32 soff = grec + j * REC + R_ST;
      u32x2 SS;
      u32x4 O;
      __builtin_amdgcn_s_sleep(D1);
      bool ok = false;
      u32 spins = 0;
      for (;;) {
        asm volatile("" ::: "memory");
        if (!ok) {
          SS = __builtin_amdgcn_raw_buffer_load_b64(wsr, soff, 0, SC1);
          O = __builtin_amdgcn_raw_buffer_load_b128(wsr, goff, 0, SC1);
          ok = (O[0] & O[1] & O[2] & O[3] & SS[0] & SS[1] & 1) != 0;
        }
        if (__all(ok) || ++spins > SPIN_CAP) break;
        __builtin_amdgcn_s_sleep(2);
      }
      const float ms = __uint_as_float(SS[0] & ~1u);
      const float f = __builtin_amdgcn_exp2f((ms - max_lo3(ms)) * LOG2E);
      const float sc = f * __builtin_amdgcn_rcpf(sum_lo3(f * __uint_as_float(SS[1])));
#pragma unroll
      for (int i = 0; i < 4; ++i) opk[i] = pkbf(sum_lo3(sc * __uint_as_float(O[i])));
    } else {
      const u32x4 hq = *reinterpret_cast<const u32x4*>(h_s + (sw - 6) * 32 + 4 * r);
#pragma unroll
      for (int i = 0; i < 4; ++i) opk[i] = hq[i];
    }
    {
      f32x4 y1 = {0.f, 0.f, 0.f, 0.f}, y2 = y1;
      const u32 o2 = (s & 2) ? ((s & 1) ? opk[3] : opk[2]) : ((s & 1) ? opk[1] : opk[0]);
      y2 = mfma(msk & o2, WO[4], y2);
#pragma unroll
      for (int i = 0; i < 4; ++i) y1 = mfma(msk & opk[i], WO[i], y1);
      if (useful) {
        *reinterpret_cast<f32x4*>(yp_s + (sw * 2 + hh) * 128 + dcol) = y1;
        *reinterpret_cast<f32x4*>(yp_s + (sw * 2 + hh) * 128 + 64 + dcol) = y2;
      }
    }
    wg_barrier();
    if (is_red && !(red_b && sw >= 4)) {
      const u32 goff = red_b ? WS_REC + ((l & 31) * NS + (s & 4) + hh) * REC + R_G3 + 256 + (s & 3) * 64 + sw * 16
                             : WS_REC + ((l >> 1) * NS + s) * REC + R_G3 + sw * 32 + (l & 1) * 16;
      u32x4 Y, Z;
      __builtin_amdgcn_s_sleep(D2);
      bool ok = false;
      u32 spins = 0;
      for (;;) {
        asm volatile("" ::: "memory");
        if (!ok) {
          Y = __builtin_amdgcn_raw_buffer_load_b128(wsr, goff, 0, SC1);
          Z = __builtin_amdgcn_raw_buffer_load_b128(wsr, red_b ? goff + 2 * REC : OOB, 0, SC1);
          ok = (Y[0] & Y[1] & Y[2] & Y[3] & (red_b ? Z[0] & Z[1] & Z[2] & Z[3] : 1u) & 1) != 0;
        }
        if (__all(ok) || ++spins > SPIN_CAP) break;
        __builtin_amdgcn_s_sleep(4);
      }
      const f32x4 yz = __builtin_bit_cast(f32x4, Y) + __builtin_bit_cast(f32x4, Z);
      float t = treduce4(yz[0], yz[1], yz[2], yz[3]);
      t += dpp<ROR8>(t); t += dpp<ROR4>(t); t += dpp<XOR2>(t);
      const float t1 = t + dpp<XOR1>(t);
      if ((l & (red_b ? 15 : 14)) == 0) out[ocol + ok2] = __builtin_bit_cast(u16, (__bf16)(__uint_as_float(xraw << 16) + (red_b ? t1 : t)));
      if (b == 0 && sw == 0) __builtin_amdgcn_raw_buffer_store_b32(cnt[C_EPOCH], wsr, l == 0 ? WS_EPOCH : OOB, 0, SC1);
    }
  } else {
    const int qd = g >> 2, wi = (g & 3) * 8 + s;
    u32x4 S1[12], xa;
    u32 xb;
    int row0[3];
#pragma unroll
    for (int k = 0; k < 3; ++k) {
      const int z = 9 * wi + (k < 2 ? 2 * w + ((wi & 1) ^ k) : 8), ln = z >> 4;
      row0[k] = (z & 15) * 40 + (k == 2 ? w * 8 : 0);
      const bool isq = ln < 12;
      const u32 rs = isq ? NH * DH * 2 : FF * 2;
      const u32 so = row0[k] * rs + (isq ? qd * 1536 + ln * 128 : qd * 384 + (ln - (ln < 15 ? 12 : 15)) * 128);
      const __amdgpu_buffer_rsrc_t rr = mkrsrc(isq ? wq : ln < 15 ? w1 : w3, DM * rs);
      const u32 vo = r * rs + j * 16;
      if (k < 2) {
#pragma unroll
        for (int i = 0; i < 5; ++i) S1[5 * k + i] = __builtin_amdgcn_raw_buffer_load_b128(rr, vo, so + 8 * i * rs, NT);
      } else {
        S1[10] = __builtin_amdgcn_raw_buffer_load_b128(rr, vo, so, NT);
        S1[11] = __builtin_amdgcn_raw_buffer_load_b128(rr, w == 0 ? vo + so + 32 * rs : OOB, 0, NT);
      }
      if (k == 0) {
        const __amdgpu_buffer_rsrc_t rx = mkrsrc(x, DM * 2);
        xa = __builtin_amdgcn_raw_buffer_load_b128(rx, l * 16, 0, 0);
        xb = __builtin_amdgcn_raw_buffer_load_b32(rx, l * 4, 1024, 0);
      }
      SCHED();
    }
    const u32 epoch = __builtin_amdgcn_raw_buffer_load_b32(wsr, WS_EPOCH, 0, SC1);
    {
      const u32x4 z = {0u, 0u, 0u, 0u};
      __builtin_amdgcn_raw_buffer_store_b128(z, wsr, (w == 3 && l < 49) ? myrec + R_G2 + l * 16 : (w == 2 && l < 32) ? myrec + R_G3 + l * 16 : OOB, 0, SC1);
    }
    if (tid < 8) cnt[tid] = 0;
    SCHED();
    wg_barrier();
    SCHED();
    {
      float xv[10];
#pragma unroll
      for (int k = 0; k < 4; ++k) { xv[2 * k] = bf_lo(xa[k]); xv[2 * k + 1] = bf_hi(xa[k]); }
      xv[8] = bf_lo(xb); xv[9] = bf_hi(xb);
      float sm = 0.f;
#pragma unroll
      for (int k = 0; k < 10; ++k) sm += xv[k];
      sm = sum_hi3(sum_lo3(sm));
      const float mean = sm * (1.f / DM);
      float sv = 0.f;
#pragma unroll
      for (int k = 0; k < 10; ++k) { const float d = xv[k] - mean; sv = fmaf(d, d, sv); }
      sv = sum_hi3(sum_lo3(sv));
      const float var = rbf(sv * (1.f / DM));
      const float mb = rbf(mean);
      const float sd = rbf(sqrtf(rbf(var + 1e-5f)));
      u32 xp[10];
#pragma unroll
      for (int k = 0; k < 10; ++k) xp[k] = pkbf(rbf(xv[k] - mb) / sd);
      *reinterpret_cast<u32x4*>(xn_s + l * 8) = u32x4{xp[0], xp[1], xp[2], xp[3]};
      *reinterpret_cast<u32x4*>(xn_s + l * 8 + 4) = u32x4{xp[4], xp[5], xp[6], xp[7]};
      *reinterpret_cast<u32x2*>(xn_s + 512 + l * 2) = u32x2{xp[8], xp[9]};
      asm volatile("s_waitcnt lgkmcnt(0)" ::: "memory");
    }
    MAKE_MSK();
    const u32 tag = epoch + 1;
    {
      u32 xr[12];
#pragma unroll
      for (int i = 0; i < 10; ++i) xr[i] = xn_s[row0[i / 5] + 8 * (i % 5) + r];
      xr[10] = xn_s[row0[2] + r];
      xr[11] = w == 0 ? xn_s[row0[2] + 32 + r] : 0u;
      f32x4 a[3];
#pragma unroll
      for (int k = 0; k < 3; ++k) a[k] = f32x4{0.f, 0.f, 0.f, 0.f};
#pragma unroll
      for (int i = 0; i < 12; ++i) a[i / 5] = mfma(msk & xr[i], S1[i], a[i / 5]);
      if (useful) {
#pragma unroll
        for (int k = 0; k < 3; ++k) *reinterpret_cast<f32x4*>(s1_s + (k < 2 ? 4 * w + 2 * ((wi & 1) ^ k) + hh : 16 + w * 2 + hh) * 64 + dcol) = a[k];
      }
      asm volatile("s_waitcnt vmcnt(0)" ::: "memory");
      if (lds_ticket(cnt + C_S1, l) == 3) {
        const int nA = 16 - ((9 * wi) & 15);
        float va = 0.f, vb = 0.f;
#pragma unroll
        for (int c = 0; c < 8; ++c) {
          const float v = s1_s[(2 * c) * 64 + l] + s1_s[(2 * c + 1) * 64 + l];
          if (c < nA) va += v; else vb += v;
        }
        float v8 = 0.f;
#pragma unroll
        for (int t = 16; t < 24; ++t) v8 += s1_s[t * 64 + l];
        if (8 < nA) va += v8; else vb += v8;
        __builtin_amdgcn_raw_buffer_store_b64(u32x2{__float_as_uint(va), tag}, wsr, myrec + R_G1 + l * 8, 0, SC1);
        __builtin_amdgcn_raw_buffer_store_b64(u32x2{__float_as_uint(vb), tag}, wsr, nA <= 8 ? myrec + R_G1 + 512 + l * 8 : OOB, 0, SC1);
      }
    }
    {
      u32 ga[10];
      bool use3[3];
#pragma unroll
      for (int k = 0; k < 3; ++k) {
        const int fc = (g & 3) * 48 + min(l, 47);
        const int ln = w == 0 ? (g & 3) * 3 + k : 12 + 3 * min(k, 1) + (fc >> 6);
        const int col = w == 0 ? l : (fc & 63);
        const int f0 = (16 * ln) / 9, f1 = (16 * ln + 15) / 9;
        use3[k] = f0 + 2 <= f1;
#pragma unroll
        for (int i = 0; i < 3; ++i) {
          const int pw = min(f0 + i, f1);
          ga[k * 3 + i] = WS_REC + (qd * 32 + pw) * REC + R_G1 + (9 * pw < 16 * ln ? 512 : 0) + col * 8;
        }
      }
      ga[9] = grec + j * REC + R_G1;
      if (w == 1) {
        ga[9] = WS_REC + ((l & 31) * NS + (red_b ? (s & 4) + hh : s)) * REC + R_G1;
        ga[8] = ga[9] + (red_b ? 2 * REC : 0);
      }
      lds_wait(cnt + C_PROG, 8);
      if (w != 2 && (w != 1 || is_red)) {
        u32x2 G[10];
        bool ok = false;
        u32 spins = 0;
        for (;;) {
          asm volatile("" ::: "memory");
          if (!ok) {
#pragma unroll
            for (int i = 0; i < 10; ++i) if (w == 0 || (w == 3 && i < 6) || (w == 1 && i >= 8)) G[i] = __builtin_amdgcn_raw_buffer_load_b64(wsr, ga[i], 0, SC1);
            u32 d_ = 0;
#pragma unroll
            for (int i = 0; i < 10; ++i) if (w == 0 || (w == 3 && i < 6) || (w == 1 && i >= 8)) d_ |= G[i][1] ^ tag;
            ok = (d_ == 0);
          }
          if (__all(ok) || ++spins > SPIN_CAP) break;
          __builtin_amdgcn_s_sleep(8);
        }
        float t[3];
#pragma unroll
        for (int k = 0; k < 3; ++k) t[k] = __uint_as_float(G[3 * k][0]) + __uint_as_float(G[3 * k + 1][0]) + (use3[k] ? __uint_as_float(G[3 * k + 2][0]) : 0.f);
        if (w == 0) {
#pragma unroll
          for (int k = 0; k < 3; ++k) ((u16*)q_s)[64 * k + l] = __builtin_bit_cast(u16, (__bf16)t[k]);
          if (l == 0) cnt[C_EPOCH] = tag;
          lds_arrive(cnt + C_Q, l);
        } else if (w == 3) {
          const float gate = rbf(t[0]), up = rbf(t[1]);
          const float hv = rbf(gate / (1.f + __expf(-gate))) * up;
          h_s[l] = l < 48 ? pkbf(hv) : 0u;
        }
      }
    }
    __builtin_amdgcn_s_setprio(3);
    wg_barrier();
    {
      const int col = min(tid, 191);
      float pv[16];
#pragma unroll
      for (int i = 0; i < 16; ++i) pv[i] = op_s[i * 192 + col];
      const float mj = m_s[j];
      const float M = max_lo3(mj);
      const float fj = __builtin_amdgcn_exp2f((mj - M) * LOG2E);
      float o = 0.f;
      if (w == 3) {
        const float L = sum_lo3(l_s[j] * fj);
        o = (l & 1) ? L : M;
      } else {
#pragma unroll
        for (int i = 0; i < 8; ++i) o = fmaf(__uint_as_float(__builtin_amdgcn_readlane(__float_as_uint(fj), i)), pv[2 * i] + pv[2 * i + 1], o);
      }
      __builtin_amdgcn_raw_buffer_store_b32(__float_as_uint(o) | 1u, wsr, tid < 194 ? myrec + R_G2 + tid * 4 : OOB, 0, SC1);
    }
    wg_barrier();
    if (w < 2) {
      float y = 0.f;
#pragma unroll
      for (int i = 0; i < 16; ++i) y += yp_s[i * 128 + tid];
      __builtin_amdgcn_raw_buffer_store_b32(__float_as_uint(y) | 1u, wsr, myrec + R_G3 + tid * 4, 0, SC1);
    }
  }
}

extern "C" void kernel_launch(void* const* d_in, const int* in_sizes, int n_in, void* d_out, int out_size, void* d_ws,
                              size_t ws_size, hipStream_t stream) {
  fused_decode_kernel<<<256, 768, 0, stream>>>((const u16*)d_in[0], (const char*)d_in[1], (const char*)d_in[2],
                                               (const char*)d_in[3], (const char*)d_in[4], (const char*)d_in[6],
                                               (const char*)d_in[7], (const char*)d_in[8], (u16*)d_out, (char*)d_ws);
}
```

```cpp
#define TU 86
#define TB 185
#include <hip/hip_runtime.h>
#include <hip/hip_bf16.h>
#include <stdint.h>

typedef unsigned short u16;
typedef unsigned int u32;
typedef unsigned long long u64;
typedef u32 u32x2 __attribute__((ext_vector_type(2)));
typedef u32 u32x4 __attribute__((ext_vector_type(4)));
typedef float f32x4 __attribute__((ext_vector_type(4)));
typedef __attribute__((ext_vector_type(2))) __bf16 bf16x2;
typedef __attribute__((ext_vector_type(8))) __bf16 bf16x8;
#define LAS __attribute__((address_space(3)))

#define D1 24
#define D2 12
#define PHS 4
#define WGA TU
#define PD 7
#define GPOS 5
#define DIAG 0

constexpr int DM = 640, NH = 32, DH = 192, SK = 32768, WIN = 4096, FF = 1536;
constexpr int NS = 8;
constexpr u32 WS_EPOCH = 0;
constexpr u32 WS_REC = 4096, REC = 2560;
constexpr u32 R_G1 = 0;
constexpr u32 R_G2 = 1024;
constexpr u32 R_ST = R_G2 + 768;
constexpr u32 R_G3 = 1920;
constexpr u32 WS_END = WS_REC + NH * NS * REC;
constexpr int WS_STAMP = 1 << 19;
constexpr int NWAVE = 12, NHELP = 4;
constexpr u32 SPIN_CAP = 1u << 19;
constexpr float LOG2E = 1.4426950408889634f;
constexpr u32 OOB = 0xfffffff0u;
constexpr int NT = 2, SC1 = 16;

__device__ __forceinline__ float bf_lo(u32 v) { return __uint_as_float(v << 16); }
__device__ __forceinline__ float bf_hi(u32 v) { return __uint_as_float(v & 0xffff0000u); }
__device__ __forceinline__ float rbf(float f) { return (float)(__bf16)f; }
__device__ __forceinline__ u32 pkbf(float f) { const bf16x2 h = {(__bf16)f, (__bf16)f}; return __builtin_bit_cast(u32, h); }
__device__ __forceinline__ float dot2(u32 a, u32 b, float c) {
  return __builtin_amdgcn_fdot2_f32_bf16(__builtin_bit_cast(bf16x2, a), __builtin_bit_cast(bf16x2, b), c, false);
}
__device__ __forceinline__ f32x4 mfma(u32x4 a, u32x4 b, f32x4 c) {
  return __builtin_amdgcn_mfma_f32_16x16x32_bf16(__builtin_bit_cast(bf16x8, a), __builtin_bit_cast(bf16x8, b), c, 0, 0, 0);
}
__device__ __forceinline__ __amdgpu_buffer_rsrc_t mkrsrc(const void* p, u32 bytes) {
  return __builtin_amdgcn_make_buffer_rsrc(const_cast<void*>(p), 0, bytes, 0x00020000);
}
template <int CTRL> __device__ __forceinline__ float dpp(float x) {
  return __builtin_bit_cast(float, __builtin_amdgcn_mov_dpp(__builtin_bit_cast(int, x), CTRL, 0xf, 0xf, true));
}
constexpr int XOR1 = 0xB1, XOR2 = 0x4E, HMIR = 0x141, ROR8 = 0x128, ROR4 = 0x124;
__device__ __forceinline__ float swap16_add(float a, float b) {
  auto s = __builtin_amdgcn_permlane16_swap(__float_as_uint(a), __float_as_uint(b), false, false);
  return __uint_as_float(s[0]) + __uint_as_float(s[1]);
}
__device__ __forceinline__ float swap32_add(float a, float b) {
  auto s = __builtin_amdgcn_permlane32_swap(__float_as_uint(a), __float_as_uint(b), false, false);
  return __uint_as_float(s[0]) + __uint_as_float(s[1]);
}
__device__ __forceinline__ float swap16_max(float a) {
  auto s = __builtin_amdgcn_permlane16_swap(__float_as_uint(a), __float_as_uint(a), false, false);
  return fmaxf(__uint_as_float(s[0]), __uint_as_float(s[1]));
}
__device__ __forceinline__ float swap32_max(float a) {
  auto s = __builtin_amdgcn_permlane32_swap(__float_as_uint(a), __float_as_uint(a), false, false);
  return fmaxf(__uint_as_float(s[0]), __uint_as_float(s[1]));
}
__device__ __forceinline__ float sum_hi3(float x) { x += dpp<ROR8>(x); x = swap16_add(x, x); return swap32_add(x, x); }
__device__ __forceinline__ float max_hi3(float x) { x = fmaxf(x, dpp<ROR8>(x)); x = swap16_max(x); return swap32_max(x); }
__device__ __forceinline__ float sum_lo3(float x) { x += dpp<XOR1>(x); x += dpp<XOR2>(x); x += dpp<HMIR>(x); return x; }
__device__ __forceinline__ float max_lo3(float x) { x = fmaxf(x, dpp<XOR1>(x)); x = fmaxf(x, dpp<XOR2>(x)); x = fmaxf(x, dpp<HMIR>(x)); return x; }
__device__ __forceinline__ float treduce4(float a0, float a1, float a2, float a3) {
  return swap32_add(swap16_add(a0, a2), swap16_add(a1, a3));
}
__device__ __forceinline__ void lds_arrive(u32* c, int l) {
  asm volatile("s_waitcnt lgkmcnt(0)" ::: "memory");
  if (l == 0) __hip_atomic_fetch_add((LAS u32*)c, 1u, __ATOMIC_RELAXED, __HIP_MEMORY_SCOPE_WORKGROUP);
  asm volatile("" ::: "memory");
}
__device__ __forceinline__ u32 lds_ticket(u32* c, int l) {
  asm volatile("s_waitcnt lgkmcnt(0)" ::: "memory");
  u32 t = 0;
  if (l == 0) t = __hip_atomic_fetch_add((LAS u32*)c, 1u, __ATOMIC_RELAXED, __HIP_MEMORY_SCOPE_WORKGROUP);
  t = __builtin_amdgcn_readfirstlane(t);
  asm volatile("" ::: "memory");
  return t;
}
__device__ __forceinline__ void lds_wait(u32* c, u32 n) {
  asm volatile("" ::: "memory");
  while (__builtin_amdgcn_readfirstlane(__hip_atomic_load((LAS u32*)c, __ATOMIC_RELAXED, __HIP_MEMORY_SCOPE_WORKGROUP)) < n) __builtin_amdgcn_s_sleep(1);
  asm volatile("" ::: "memory");
}
#define SCHED() __builtin_amdgcn_sched_barrier(0)
__device__ __forceinline__ void wg_barrier() {
  asm volatile("s_waitcnt lgkmcnt(0)" ::: "memory");
  __builtin_amdgcn_s_barrier();
  asm volatile("" ::: "memory");
}
#define STAMP(i)
#define RETRY(n)
#define STR2(x) #x
#define STR(x) STR2(x)
#define PACE() asm volatile("s_waitcnt vmcnt(" STR(PD) ")" ::: "memory")

enum { C_S1 = 0, C_PROG, C_Q, C_EPOCH };

__global__ void __launch_bounds__(768, 3)
fused_decode_kernel(const u16* __restrict__ x, const char* __restrict__ kc, const char* __restrict__ vc,
                    const char* __restrict__ wq, const char* __restrict__ wo, const char* __restrict__ w1,
                    const char* __restrict__ w3, const char* __restrict__ w2, u16* __restrict__ out, char* ws) {
  __shared__ __attribute__((aligned(16))) float lds[640 + 96 + 32 + 64 + 24 * 64 + 16 * 192 + 16 * 128];
  u32* xn_s = (u32*)lds;
  u32* q_s = (u32*)(lds + 640);
  float* m_s = lds + 736;
  float* l_s = lds + 744;
  u32* cnt = (u32*)(lds + 752);
  u32* h_s = (u32*)(lds + 768);
  float* s1_s = lds + 832;
  float* op_s = s1_s + 24 * 64;
  float* yp_s = op_s + 16 * 192;
  STAMP(0);
  u32 t0 = (u32)__builtin_amdgcn_s_memrealtime();
  asm volatile("" : "+s"(t0));
  const int b = blockIdx.x;
  const int g = (b & 7) * 4 + (b >> 6), s = (b >> 3) & 7;
  const int tid = threadIdx.x;
  const int w = __builtin_amdgcn_readfirstlane(tid >> 6), l = tid & 63;
  const int r = l >> 3, j = l & 7;
  const __amdgpu_buffer_rsrc_t wsr = mkrsrc(ws, WS_END);
  const u32 grec = WS_REC + g * NS * REC, myrec = grec + s * REC;
  const bool is_red = (s == (g >> 2)) && !(g & 1);
  const bool red_b = (g & 2) != 0;
  const int hh = l >> 5;
  const bool useful = ((l >> 3) & 1) == hh;
  const int dcol = 8 * j + 4 * ((l >> 4) & 1);
#define MAKE_MSK() u32x4 msk; _Pragma("unroll") for (int d = 0; d < 4; ++d) msk[d] = (j >> 1) == d ? ((j & 1) ? 0xffff0000u : 0x0000ffffu) : 0u

  if (w >= 4) {
    const int sw = w - 4;
    const size_t kvbase = ((size_t)g * SK + (SK - WIN)) * (DH * 2);
    const __amdgpu_buffer_rsrc_t rk = mkrsrc(kc + kvbase, WIN * DH * 2), rv = mkrsrc(vc + kvbase, WIN * DH * 2);
    const u32 ll = (u32)((sw * 8 + s) * 3072 + r * 384 + j * 16);
#define UOFF(u) ((((u) + g) & 7) * 196608)
    u32x4 K[8][3], V[8][3];
    const u32 tz = t0 + TB + ((g & (PHS - 1)) * 8 + sw) * TU / (8 * PHS);
#define GATE(n) GATET((n) * TU)
#define GATET(t) do { while ((int)((u32)__builtin_amdgcn_s_memrealtime() - (tz + (t))) < 0) __builtin_amdgcn_s_sleep(1); asm volatile("" ::: "memory"); } while (0)
#define LOAD1(dst, rs, u, c) do { PACE(); dst[u][c] = __builtin_amdgcn_raw_buffer_load_b128(rs, ll + (c) * 128, UOFF(u), NT); } while (0)
#define LOAD3(dst, rs, u) do { GATE(8 + (u)); LOAD1(dst, rs, u, 0); LOAD1(dst, rs, u, 1); LOAD1(dst, rs, u, 2); SCHED(); } while (0)
    wg_barrier();
#pragma unroll
    for (int i = 0; i < 24; ++i) { if (i % 3 == 0 && i) GATE(i / 3); LOAD1(K, rk, i / 3, i % 3); if (i == GPOS * 3 + 2) lds_arrive(cnt + C_PROG, l); }
    SCHED();
    STAMP(1);
    LOAD3(V, rv, 0); LOAD3(V, rv, 1); LOAD3(V, rv, 2);
    STAMP(2);
    lds_wait(cnt + C_Q, 1);
    STAMP(3);
    MAKE_MSK();
    u32 ppk[8];
    {
      u32x4 q[3];
      float p[8];
#pragma unroll
      for (int c = 0; c < 3; ++c) q[c] = *reinterpret_cast<const u32x4*>(q_s + c * 32 + j * 4);
#define QK(u) do { float d = 0.f; _Pragma("unroll") for (int c = 0; c < 3; ++c) _Pragma("unroll") for (int k = 0; k < 4; ++k) d = dot2(K[u][c][k], q[c][k], d); \
      p[u] = rbf(rbf(sum_lo3(d)) * 0.07216878364870322f); } while (0)
#define QK2(u) do { QK(u); QK(u + 1); asm volatile("" : "+v"(p[u]), "+v"(p[u + 1])); SCHED(); } while (0)
      QK2(0); LOAD3(V, rv, 3);
      QK2(2); LOAD3(V, rv, 4);
      QK2(4); LOAD3(V, rv, 5);
      QK2(6);
      const float mw = max_hi3(fmaxf(fmaxf(fmaxf(p[0], p[1]), fmaxf(p[2], p[3])), fmaxf(fmaxf(p[4], p[5]), fmaxf(p[6], p[7]))));
      float ls = 0.f;
#pragma unroll
      for (int u = 0; u < 8; ++u) { const float e = __builtin_amdgcn_exp2f((p[u] - mw) * LOG2E); ls += e; ppk[u] = pkbf(e); }
      ls = sum_hi3(ls);
      if (l == 0) { m_s[sw] = mw; l_s[sw] = ls; }
    }
    STAMP(4);
    f32x4 acc[3];
#pragma unroll
    for (int c = 0; c < 3; ++c) acc[c] = f32x4{0.f, 0.f, 0.f, 0.f};
#define PV(u) do { const u32x4 a_ = msk & ppk[u]; _Pragma("unroll") for (int c = 0; c < 3; ++c) acc[c] = mfma(a_, V[u][c], acc[c]); \
      asm volatile("" : "+v"(acc[0]), "+v"(acc[1]), "+v"(acc[2])); SCHED(); } while (0)
    LOAD3(V, rv, 6);
    PV(0); PV(1); PV(2); PV(3);
    LOAD3(V, rv, 7);
    u32x4 WO[5];
    const int ocol = red_b ? 512 + s * 16 + 4 * (sw & 3) : s * 64 + 8 * sw + 4 * (l & 1);
    const int ok2 = 2 * ((l >> 4) & 1) + hh;
    u32 xraw;
    {
      const __amdgpu_buffer_rsrc_t rw = mkrsrc((sw < 6) ? wo + (size_t)(g * DH + sw * 32) * (DM * 2) : w2 + (size_t)(g * 48 + (sw - 6) * 32) * (DM * 2), 32 * DM * 2);
      const u32 wl = ((sw == 7) ? (r & 3) : r) * (4 * DM * 2) + j * 16;
#define WLD(i) WO[i] = __builtin_amdgcn_raw_buffer_load_b128(rw, wl + (i) * (DM * 2), s * 128, NT)
#define WLD4() do { WO[4] = __builtin_amdgcn_raw_buffer_load_b128(rw, wl, (s & 3) * (DM * 2) + (8 + (s >> 2)) * 128, NT); xraw = x[ocol + ok2]; } while (0)
      PV(4); PV(5); PV(6);
      GATET(15 * TU + WGA);
      WLD(0); WLD(1); WLD(2);
      SCHED();
    PV(7);
    STAMP(5);
    if (useful) {
#pragma unroll
      for (int c = 0; c < 3; ++c) *reinterpret_cast<f32x4*>(op_s + (sw * 2 + hh) * 192 + c * 64 + dcol) = acc[c];
    }
    wg_barrier();
    WLD(3); WLD4();
    SCHED();
    }
    STAMP(6);
    u32 opk[4];
    if (sw < 6) {
      const u32 goff = grec + j * REC + R_G2 + sw * 128 + r * 16;
      const u32 soff = grec + j * REC + R_ST;
      u32x2 SS;
      u32x4 O;
      __builtin_amdgcn_s_sleep(D1);
      bool ok = false;
      u32 spins = 0;
      for (;;) {
        asm volatile("" ::: "memory");
        if (!ok) {
          SS = __builtin_amdgcn_raw_buffer_load_b64(wsr, soff, 0, SC1);
          O = __builtin_amdgcn_raw_buffer_load_b128(wsr, goff, 0, SC1);
          ok = (O[0] & O[1] & O[2] & O[3] & SS[0] & SS[1] & 1) != 0;
        }
        if (__all(ok) || ++spins > SPIN_CAP) break;
        __builtin_amdgcn_s_sleep(2);
      }
      STAMP(7);
      const float ms = __uint_as_float(SS[0] & ~1u);
      const float f = __builtin_amdgcn_exp2f((ms - max_lo3(ms)) * LOG2E);
      const float sc = f * __builtin_amdgcn_rcpf(sum_lo3(f * __uint_as_float(SS[1])));
#pragma unroll
      for (int i = 0; i < 4; ++i) opk[i] = pkbf(sum_lo3(sc * __uint_as_float(O[i])));
    } else {
      const u32x4 hq = *reinterpret_cast<const u32x4*>(h_s + (sw - 6) * 32 + 4 * r);
#pragma unroll
      for (int i = 0; i < 4; ++i) opk[i] = hq[i];
    }
    {
      f32x4 y1 = {0.f, 0.f, 0.f, 0.f}, y2 = y1;
      const u32 o2 = (s & 2) ? ((s & 1) ? opk[3] : opk[2]) : ((s & 1) ? opk[1] : opk[0]);
      y2 = mfma(msk & o2, WO[4], y2);
#pragma unroll
      for (int i = 0; i < 4; ++i) y1 = mfma(msk & opk[i], WO[i], y1);
      if (useful) {
        *reinterpret_cast<f32x4*>(yp_s + (sw * 2 + hh) * 128 + dcol) = y1;
        *reinterpret_cast<f32x4*>(yp_s + (sw * 2 + hh) * 128 + 64 + dcol) = y2;
      }
    }
    wg_barrier();
    STAMP(8);
    if (is_red && !(red_b && sw >= 4)) {
      const u32 goff = red_b ? WS_REC + ((l & 31) * NS + (s & 4) + hh) * REC + R_G3 + 256 + (s & 3) * 64 + sw * 16
                             : WS_REC + ((l >> 1) * NS + s) * REC + R_G3 + sw * 32 + (l & 1) * 16;
      u32x4 Y, Z;
      __builtin_amdgcn_s_sleep(D2);
      bool ok = false;
      u32 spins = 0;
      for (;;) {
        asm volatile("" ::: "memory");
        if (!ok) {
          Y = __builtin_amdgcn_raw_buffer_load_b128(wsr, goff, 0, SC1);
          Z = __builtin_amdgcn_raw_buffer_load_b128(wsr, red_b ? goff + 2 * REC : OOB, 0, SC1);
          ok = (Y[0] & Y[1] & Y[2] & Y[3] & (red_b ? Z[0] & Z[1] & Z[2] & Z[3] : 1u) & 1) != 0;
        }
        if (__all(ok) || ++spins > SPIN_CAP) break;
        __builtin_amdgcn_s_sleep(4);
      }
      const f32x4 yz = __builtin_bit_cast(f32x4, Y) + __builtin_bit_cast(f32x4, Z);
      float t = treduce4(yz[0], yz[1], yz[2], yz[3]);
      t += dpp<ROR8>(t); t += dpp<ROR4>(t); t += dpp<XOR2>(t);
      const float t1 = t + dpp<XOR1>(t);
      if ((l & (red_b ? 15 : 14)) == 0) out[ocol + ok2] = __builtin_bit_cast(u16, (__bf16)(__uint_as_float(xraw << 16) + (red_b ? t1 : t)));
      if (b == 0 && sw == 0) __builtin_amdgcn_raw_buffer_store_b32(cnt[C_EPOCH], wsr, l == 0 ? WS_EPOCH : OOB, 0, SC1);
    }
  } else {
    const int qd = g >> 2, wi = (g & 3) * 8 + s;
    u32x4 S1[12], xa;
    u32 xb;
    int row0[3];
#pragma unroll
    for (int k = 0; k < 3; ++k) {
      const int z = 9 * wi + (k < 2 ? 2 * w + ((wi & 1) ^ k) : 8), ln = z >> 4;
      row0[k] = (z & 15) * 40 + (k == 2 ? w * 8 : 0);
      const bool isq = ln < 12;
      const u32 rs = isq ? NH * DH * 2 : FF * 2;
      const u32 so = row0[k] * rs + (isq ? qd * 1536 + ln * 128 : qd * 384 + (ln - (ln < 15 ? 12 : 15)) * 128);
      const __amdgpu_buffer_rsrc_t rr = mkrsrc(isq ? wq : ln < 15 ? w1 : w3, DM * rs);
      const u32 vo = r * rs + j * 16;
      if (k < 2) {
#pragma unroll
        for (int i = 0; i < 5; ++i) S1[5 * k + i] = __builtin_amdgcn_raw_buffer_load_b128(rr, vo, so + 8 * i * rs, NT);
      } else {
        S1[10] = __builtin_amdgcn_raw_buffer_load_b128(rr, vo, so, NT);
        S1[11] = __builtin_amdgcn_raw_buffer_load_b128(rr, w == 0 ? vo + so + 32 * rs : OOB, 0, NT);
      }
      if (k == 0) {
        const __amdgpu_buffer_rsrc_t rx = mkrsrc(x, DM * 2);
        xa = __builtin_amdgcn_raw_buffer_load_b128(rx, l * 16, 0, 0);
        xb = __builtin_amdgcn_raw_buffer_load_b32(rx, l * 4, 1024, 0);
      }
      SCHED();
    }
    const u32 epoch = __builtin_amdgcn_raw_buffer_load_b32(wsr, WS_EPOCH, 0, SC1);
    {
      const u32x4 z = {0u, 0u, 0u, 0u};
      __builtin_amdgcn_raw_buffer_store_b128(z, wsr, (w == 3 && l < 49) ? myrec + R_G2 + l * 16 : (w == 2 && l < 32) ? myrec + R_G3 + l * 16 : OOB, 0, SC1);
    }
    if (tid < 8) cnt[tid] = 0;
    SCHED();
    wg_barrier();
    SCHED();
    {
      float xv[10];
#pragma unroll
      for (int k = 0; k < 4; ++k) { xv[2 * k] = bf_lo(xa[k]); xv[2 * k + 1] = bf_hi(xa[k]); }
      xv[8] = bf_lo(xb); xv[9] = bf_hi(xb);
      float sm = 0.f;
#pragma unroll
      for (int k = 0; k < 10; ++k) sm += xv[k];
      sm = sum_hi3(sum_lo3(sm));
      const float mean = sm * (1.f / DM);
      float sv = 0.f;
#pragma unroll
      for (int k = 0; k < 10; ++k) { const float d = xv[k] - mean; sv = fmaf(d, d, sv); }
      sv = sum_hi3(sum_lo3(sv));
      const float var = rbf(sv * (1.f / DM));
      const float mb = rbf(mean);
      const float sd = rbf(sqrtf(rbf(var + 1e-5f)));
      u32 xp[10];
#pragma unroll
      for (int k = 0; k < 10; ++k) xp[k] = pkbf(rbf(xv[k] - mb) / sd);
      *reinterpret_cast<u32x4*>(xn_s + l * 8) = u32x4{xp[0], xp[1], xp[2], xp[3]};
      *reinterpret_cast<u32x4*>(xn_s + l * 8 + 4) = u32x4{xp[4], xp[5], xp[6], xp[7]};
      *reinterpret_cast<u32x2*>(xn_s + 512 + l * 2) = u32x2{xp[8], xp[9]};
      asm volatile("s_waitcnt lgkmcnt(0)" ::: "memory");
    }
    MAKE_MSK();
    const u32 tag = epoch + 1;
    {
      u32 xr[12];
#pragma unroll
      for (int i = 0; i < 10; ++i) xr[i] = xn_s[row0[i / 5] + 8 * (i % 5) + r];
      xr[10] = xn_s[row0[2] + r];
      xr[11] = w == 0 ? xn_s[row0[2] + 32 + r] : 0u;
      f32x4 a[3];
#pragma unroll
      for (int k = 0; k < 3; ++k) a[k] = f32x4{0.f, 0.f, 0.f, 0.f};
#pragma unroll
      for (int i = 0; i < 12; ++i) a[i / 5] = mfma(msk & xr[i], S1[i], a[i / 5]);
      if (useful) {
#pragma unroll
        for (int k = 0; k < 3; ++k) *reinterpret_cast<f32x4*>(s1_s + (k < 2 ? 4 * w + 2 * ((wi & 1) ^ k) + hh : 16 + w * 2 + hh) * 64 + dcol) = a[k];
      }
      asm volatile("s_waitcnt vmcnt(0)" ::: "memory");
      if (lds_ticket(cnt + C_S1, l) == 3) {
        const int nA = 16 - ((9 * wi) & 15);
        float va = 0.f, vb = 0.f;
#pragma unroll
        for (int c = 0; c < 8; ++c) {
          const float v = s1_s[(2 * c) * 64 + l] + s1_s[(2 * c + 1) * 64 + l];
          if (c < nA) va += v; else vb += v;
        }
        float v8 = 0.f;
#pragma unroll
        for (int t = 16; t < 24; ++t) v8 += s1_s[t * 64 + l];
        if (8 < nA) va += v8; else vb += v8;
        __builtin_amdgcn_raw_buffer_store_b64(u32x2{__float_as_uint(va), tag}, wsr, myrec + R_G1 + l * 8, 0, SC1);
        __builtin_amdgcn_raw_buffer_store_b64(u32x2{__float_as_uint(vb), tag}, wsr, nA <= 8 ? myrec + R_G1 + 512 + l * 8 : OOB, 0, SC1);
      }
    }
    STAMP(1);
    {
      u32 ga[10];
      bool use3[3];
#pragma unroll
      for (int k = 0; k < 3; ++k) {
        const int fc = (g & 3) * 48 + min(l, 47);
        const int ln = w == 0 ? (g & 3) * 3 + k : 12 + 3 * min(k, 1) + (fc >> 6);
        const int col = w == 0 ? l : (fc & 63);
        const int f0 = (16 * ln) / 9, f1 = (16 * ln + 15) / 9;
        use3[k] = f0 + 2 <= f1;
#pragma unroll
        for (int i = 0; i < 3; ++i) {
          const int pw = min(f0 + i, f1);
          ga[k * 3 + i] = WS_REC + (qd * 32 + pw) * REC + R_G1 + (9 * pw < 16 * ln ? 512 : 0) + col * 8;
        }
      }
      ga[9] = grec + j * REC + R_G1;
      if (w == 1) {
        ga[9] = WS_REC + ((l & 31) * NS + (red_b ? (s & 4) + hh : s)) * REC + R_G1;
        ga[8] = ga[9] + (red_b ? 2 * REC : 0);
      }
      lds_wait(cnt + C_PROG, 8);
      if (w != 2 && (w != 1 || is_red)) {
        u32x2 G[10];
        bool ok = false;
        u32 spins = 0;
        for (;;) {
          asm volatile("" ::: "memory");
          if (!ok) {
#pragma unroll
            for (int i = 0; i < 10; ++i) if (w == 0 || (w == 3 && i < 6) || (w == 1 && i >= 8)) G[i] = __builtin_amdgcn_raw_buffer_load_b64(wsr, ga[i], 0, SC1);
            u32 d_ = 0;
#pragma unroll
            for (int i = 0; i < 10; ++i) if (w == 0 || (w == 3 && i < 6) || (w == 1 && i >= 8)) d_ |= G[i][1] ^ tag;
            ok = (d_ == 0);
          }
          if (__all(ok) || ++spins > SPIN_CAP) break;
          RETRY(w == 0 ? 1 : w == 3 ? 1000 : 100000);
          __builtin_amdgcn_s_sleep(8);
        }
        float t[3];
#pragma unroll
        for (int k = 0; k < 3; ++k) t[k] = __uint_as_float(G[3 * k][0]) + __uint_as_float(G[3 * k + 1][0]) + (use3[k] ? __uint_as_float(G[3 * k + 2][0]) : 0.f);
        if (w == 0) {
#pragma unroll
          for (int k = 0; k < 3; ++k) ((u16*)q_s)[64 * k + l] = __builtin_bit_cast(u16, (__bf16)t[k]);
          if (l == 0) cnt[C_EPOCH] = tag;
          lds_arrive(cnt + C_Q, l);
        } else if (w == 3) {
          const float gate = rbf(t[0]), up = rbf(t[1]);
          const float hv = rbf(gate / (1.f + __expf(-gate))) * up;
          h_s[l] = l < 48 ? pkbf(hv) : 0u;
        }
      }
    }
    STAMP(2);
    __builtin_amdgcn_s_setprio(3);
    wg_barrier();
    STAMP(5);
    {
      const int col = min(tid, 191);
      float pv[16];
#pragma unroll
      for (int i = 0; i < 16; ++i) pv[i] = op_s[i * 192 + col];
      const float mj = m_s[j];
      const float M = max_lo3(mj);
      const float fj = __builtin_amdgcn_exp2f((mj - M) * LOG2E);
      float o = 0.f;
      if (w == 3) {
        const float L = sum_lo3(l_s[j] * fj);
        o = (l & 1) ? L : M;
      } else {
#pragma unroll
        for (int i = 0; i < 8; ++i) o = fmaf(__uint_as_float(__builtin_amdgcn_readlane(__float_as_uint(fj), i)), pv[2 * i] + pv[2 * i + 1], o);
      }
      __builtin_amdgcn_raw_buffer_store_b32(__float_as_uint(o) | 1u, wsr, tid < 194 ? myrec + R_G2 + tid * 4 : OOB, 0, SC1);
    }
    STAMP(6);
    wg_barrier();
    STAMP(7);
    if (w < 2) {
      float y = 0.f;
#pragma unroll
      for (int i = 0; i < 16; ++i) y += yp_s[i * 128 + tid];
      __builtin_amdgcn_raw_buffer_store_b32(__float_as_uint(y) | 1u, wsr, myrec + R_G3 + tid * 4, 0, SC1);
    }
    STAMP(8);
  }
  STAMP(9);
}

extern "C" void kernel_launch(void* const* d_in, const int* in_sizes, int n_in, void* d_out, int out_size, void* d_ws,
                              size_t ws_size, hipStream_t stream) {
  fused_decode_kernel<<<256, 768, 0, stream>>>((const u16*)d_in[0], (const char*)d_in[1], (const char*)d_in[2],
                                               (const char*)d_in[3], (const char*)d_in[4], (const char*)d_in[6],
                                               (const char*)d_in[7], (const char*)d_in[8], (u16*)d_out, (char*)d_ws);
}
```

```cpp
#define TU 86
#define TB 170
#include <hip/hip_runtime.h>
#include <hip/hip_bf16.h>
#include <stdint.h>

typedef unsigned short u16;
typedef unsigned int u32;
typedef unsigned long long u64;
typedef u32 u32x2 __attribute__((ext_vector_type(2)));
typedef u32 u32x4 __attribute__((ext_vector_type(4)));
typedef float f32x4 __attribute__((ext_vector_type(4)));
typedef __attribute__((ext_vector_type(2))) __bf16 bf16x2;
typedef __attribute__((ext_vector_type(8))) __bf16 bf16x8;
#define LAS __attribute__((address_space(3)))

#define D1 22
#define D2 18
#define PD 7
#define GPOS 5

constexpr int DM = 640, NH = 32, DH = 192, SK = 32768, WIN = 4096, FF = 1536;
constexpr int NS = 8;
constexpr u32 WS_EPOCH = 0;
constexpr u32 WS_REC = 4096, REC = 2560;
constexpr u32 R_G1 = 0;
constexpr u32 R_G2 = 1024;
constexpr u32 R_ST = R_G2 + 768;
constexpr u32 R_G3 = 1920;
constexpr u32 WS_END = WS_REC + NH * NS * REC;
constexpr u32 SPIN_CAP = 1u << 19;
constexpr float LOG2E = 1.4426950408889634f;
constexpr u32 OOB = 0xfffffff0u;
constexpr int NT = 2, SC1 = 16;

__device__ __forceinline__ float bf_lo(u32 v) { return __uint_as_float(v << 16); }
__device__ __forceinline__ float bf_hi(u32 v) { return __uint_as_float(v & 0xffff0000u); }
__device__ __forceinline__ float rbf(float f) { return (float)(__bf16)f; }
__device__ __forceinline__ u32 pkbf(float f) { const bf16x2 h = {(__bf16)f, (__bf16)f}; return __builtin_bit_cast(u32, h); }
__device__ __forceinline__ float dot2(u32 a, u32 b, float c) {
  return __builtin_amdgcn_fdot2_f32_bf16(__builtin_bit_cast(bf16x2, a), __builtin_bit_cast(bf16x2, b), c, false);
}
__device__ __forceinline__ f32x4 mfma(u32x4 a, u32x4 b, f32x4 c) {
  return __builtin_amdgcn_mfma_f32_16x16x32_bf16(__builtin_bit_cast(bf16x8, a), __builtin_bit_cast(bf16x8, b), c, 0, 0, 0);
}
__device__ __forceinline__ __amdgpu_buffer_rsrc_t mkrsrc(const void* p, u32 bytes) {
  return __builtin_amdgcn_make_buffer_rsrc(const_cast<void*>(p), 0, bytes, 0x00020000);
}
template <int CTRL> __device__ __forceinline__ float dpp(float x) {
  return __builtin_bit_cast(float, __builtin_amdgcn_mov_dpp(__builtin_bit_cast(int, x), CTRL, 0xf, 0xf, true));
}
constexpr int XOR1 = 0xB1, XOR2 = 0x4E, HMIR = 0x141, ROR8 = 0x128, ROR4 = 0x124;
__device__ __forceinline__ float swap16_add(float a, float b) {
  auto s = __builtin_amdgcn_permlane16_swap(__float_as_uint(a), __float_as_uint(b), false, false);
  return __uint_as_float(s[0]) + __uint_as_float(s[1]);
}
__device__ __forceinline__ float swap32_add(float a, float b) {
  auto s = __builtin_amdgcn_permlane32_swap(__float_as_uint(a), __float_as_uint(b), false, false);
  return __uint_as_float(s[0]) + __uint_as_float(s[1]);
}
__device__ __forceinline__ float swap16_max(float a) {
  auto s = __builtin_amdgcn_permlane16_swap(__float_as_uint(a), __float_as_uint(a), false, false);
  return fmaxf(__uint_as_float(s[0]), __uint_as_float(s[1]));
}
__device__ __forceinline__ float swap32_max(float a) {
  auto s = __builtin_amdgcn_permlane32_swap(__float_as_uint(a), __float_as_uint(a), false, false);
  return fmaxf(__uint_as_float(s[0]), __uint_as_float(s[1]));
}
__device__ __forceinline__ float sum_hi3(float x) { x += dpp<ROR8>(x); x = swap16_add(x, x); return swap32_add(x, x); }
__device__ __forceinline__ float max_hi3(float x) { x = fmaxf(x, dpp<ROR8>(x)); x = swap16_max(x); return swap32_max(x); }
__device__ __forceinline__ float sum_lo3(float x) { x += dpp<XOR1>(x); x += dpp<XOR2>(x); x += dpp<HMIR>(x); return x; }
__device__ __forceinline__ float max_lo3(float x) { x = fmaxf(x, dpp<XOR1>(x)); x = fmaxf(x, dpp<XOR2>(x)); x = fmaxf(x, dpp<HMIR>(x)); return x; }
__device__ __forceinline__ float treduce4(float a0, float a1, float a2, float a3) {
  return swap32_add(swap16_add(a0, a2), swap16_add(a1, a3));
}
__device__ __forceinline__ void lds_arrive(u32* c, int l) {
  asm volatile("s_waitcnt lgkmcnt(0)" ::: "memory");
  if (l == 0) __hip_atomic_fetch_add((LAS u32*)c, 1u, __ATOMIC_RELAXED, __HIP_MEMORY_SCOPE_WORKGROUP);
  asm volatile("" ::: "memory");
}
__device__ __forceinline__ u32 lds_ticket(u32* c, int l) {
  asm volatile("s_waitcnt lgkmcnt(0)" ::: "memory");
  u32 t = 0;
  if (l == 0) t = __hip_atomic_fetch_add((LAS u32*)c, 1u, __ATOMIC_RELAXED, __HIP_MEMORY_SCOPE_WORKGROUP);
  t = __builtin_amdgcn_readfirstlane(t);
  asm volatile("" ::: "memory");
  return t;
}
__device__ __forceinline__ void lds_wait(u32* c, u32 n) {
  asm volatile("" ::: "memory");
  while (__builtin_amdgcn_readfirstlane(__hip_atomic_load((LAS u32*)c, __ATOMIC_RELAXED, __HIP_MEMORY_SCOPE_WORKGROUP)) < n) __builtin_amdgcn_s_sleep(1);
  asm volatile("" ::: "memory");
}
#define SCHED() __builtin_amdgcn_sched_barrier(0)
__device__ __forceinline__ void wg_barrier() {
  asm volatile("s_waitcnt lgkmcnt(0)" ::: "memory");
  __builtin_amdgcn_s_barrier();
  asm volatile("" ::: "memory");
}
#define STR2(x) #x
#define STR(x) STR2(x)
#define PACE() asm volatile("s_waitcnt vmcnt(" STR(PD) ")" ::: "memory")

enum { C_S1 = 0, C_PROG, C_Q, C_EPOCH };

__global__ void __launch_bounds__(768, 3)
fused_decode_kernel(const u16* __restrict__ x, const char* __restrict__ kc, const char* __restrict__ vc,
                    const char* __restrict__ wq, const char* __restrict__ wo, const char* __restrict__ w1,
                    const char* __restrict__ w3, const char* __restrict__ w2, u16* __restrict__ out, char* ws) {
  __shared__ __attribute__((aligned(16))) float lds[640 + 96 + 32 + 64 + 24 * 64 + 16 * 192 + 16 * 128];
  u32* xn_s = (u32*)lds;
  u32* q_s = (u32*)(lds + 640);
  float* m_s = lds + 736;
  float* l_s = lds + 744;
  u32* cnt = (u32*)(lds + 752);
  u32* h_s = (u32*)(lds + 768);
  float* s1_s = lds + 832;
  float* op_s = s1_s + 24 * 64;
  float* yp_s = op_s + 16 * 192;
  u32 t0 = (u32)__builtin_amdgcn_s_memrealtime();
  asm volatile("" : "+s"(t0));
  const int b = blockIdx.x;
  const int g = (b & 7) * 4 + (b >> 6), s = (b >> 3) & 7;
  const int tid = threadIdx.x;
  const int w = __builtin_amdgcn_readfirstlane(tid >> 6), l = tid & 63;
  const int r = l >> 3, j = l & 7;
  const __amdgpu_buffer_rsrc_t wsr = mkrsrc(ws, WS_END);
  const u32 grec = WS_REC + g * NS * REC, myrec = grec + s * REC;
  const bool is_red = (s == (g >> 2)) && !(g & 1);
  const bool red_b = (g & 2) != 0;
  const int hh = l >> 5;
  const bool useful = ((l >> 3) & 1) == hh;
  const int dcol = 8 * j + 4 * ((l >> 4) & 1);
#define MAKE_MSK() u32x4 msk; _Pragma("unroll") for (int d = 0; d < 4; ++d) msk[d] = (j >> 1) == d ? ((j & 1) ? 0xffff0000u : 0x0000ffffu) : 0u

  if (w >= 4) {
    const int sw = w - 4;
    const size_t kvbase = ((size_t)g * SK + (SK - WIN)) * (DH * 2);
    const __amdgpu_buffer_rsrc_t rk = mkrsrc(kc + kvbase, WIN * DH * 2), rv = mkrsrc(vc + kvbase, WIN * DH * 2);
    const u32 ll = (u32)((sw * 8 + s) * 3072 + r * 384 + j * 16);
#define UOFF(u) ((((u) + g) & 7) * 196608)
    u32x4 K[8][3], V[8][3];
    const u32 tz = t0 + TB + ((g & 3) * 8 + sw) * TU / 32;
#define GATE(n) do { while ((int)((u32)__builtin_amdgcn_s_memrealtime() - (tz + (n) * TU)) < 0) __builtin_amdgcn_s_sleep(1); asm volatile("" ::: "memory"); } while (0)
#define LOAD1(dst, rs, u, c) do { PACE(); dst[u][c] = __builtin_amdgcn_raw_buffer_load_b128(rs, ll + (c) * 128, UOFF(u), NT); } while (0)
#define LOAD3(dst, rs, u) do { GATE(8 + (u)); LOAD1(dst, rs, u, 0); LOAD1(dst, rs, u, 1); LOAD1(dst, rs, u, 2); SCHED(); } while (0)
    wg_barrier();
#pragma unroll
    for (int i = 0; i < 24; ++i) { if (i % 3 == 0 && i) GATE(i / 3); LOAD1(K, rk, i / 3, i % 3); if (i == GPOS * 3 + 2) lds_arrive(cnt + C_PROG, l); }
    SCHED();
    LOAD3(V, rv, 0); LOAD3(V, rv, 1); LOAD3(V, rv, 2);
    lds_wait(cnt + C_Q, 1);
    MAKE_MSK();
    u32 ppk[8];
    {
      u32x4 q[3];
      float p[8];
#pragma unroll
      for (int c = 0; c < 3; ++c) q[c] = *reinterpret_cast<const u32x4*>(q_s + c * 32 + j * 4);
#define QK(u) do { float d = 0.f; _Pragma("unroll") for (int c = 0; c < 3; ++c) _Pragma("unroll") for (int k = 0; k < 4; ++k) d = dot2(K[u][c][k], q[c][k], d); \
      p[u] = rbf(rbf(sum_lo3(d)) * 0.07216878364870322f); } while (0)
#define QK2(u) do { QK(u); QK(u + 1); asm volatile("" : "+v"(p[u]), "+v"(p[u + 1])); SCHED(); } while (0)
      QK2(0); LOAD3(V, rv, 3);
      QK2(2); LOAD3(V, rv, 4);
      QK2(4); LOAD3(V, rv, 5);
      QK2(6);
      const float mw = max_hi3(fmaxf(fmaxf(fmaxf(p[0], p[1]), fmaxf(p[2], p[3])), fmaxf(fmaxf(p[4], p[5]), fmaxf(p[6], p[7]))));
      float ls = 0.f;
#pragma unroll
      for (int u = 0; u < 8; ++u) { const float e = __builtin_amdgcn_exp2f((p[u] - mw) * LOG2E); ls += e; ppk[u] = pkbf(e); }
      ls = sum_hi3(ls);
      if (l == 0) { m_s[sw] = mw; l_s[sw] = ls; }
    }
    f32x4 acc[3];
#pragma unroll
    for (int c = 0; c < 3; ++c) acc[c] = f32x4{0.f, 0.f, 0.f, 0.f};
#define PV(u) do { const u32x4 a_ = msk & ppk[u]; _Pragma("unroll") for (int c = 0; c < 3; ++c) acc[c] = mfma(a_, V[u][c], acc[c]); \
      asm volatile("" : "+v"(acc[0]), "+v"(acc[1]), "+v"(acc[2])); SCHED(); } while (0)
    LOAD3(V, rv, 6);
    PV(0); PV(1); PV(2); PV(3);
    LOAD3(V, rv, 7);
    u32x4 WO[5];
    const int ocol = red_b ? 512 + s * 16 + 4 * (sw & 3) : s * 64 + 8 * sw + 4 * (l & 1);
    const int ok2 = 2 * ((l >> 4) & 1) + hh;
    u32 xraw;
    {
      const __amdgpu_buffer_rsrc_t rw = mkrsrc((sw < 6) ? wo + (size_t)(g * DH + sw * 32) * (DM * 2) : w2 + (size_t)(g * 48 + (sw - 6) * 32) * (DM * 2), 32 * DM * 2);
      const u32 wl = ((sw == 7) ? (r & 3) : r) * (4 * DM * 2) + j * 16;
#define WLD(i) WO[i] = __builtin_amdgcn_raw_buffer_load_b128(rw, wl + (i) * (DM * 2), s * 128, NT)
#define WLD4() do { WO[4] = __builtin_amdgcn_raw_buffer_load_b128(rw, wl, (s & 3) * (DM * 2) + (8 + (s >> 2)) * 128, NT); xraw = x[ocol + ok2]; } while (0)
      PV(4); PV(5); PV(6);
      GATE(16);
      WLD(0); WLD(1); WLD(2);
      SCHED();
      PV(7);
      if (useful) {
#pragma unroll
        for (int c = 0; c < 3; ++c) *reinterpret_cast<f32x4*>(op_s + (sw * 2 + hh) * 192 + c * 64 + dcol) = acc[c];
      }
      wg_barrier();
      __builtin_amdgcn_s_sleep(8);
      asm volatile("" ::: "memory");
      WLD(3); WLD4();
      SCHED();
    }
    u32 opk[4];
    if (sw < 6) {
      const u32 goff = grec + j * REC + R_G2 + sw * 128 + r * 16;
      const u32 soff = grec + j * REC + R_ST;
      u32x2 SS;
      u32x4 O;
      __builtin_amdgcn_s_sleep(D1);
      bool ok = false;
      u32 spins = 0;
      for (;;) {
        asm volatile("" ::: "memory");
        if (!ok) {
          SS = __builtin_amdgcn_raw_buffer_load_b64(wsr, soff, 0, SC1);
          O = __builtin_amdgcn_raw_buffer_load_b128(wsr, goff, 0, SC1);
          ok = (O[0] & O[1] & O[2] & O[3] & SS[0] & SS[1] & 1) != 0;
        }
        if (__all(ok) || ++spins > SPIN_CAP) break;
        __builtin_amdgcn_s_sleep(2);
      }
      const float ms = __uint_as_float(SS[0] & ~1u);
      const float f = __builtin_amdgcn_exp2f((ms - max_lo3(ms)) * LOG2E);
      const float sc = f * __builtin_amdgcn_rcpf(sum_lo3(f * __uint_as_float(SS[1])));
#pragma unroll
      for (int i = 0; i < 4; ++i) opk[i] = pkbf(sum_lo3(sc * __uint_as_float(O[i])));
    } else {
      const u32x4 hq = *reinterpret_cast<const u32x4*>(h_s + (sw - 6) * 32 + 4 * r);
#pragma unroll
      for (int i = 0; i < 4; ++i) opk[i] = hq[i];
    }
    {
      f32x4 y1 = {0.f, 0.f, 0.f, 0.f}, y2 = y1;
      const u32 o2 = (s & 2) ? ((s & 1) ? opk[3] : opk[2]) : ((s & 1) ? opk[1] : opk[0]);
      y2 = mfma(msk & o2, WO[4], y2);
#pragma unroll
      for (int i = 0; i < 4; ++i) y1 = mfma(msk & opk[i], WO[i], y1);
      if (useful) {
        *reinterpret_cast<f32x4*>(yp_s + (sw * 2 + hh) * 128 + dcol) = y1;
        *reinterpret_cast<f32x4*>(yp_s + (sw * 2 + hh) * 128 + 64 + dcol) = y2;
      }
    }
    wg_barrier();
    if (is_red && !(red_b && sw >= 4)) {
      const u32 goff = red_b ? WS_REC + ((l & 31) * NS + (s & 4) + hh) * REC + R_G3 + 256 + (s & 3) * 64 + sw * 16
                             : WS_REC + ((l >> 1) * NS + s) * REC + R_G3 + sw * 32 + (l & 1) * 16;
      u32x4 Y, Z;
      __builtin_amdgcn_s_sleep(D2);
      bool ok = false;
      u32 spins = 0;
      for (;;) {
        asm volatile("" ::: "memory");
        if (!ok) {
          Y = __builtin_amdgcn_raw_buffer_load_b128(wsr, goff, 0, SC1);
          Z = __builtin_amdgcn_raw_buffer_load_b128(wsr, red_b ? goff + 2 * REC : OOB, 0, SC1);
          ok = (Y[0] & Y[1] & Y[2] & Y[3] & (red_b ? Z[0] & Z[1] & Z[2] & Z[3] : 1u) & 1) != 0;
        }
        if (__all(ok) || ++spins > SPIN_CAP) break;
        __builtin_amdgcn_s_sleep(4);
      }
      const f32x4 yz = __builtin_bit_cast(f32x4, Y) + __builtin_bit_cast(f32x4, Z);
      float t = treduce4(yz[0], yz[1], yz[2], yz[3]);
      t += dpp<ROR8>(t); t += dpp<ROR4>(t); t += dpp<XOR2>(t);
      const float t1 = t + dpp<XOR1>(t);
      if ((l & (red_b ? 15 : 14)) == 0) out[ocol + ok2] = __builtin_bit_cast(u16, (__bf16)(__uint_as_float(xraw << 16) + (red_b ? t1 : t)));
      if (b == 0 && sw == 0) __builtin_amdgcn_raw_buffer_store_b32(cnt[C_EPOCH], wsr, l == 0 ? WS_EPOCH : OOB, 0, SC1);
    }
  } else {
    const int qd = g >> 2, wi = (g & 3) * 8 + s;
    u32x4 S1[12], xa;
    u32 xb;
    int row0[3];
#pragma unroll
    for (int k = 0; k < 3; ++k) {
      const int z = 9 * wi + (k < 2 ? 2 * w + ((wi & 1) ^ k) : 8), ln = z >> 4;
      row0[k] = (z & 15) * 40 + (k == 2 ? w * 8 : 0);
      const bool isq = ln < 12;
      const u32 rs = isq ? NH * DH * 2 : FF * 2;
      const u32 so = row0[k] * rs + (isq ? qd * 1536 + ln * 128 : qd * 384 + (ln - (ln < 15 ? 12 : 15)) * 128);
      const __amdgpu_buffer_rsrc_t rr = mkrsrc(isq ? wq : ln < 15 ? w1 : w3, DM * rs);
      const u32 vo = r * rs + j * 16;
      if (k < 2) {
#pragma unroll
        for (int i = 0; i < 5; ++i) S1[5 * k + i] = __builtin_amdgcn_raw_buffer_load_b128(rr, vo, so + 8 * i * rs, NT);
      } else {
        S1[10] = __builtin_amdgcn_raw_buffer_load_b128(rr, vo, so, NT);
        S1[11] = __builtin_amdgcn_raw_buffer_load_b128(rr, w == 0 ? vo + so + 32 * rs : OOB, 0, NT);
      }
      if (k == 0) {
        const __amdgpu_buffer_rsrc_t rx = mkrsrc(x, DM * 2);
        xa = __builtin_amdgcn_raw_buffer_load_b128(rx, l * 16, 0, 0);
        xb = __builtin_amdgcn_raw_buffer_load_b32(rx, l * 4, 1024, 0);
      }
      SCHED();
    }
    const u32 epoch = __builtin_amdgcn_raw_buffer_load_b32(wsr, WS_EPOCH, 0, SC1);
    {
      const u32x4 z = {0u, 0u, 0u, 0u};
      __builtin_amdgcn_raw_buffer_store_b128(z, wsr, (w == 3 && l < 49) ? myrec + R_G2 + l * 16 : (w == 2 && l < 32) ? myrec + R_G3 + l * 16 : OOB, 0, SC1);
    }
    if (tid < 8) cnt[tid] = 0;
    SCHED();
    wg_barrier();
    SCHED();
    {
      float xv[10];
#pragma unroll
      for (int k = 0; k < 4; ++k) { xv[2 * k] = bf_lo(xa[k]); xv[2 * k + 1] = bf_hi(xa[k]); }
      xv[8] = bf_lo(xb); xv[9] = bf_hi(xb);
      float sm = 0.f;
#pragma unroll
      for (int k = 0; k < 10; ++k) sm += xv[k];
      sm = sum_hi3(sum_lo3(sm));
      const float mean = sm * (1.f / DM);
      float sv = 0.f;
#pragma unroll
      for (int k = 0; k < 10; ++k) { const float d = xv[k] - mean; sv = fmaf(d, d, sv); }
      sv = sum_hi3(sum_lo3(sv));
      const float var = rbf(sv * (1.f / DM));
      const float mb = rbf(mean);
      const float sd = rbf(sqrtf(rbf(var + 1e-5f)));
      u32 xp[10];
#pragma unroll
      for (int k = 0; k < 10; ++k) xp[k] = pkbf(rbf(xv[k] - mb) / sd);
      *reinterpret_cast<u32x4*>(xn_s + l * 8) = u32x4{xp[0], xp[1], xp[2], xp[3]};
      *reinterpret_cast<u32x4*>(xn_s + l * 8 + 4) = u32x4{xp[4], xp[5], xp[6], xp[7]};
      *reinterpret_cast<u32x2*>(xn_s + 512 + l * 2) = u32x2{xp[8], xp[9]};
      asm volatile("s_waitcnt lgkmcnt(0)" ::: "memory");
    }
    MAKE_MSK();
    const u32 tag = epoch + 1;
    {
      u32 xr[12];
#pragma unroll
      for (int i = 0; i < 10; ++i) xr[i] = xn_s[row0[i / 5] + 8 * (i % 5) + r];
      xr[10] = xn_s[row0[2] + r];
      xr[11] = w == 0 ? xn_s[row0[2] + 32 + r] : 0u;
      f32x4 a[3];
#pragma unroll
      for (int k = 0; k < 3; ++k) a[k] = f32x4{0.f, 0.f, 0.f, 0.f};
#pragma unroll
      for (int i = 0; i < 12; ++i) a[i / 5] = mfma(msk & xr[i], S1[i], a[i / 5]);
      if (useful) {
#pragma unroll
        for (int k = 0; k < 3; ++k) *reinterpret_cast<f32x4*>(s1_s + (k < 2 ? 4 * w + 2 * ((wi & 1) ^ k) + hh : 16 + w * 2 + hh) * 64 + dcol) = a[k];
      }
      asm volatile("s_waitcnt vmcnt(0)" ::: "memory");
      if (lds_ticket(cnt + C_S1, l) == 3) {
        const int nA = 16 - ((9 * wi) & 15);
        float va = 0.f, vb = 0.f;
#pragma unroll
        for (int c = 0; c < 8; ++c) {
          const float v = s1_s[(2 * c) * 64 + l] + s1_s[(2 * c + 1) * 64 + l];
          if (c < nA) va += v; else vb += v;
        }
        float v8 = 0.f;
#pragma unroll
        for (int t = 16; t < 24; ++t) v8 += s1_s[t * 64 + l];
        if (8 < nA) va += v8; else vb += v8;
        __builtin_amdgcn_raw_buffer_store_b64(u32x2{__float_as_uint(va), tag}, wsr, myrec + R_G1 + l * 8, 0, SC1);
        __builtin_amdgcn_raw_buffer_store_b64(u32x2{__float_as_uint(vb), tag}, wsr, nA <= 8 ? myrec + R_G1 + 512 + l * 8 : OOB, 0, SC1);
      }
    }
    {
      u32 ga[10];
      bool use3[3];
#pragma unroll
      for (int k = 0; k < 3; ++k) {
        const int fc = (g & 3) * 48 + min(l, 47);
        const int ln = w == 0 ? (g & 3) * 3 + k : 12 + 3 * min(k, 1) + (fc >> 6);
        const int col = w == 0 ? l : (fc & 63);
        const int f0 = (16 * ln) / 9, f1 = (16 * ln + 15) / 9;
        use3[k] = f0 + 2 <= f1;
#pragma unroll
        for (int i = 0; i < 3; ++i) {
          const int pw = min(f0 + i, f1);
          ga[k * 3 + i] = WS_REC + (qd * 32 + pw) * REC + R_G1 + (9 * pw < 16 * ln ? 512 : 0) + col * 8;
        }
      }
      ga[9] = grec + j * REC + R_G1;
      if (w == 1) {
        ga[9] = WS_REC + ((l & 31) * NS + (red_b ? (s & 4) + hh : s)) * REC + R_G1;
        ga[8] = ga[9] + (red_b ? 2 * REC : 0);
      }
      lds_wait(cnt + C_PROG, 8);
      if (w != 2 && (w != 1 || is_red)) {
        u32x2 G[10];
        bool ok = false;
        u32 spins = 0;
        for (;;) {
          asm volatile("" ::: "memory");
          if (!ok) {
#pragma unroll
            for (int i = 0; i < 10; ++i) if (w == 0 || (w == 3 && i < 6) || (w == 1 && i >= 8)) G[i] = __builtin_amdgcn_raw_buffer_load_b64(wsr, ga[i], 0, SC1);
            u32 d_ = 0;
#pragma unroll
            for (int i = 0; i < 10; ++i) if (w == 0 || (w == 3 && i < 6) || (w == 1 && i >= 8)) d_ |= G[i][1] ^ tag;
            ok = (d_ == 0);
          }
          if (__all(ok) || ++spins > SPIN_CAP) break;
          __builtin_amdgcn_s_sleep(8);
        }
        float t[3];
#pragma unroll
        for (int k = 0; k < 3; ++k) t[k] = __uint_as_float(G[3 * k][0]) + __uint_as_float(G[3 * k + 1][0]) + (use3[k] ? __uint_as_float(G[3 * k + 2][0]) : 0.f);
        if (w == 0) {
#pragma unroll
          for (int k = 0; k < 3; ++k) ((u16*)q_s)[64 * k + l] = __builtin_bit_cast(u16, (__bf16)t[k]);
          if (l == 0) cnt[C_EPOCH] = tag;
          lds_arrive(cnt + C_Q, l);
        } else if (w == 3) {
          const float gate = rbf(t[0]), up = rbf(t[1]);
          const float hv = rbf(gate / (1.f + __expf(-gate))) * up;
          h_s[l] = l < 48 ? pkbf(hv) : 0u;
        }
      }
    }
    __builtin_amdgcn_s_setprio(3);
    wg_barrier();
    {
      const int col = min(tid, 191);
      float pv[16];
#pragma unroll
      for (int i = 0; i < 16; ++i) pv[i] = op_s[i * 192 + col];
      const float mj = m_s[j];
      const float M = max_lo3(mj);
      const float fj = __builtin_amdgcn_exp2f((mj - M) * LOG2E);
      float o = 0.f;
      if (w == 3) {
        const float L = sum_lo3(l_s[j] * fj);
        o = (l & 1) ? L : M;
      } else {
#pragma unroll
        for (int i = 0; i < 8; ++i) o = fmaf(__uint_as_float(__builtin_amdgcn_readlane(__float_as_uint(fj), i)), pv[2 * i] + pv[2 * i + 1], o);
      }
      __builtin_amdgcn_raw_buffer_store_b32(__float_as_uint(o) | 1u, wsr, tid < 194 ? myrec + R_G2 + tid * 4 : OOB, 0, SC1);
    }
    wg_barrier();
    if (w < 2) {
      float y = 0.f;
#pragma unroll
      for (int i = 0; i < 16; ++i) y += yp_s[i * 128 + tid];
      __builtin_amdgcn_raw_buffer_store_b32(__float_as_uint(y) | 1u, wsr, myrec + R_G3 + tid * 4, 0, SC1);
    }
  }
}

extern "C" void kernel_launch(void* const* d_in, const int* in_sizes, int n_in, void* d_out, int out_size, void* d_ws,
                              size_t ws_size, hipStream_t stream) {
  fused_decode_kernel<<<256, 768, 0, stream>>>((const u16*)d_in[0], (const char*)d_in[1], (const char*)d_in[2],
                                               (const char*)d_in[3], (const char*)d_in[4], (const char*)d_in[6],
                                               (const char*)d_in[7], (const char*)d_in[8], (u16*)d_out, (char*)d_ws);
}
```

```cpp
#define MODE 2
#include <hip/hip_runtime.h>
#include <hip/hip_bf16.h>
#include <stdint.h>

typedef unsigned short u16;
typedef unsigned int u32;
typedef unsigned long long u64;
typedef u32 u32x2 __attribute__((ext_vector_type(2)));
typedef u32 u32x4 __attribute__((ext_vector_type(4)));
typedef float f32x4 __attribute__((ext_vector_type(4)));
typedef __attribute__((ext_vector_type(2))) __bf16 bf16x2;
typedef __attribute__((ext_vector_type(8))) __bf16 bf16x8;
#define LAS __attribute__((address_space(3)))

#define TU 86
#define TB 185
#define D1 22
#define D2 18
#define PD 7
#define GPOS 5

constexpr int DM = 640, NH = 32, DH = 192, SK = 32768, WIN = 4096, FF = 1536;
constexpr int NS = 8;
constexpr u32 WS_EPOCH = 0;
constexpr u32 WS_REC = 4096, REC = 2560;
constexpr u32 R_G1 = 0;
constexpr u32 R_G2 = 1024;
constexpr u32 R_ST = R_G2 + 768;
constexpr u32 R_G3 = 1920;
constexpr u32 R_T0 = 2432;
constexpr u32 WS_END = WS_REC + NH * NS * REC;
constexpr u32 SPIN_CAP = 1u << 19;
constexpr float LOG2E = 1.4426950408889634f;
constexpr u32 OOB = 0xfffffff0u;
constexpr int NT = 2, SC1 = 16;

__device__ __forceinline__ float bf_lo(u32 v) { return __uint_as_float(v << 16); }
__device__ __forceinline__ float bf_hi(u32 v) { return __uint_as_float(v & 0xffff0000u); }
__device__ __forceinline__ float rbf(float f) { return (float)(__bf16)f; }
__device__ __forceinline__ u32 pkbf(float f) { const bf16x2 h = {(__bf16)f, (__bf16)f}; return __builtin_bit_cast(u32, h); }
__device__ __forceinline__ float dot2(u32 a, u32 b, float c) {
  return __builtin_amdgcn_fdot2_f32_bf16(__builtin_bit_cast(bf16x2, a), __builtin_bit_cast(bf16x2, b), c, false);
}
__device__ __forceinline__ f32x4 mfma(u32x4 a, u32x4 b, f32x4 c) {
  return __builtin_amdgcn_mfma_f32_16x16x32_bf16(__builtin_bit_cast(bf16x8, a), __builtin_bit_cast(bf16x8, b), c, 0, 0, 0);
}
__device__ __forceinline__ __amdgpu_buffer_rsrc_t mkrsrc(const void* p, u32 bytes) {
  return __builtin_amdgcn_make_buffer_rsrc(const_cast<void*>(p), 0, bytes, 0x00020000);
}
template <int CTRL> __device__ __forceinline__ float dpp(float x) {
  return __builtin_bit_cast(float, __builtin_amdgcn_mov_dpp(__builtin_bit_cast(int, x), CTRL, 0xf, 0xf, true));
}
constexpr int XOR1 = 0xB1, XOR2 = 0x4E, HMIR = 0x141, ROR8 = 0x128, ROR4 = 0x124;
__device__ __forceinline__ float swap16_add(float a, float b) {
  auto s = __builtin_amdgcn_permlane16_swap(__float_as_uint(a), __float_as_uint(b), false, false);
  return __uint_as_float(s[0]) + __uint_as_float(s[1]);
}
__device__ __forceinline__ float swap32_add(float a, float b) {
  auto s = __builtin_amdgcn_permlane32_swap(__float_as_uint(a), __float_as_uint(b), false, false);
  return __uint_as_float(s[0]) + __uint_as_float(s[1]);
}
__device__ __forceinline__ float swap16_max(float a) {
  auto s = __builtin_amdgcn_permlane16_swap(__float_as_uint(a), __float_as_uint(a), false, false);
  return fmaxf(__uint_as_float(s[0]), __uint_as_float(s[1]));
}
__device__ __forceinline__ float swap32_max(float a) {
  auto s = __builtin_amdgcn_permlane32_swap(__float_as_uint(a), __float_as_uint(a), false, false);
  return fmaxf(__uint_as_float(s[0]), __uint_as_float(s[1]));
}
__device__ __forceinline__ float sum_hi3(float x) { x += dpp<ROR8>(x); x = swap16_add(x, x); return swap32_add(x, x); }
__device__ __forceinline__ float max_hi3(float x) { x = fmaxf(x, dpp<ROR8>(x)); x = swap16_max(x); return swap32_max(x); }
__device__ __forceinline__ float sum_lo3(float x) { x += dpp<XOR1>(x); x += dpp<XOR2>(x); x += dpp<HMIR>(x); return x; }
__device__ __forceinline__ float max_lo3(float x) { x = fmaxf(x, dpp<XOR1>(x)); x = fmaxf(x, dpp<XOR2>(x)); x = fmaxf(x, dpp<HMIR>(x)); return x; }
__device__ __forceinline__ float treduce4(float a0, float a1, float a2, float a3) {
  return swap32_add(swap16_add(a0, a2), swap16_add(a1, a3));
}
__device__ __forceinline__ void lds_arrive(u32* c, int l) {
  asm volatile("s_waitcnt lgkmcnt(0)" ::: "memory");
  if (l == 0) __hip_atomic_fetch_add((LAS u32*)c, 1u, __ATOMIC_RELAXED, __HIP_MEMORY_SCOPE_WORKGROUP);
  asm volatile("" ::: "memory");
}
__device__ __forceinline__ u32 lds_ticket(u32* c, int l) {
  asm volatile("s_waitcnt lgkmcnt(0)" ::: "memory");
  u32 t = 0;
  if (l == 0) t = __hip_atomic_fetch_add((LAS u32*)c, 1u, __ATOMIC_RELAXED, __HIP_MEMORY_SCOPE_WORKGROUP);
  t = __builtin_amdgcn_readfirstlane(t);
  asm volatile("" ::: "memory");
  return t;
}
__device__ __forceinline__ void lds_wait(u32* c, u32 n) {
  asm volatile("" ::: "memory");
  while (__builtin_amdgcn_readfirstlane(__hip_atomic_load((LAS u32*)c, __ATOMIC_RELAXED, __HIP_MEMORY_SCOPE_WORKGROUP)) < n) __builtin_amdgcn_s_sleep(1);
  asm volatile("" ::: "memory");
}
#define SCHED() __builtin_amdgcn_sched_barrier(0)
__device__ __forceinline__ void wg_barrier() {
  asm volatile("s_waitcnt lgkmcnt(0)" ::: "memory");
  __builtin_amdgcn_s_barrier();
  asm volatile("" ::: "memory");
}
#define STR2(x) #x
#define STR(x) STR2(x)
#define PACE() asm volatile("s_waitcnt vmcnt(" STR(PD) ")" ::: "memory")

enum { C_S1 = 0, C_PROG, C_Q, C_EPOCH, C_TZ };

__global__ void __launch_bounds__(768, 3)
fused_decode_kernel(const u16* __restrict__ x, const char* __restrict__ kc, const char* __restrict__ vc,
                    const char* __restrict__ wq, const char* __restrict__ wo, const char* __restrict__ w1,
                    const char* __restrict__ w3, const char* __restrict__ w2, u16* __restrict__ out, char* ws) {
  __shared__ __attribute__((aligned(16))) float lds[640 + 96 + 32 + 64 + 24 * 64 + 16 * 192 + 16 * 128];
  u32* xn_s = (u32*)lds;
  u32* q_s = (u32*)(lds + 640);
  float* m_s = lds + 736;
  float* l_s = lds + 744;
  u32* cnt = (u32*)(lds + 752);
  u32* h_s = (u32*)(lds + 768);
  float* s1_s = lds + 832;
  float* op_s = s1_s + 24 * 64;
  float* yp_s = op_s + 16 * 192;
  u32 t0 = (u32)__builtin_amdgcn_s_memrealtime();
  asm volatile("" : "+s"(t0));
  const int b = blockIdx.x;
  const int g = (b & 7) * 4 + (b >> 6), s = (b >> 3) & 7;
  const int tid = threadIdx.x;
  const int w = __builtin_amdgcn_readfirstlane(tid >> 6), l = tid & 63;
  const int r = l >> 3, j = l & 7;
  const __amdgpu_buffer_rsrc_t wsr = mkrsrc(ws, WS_END);
  const u32 grec = WS_REC + g * NS * REC, myrec = grec + s * REC;
  const bool is_red = (s == (g >> 2)) && !(g & 1);
  const bool red_b = (g & 2) != 0;
  const int hh = l >> 5;
  const bool useful = ((l >> 3) & 1) == hh;
  const int dcol = 8 * j + 4 * ((l >> 4) & 1);
#define MAKE_MSK() u32x4 msk; _Pragma("unroll") for (int d = 0; d < 4; ++d) msk[d] = (j >> 1) == d ? ((j & 1) ? 0xffff0000u : 0x0000ffffu) : 0u

  if (w >= 4) {
    const int sw = w - 4;
    const size_t kvbase = ((size_t)g * SK + (SK - WIN)) * (DH * 2);
    const __amdgpu_buffer_rsrc_t rk = mkrsrc(kc + kvbase, WIN * DH * 2), rv = mkrsrc(vc + kvbase, WIN * DH * 2);
    const u32 ll = (u32)((sw * 8 + s) * 3072 + r * 384 + j * 16);
#define UOFF(u) ((((u) + g) & 7) * 196608)
    u32x4 K[8][3], V[8][3];
    const u32 tz = t0 + TB + ((g & 3) * 8 + sw) * TU / 32;
#define GATE(n) do { while ((int)((u32)__builtin_amdgcn_s_memrealtime() - (tz + (n) * TU)) < 0) __builtin_amdgcn_s_sleep(1); asm volatile("" ::: "memory"); } while (0)
#define LOAD1(dst, rs, u, c) do { PACE(); dst[u][c] = __builtin_amdgcn_raw_buffer_load_b128(rs, ll + (c) * 128, UOFF(u), NT); } while (0)
#define LOAD3(dst, rs, u) do { GATE(8 + (u)); LOAD1(dst, rs, u, 0); LOAD1(dst, rs, u, 1); LOAD1(dst, rs, u, 2); SCHED(); } while (0)
    wg_barrier();
#pragma unroll
    for (int i = 0; i < 24; ++i) { if (i % 3 == 0 && i) GATE(i / 3); LOAD1(K, rk, i / 3, i % 3); if (i == GPOS * 3 + 2) lds_arrive(cnt + C_PROG, l); }
    SCHED();
    LOAD3(V, rv, 0); LOAD3(V, rv, 1); LOAD3(V, rv, 2);
    lds_wait(cnt + C_Q, 1);
    MAKE_MSK();
    u32 ppk[8];
    {
      u32x4 q[3];
      float p[8];
#pragma unroll
      for (int c = 0; c < 3; ++c) q[c] = *reinterpret_cast<const u32x4*>(q_s + c * 32 + j * 4);
#define QK(u) do { float d = 0.f; _Pragma("unroll") for (int c = 0; c < 3; ++c) _Pragma("unroll") for (int k = 0; k < 4; ++k) d = dot2(K[u][c][k], q[c][k], d); \
      p[u] = rbf(rbf(sum_lo3(d)) * 0.07216878364870322f); } while (0)
#define QK2(u) do { QK(u); QK(u + 1); asm volatile("" : "+v"(p[u]), "+v"(p[u + 1])); SCHED(); } while (0)
      QK2(0); LOAD3(V, rv, 3);
      QK2(2); LOAD3(V, rv, 4);
      QK2(4); LOAD3(V, rv, 5);
      QK2(6);
      const float mw = max_hi3(fmaxf(fmaxf(fmaxf(p[0], p[1]), fmaxf(p[2], p[3])), fmaxf(fmaxf(p[4], p[5]), fmaxf(p[6], p[7]))));
      float ls = 0.f;
#pragma unroll
      for (int u = 0; u < 8; ++u) { const float e = __builtin_amdgcn_exp2f((p[u] - mw) * LOG2E); ls += e; ppk[u] = pkbf(e); }
      ls = sum_hi3(ls);
      if (l == 0) { m_s[sw] = mw; l_s[sw] = ls; }
    }
    f32x4 acc[3];
#pragma unroll
    for (int c = 0; c < 3; ++c) acc[c] = f32x4{0.f, 0.f, 0.f, 0.f};
#define PV(u) do { const u32x4 a_ = msk & ppk[u]; _Pragma("unroll") for (int c = 0; c < 3; ++c) acc[c] = mfma(a_, V[u][c], acc[c]); \
      asm volatile("" : "+v"(acc[0]), "+v"(acc[1]), "+v"(acc[2])); SCHED(); } while (0)
    LOAD3(V, rv, 6);
    PV(0); PV(1); PV(2); PV(3);
    LOAD3(V, rv, 7);
    u32x4 WO[5];
    const int ocol = red_b ? 512 + s * 16 + 4 * (sw & 3) : s * 64 + 8 * sw + 4 * (l & 1);
    const int ok2 = 2 * ((l >> 4) & 1) + hh;
    u32 xraw;
    {
      const __amdgpu_buffer_rsrc_t rw = mkrsrc((sw < 6) ? wo + (size_t)(g * DH + sw * 32) * (DM * 2) : w2 + (size_t)(g * 48 + (sw - 6) * 32) * (DM * 2), 32 * DM * 2);
      const u32 wl = ((sw == 7) ? (r & 3) : r) * (4 * DM * 2) + j * 16;
#define WLD(i) WO[i] = __builtin_amdgcn_raw_buffer_load_b128(rw, wl + (i) * (DM * 2), s * 128, NT)
#define WLD4() do { WO[4] = __builtin_amdgcn_raw_buffer_load_b128(rw, wl, (s & 3) * (DM * 2) + (8 + (s >> 2)) * 128, NT); xraw = x[ocol + ok2]; } while (0)
      PV(4); PV(5); PV(6);
      GATE(16);
      WLD(0); WLD(1); WLD(2);
      SCHED();
      PV(7);
      if (useful) {
#pragma unroll
        for (int c = 0; c < 3; ++c) *reinterpret_cast<f32x4*>(op_s + (sw * 2 + hh) * 192 + c * 64 + dcol) = acc[c];
      }
      wg_barrier();
      __builtin_amdgcn_s_sleep(8);
      asm volatile("" ::: "memory");
      WLD(3); WLD4();
      SCHED();
    }
    u32 opk[4];
    if (sw < 6) {
      const u32 goff = grec + j * REC + R_G2 + sw * 128 + r * 16;
      const u32 soff = grec + j * REC + R_ST;
      u32x2 SS;
      u32x4 O;
      __builtin_amdgcn_s_sleep(D1);
      bool ok = false;
      u32 spins = 0;
      for (;;) {
        asm volatile("" ::: "memory");
        if (!ok) {
          SS = __builtin_amdgcn_raw_buffer_load_b64(wsr, soff, 0, SC1);
          O = __builtin_amdgcn_raw_buffer_load_b128(wsr, goff, 0, SC1);
          ok = (O[0] & O[1] & O[2] & O[3] & SS[0] & SS[1] & 1) != 0;
        }
        if (__all(ok) || ++spins > SPIN_CAP) break;
        __builtin_amdgcn_s_sleep(2);
      }
      const float ms = __uint_as_float(SS[0] & ~1u);
      const float f = __builtin_amdgcn_exp2f((ms - max_lo3(ms)) * LOG2E);
      const float sc = f * __builtin_amdgcn_rcpf(sum_lo3(f * __uint_as_float(SS[1])));
#pragma unroll
      for (int i = 0; i < 4; ++i) opk[i] = pkbf(sum_lo3(sc * __uint_as_float(O[i])));
    } else {
      const u32x4 hq = *reinterpret_cast<const u32x4*>(h_s + (sw - 6) * 32 + 4 * r);
#pragma unroll
      for (int i = 0; i < 4; ++i) opk[i] = hq[i];
    }
    {
      f32x4 y1 = {0.f, 0.f, 0.f, 0.f}, y2 = y1;
      const u32 o2 = (s & 2) ? ((s & 1) ? opk[3] : opk[2]) : ((s & 1) ? opk[1] : opk[0]);
      y2 = mfma(msk & o2, WO[4], y2);
#pragma unroll
      for (int i = 0; i < 4; ++i) y1 = mfma(msk & opk[i], WO[i], y1);
      if (useful) {
        *reinterpret_cast<f32x4*>(yp_s + (sw * 2 + hh) * 128 + dcol) = y1;
        *reinterpret_cast<f32x4*>(yp_s + (sw * 2 + hh) * 128 + 64 + dcol) = y2;
      }
    }
    wg_barrier();
    if (is_red && !(red_b && sw >= 4)) {
      const u32 goff = red_b ? WS_REC + ((l & 31) * NS + (s & 4) + hh) * REC + R_G3 + 256 + (s & 3) * 64 + sw * 16
                             : WS_REC + ((l >> 1) * NS + s) * REC + R_G3 + sw * 32 + (l & 1) * 16;
      u32x4 Y, Z;
      __builtin_amdgcn_s_sleep(D2);
      bool ok = false;
      u32 spins = 0;
      for (;;) {
        asm volatile("" ::: "memory");
        if (!ok) {
          Y = __builtin_amdgcn_raw_buffer_load_b128(wsr, goff, 0, SC1);
          Z = __builtin_amdgcn_raw_buffer_load_b128(wsr, red_b ? goff + 2 * REC : OOB, 0, SC1);
          ok = (Y[0] & Y[1] & Y[2] & Y[3] & (red_b ? Z[0] & Z[1] & Z[2] & Z[3] : 1u) & 1) != 0;
        }
        if (__all(ok) || ++spins > SPIN_CAP) break;
        __builtin_amdgcn_s_sleep(4);
      }
      const f32x4 yz = __builtin_bit_cast(f32x4, Y) + __builtin_bit_cast(f32x4, Z);
      float t = treduce4(yz[0], yz[1], yz[2], yz[3]);
      t += dpp<ROR8>(t); t += dpp<ROR4>(t); t += dpp<XOR2>(t);
      const float t1 = t + dpp<XOR1>(t);
      if ((l & (red_b ? 15 : 14)) == 0) out[ocol + ok2] = __builtin_bit_cast(u16, (__bf16)(__uint_as_float(xraw << 16) + (red_b ? t1 : t)));
      if (b == 0 && sw == 0) __builtin_amdgcn_raw_buffer_store_b32(cnt[C_EPOCH], wsr, l == 0 ? WS_EPOCH : OOB, 0, SC1);
      if (b == 0 && sw == 0) { const u32 tend = t0 + 4000 + 20 * __builtin_amdgcn_readfirstlane(cnt[C_TZ]); while ((int)((u32)__builtin_amdgcn_s_memrealtime() - tend) < 0) __builtin_amdgcn_s_sleep(1); }
    }
  } else {
    const int qd = g >> 2, wi = (g & 3) * 8 + s;
    u32x4 S1[12], xa;
    u32 xb;
    int row0[3];
#pragma unroll
    for (int k = 0; k < 3; ++k) {
      const int z = 9 * wi + (k < 2 ? 2 * w + ((wi & 1) ^ k) : 8), ln = z >> 4;
      row0[k] = (z & 15) * 40 + (k == 2 ? w * 8 : 0);
      const bool isq = ln < 12;
      const u32 rs = isq ? NH * DH * 2 : FF * 2;
      const u32 so = row0[k] * rs + (isq ? qd * 1536 + ln * 128 : qd * 384 + (ln - (ln < 15 ? 12 : 15)) * 128);
      const __amdgpu_buffer_rsrc_t rr = mkrsrc(isq ? wq : ln < 15 ? w1 : w3, DM * rs);
      const u32 vo = r * rs + j * 16;
      if (k < 2) {
#pragma unroll
        for (int i = 0; i < 5; ++i) S1[5 * k + i] = __builtin_amdgcn_raw_buffer_load_b128(rr, vo, so + 8 * i * rs, NT);
      } else {
        S1[10] = __builtin_amdgcn_raw_buffer_load_b128(rr, vo, so, NT);
        S1[11] = __builtin_amdgcn_raw_buffer_load_b128(rr, w == 0 ? vo + so + 32 * rs : OOB, 0, NT);
      }
      if (k == 0) {
        const __amdgpu_buffer_rsrc_t rx = mkrsrc(x, DM * 2);
        xa = __builtin_amdgcn_raw_buffer_load_b128(rx, l * 16, 0, 0);
        xb = __builtin_amdgcn_raw_buffer_load_b32(rx, l * 4, 1024, 0);
      }
      SCHED();
    }
    const u32 epoch = __builtin_amdgcn_raw_buffer_load_b32(wsr, WS_EPOCH, 0, SC1);
    {
      const u32x4 z = {0u, 0u, 0u, 0u};
      __builtin_amdgcn_raw_buffer_store_b128(z, wsr, (w == 3 && l < 49) ? myrec + R_G2 + l * 16 : (w == 2 && l < 32) ? myrec + R_G3 + l * 16 : OOB, 0, SC1);
    }
    if (tid < 8) cnt[tid] = 0;
    SCHED();
    wg_barrier();
    SCHED();
    {
      float xv[10];
#pragma unroll
      for (int k = 0; k < 4; ++k) { xv[2 * k] = bf_lo(xa[k]); xv[2 * k + 1] = bf_hi(xa[k]); }
      xv[8] = bf_lo(xb); xv[9] = bf_hi(xb);
      float sm = 0.f;
#pragma unroll
      for (int k = 0; k < 10; ++k) sm += xv[k];
      sm = sum_hi3(sum_lo3(sm));
      const float mean = sm * (1.f / DM);
      float sv = 0.f;
#pragma unroll
      for (int k = 0; k < 10; ++k) { const float d = xv[k] - mean; sv = fmaf(d, d, sv); }
      sv = sum_hi3(sum_lo3(sv));
      const float var = rbf(sv * (1.f / DM));
      const float mb = rbf(mean);
      const float sd = rbf(sqrtf(rbf(var + 1e-5f)));
      u32 xp[10];
#pragma unroll
      for (int k = 0; k < 10; ++k) xp[k] = pkbf(rbf(xv[k] - mb) / sd);
      *reinterpret_cast<u32x4*>(xn_s + l * 8) = u32x4{xp[0], xp[1], xp[2], xp[3]};
      *reinterpret_cast<u32x4*>(xn_s + l * 8 + 4) = u32x4{xp[4], xp[5], xp[6], xp[7]};
      *reinterpret_cast<u32x2*>(xn_s + 512 + l * 2) = u32x2{xp[8], xp[9]};
      asm volatile("s_waitcnt lgkmcnt(0)" ::: "memory");
    }
    MAKE_MSK();
    const u32 tag = epoch + 1;
    {
      u32 xr[12];
#pragma unroll
      for (int i = 0; i < 10; ++i) xr[i] = xn_s[row0[i / 5] + 8 * (i % 5) + r];
      xr[10] = xn_s[row0[2] + r];
      xr[11] = w == 0 ? xn_s[row0[2] + 32 + r] : 0u;
      f32x4 a[3];
#pragma unroll
      for (int k = 0; k < 3; ++k) a[k] = f32x4{0.f, 0.f, 0.f, 0.f};
#pragma unroll
      for (int i = 0; i < 12; ++i) a[i / 5] = mfma(msk & xr[i], S1[i], a[i / 5]);
      if (useful) {
#pragma unroll
        for (int k = 0; k < 3; ++k) *reinterpret_cast<f32x4*>(s1_s + (k < 2 ? 4 * w + 2 * ((wi & 1) ^ k) + hh : 16 + w * 2 + hh) * 64 + dcol) = a[k];
      }
      asm volatile("s_waitcnt vmcnt(0)" ::: "memory");
      if (lds_ticket(cnt + C_S1, l) == 3) {
        const int nA = 16 - ((9 * wi) & 15);
        float va = 0.f, vb = 0.f;
#pragma unroll
        for (int c = 0; c < 8; ++c) {
          const float v = s1_s[(2 * c) * 64 + l] + s1_s[(2 * c + 1) * 64 + l];
          if (c < nA) va += v; else vb += v;
        }
        float v8 = 0.f;
#pragma unroll
        for (int t = 16; t < 24; ++t) v8 += s1_s[t * 64 + l];
        if (8 < nA) va += v8; else vb += v8;
        __builtin_amdgcn_raw_buffer_store_b64(u32x2{__float_as_uint(va), tag}, wsr, myrec + R_G1 + l * 8, 0, SC1);
        __builtin_amdgcn_raw_buffer_store_b64(u32x2{__float_as_uint(vb), tag}, wsr, nA <= 8 ? myrec + R_G1 + 512 + l * 8 : OOB, 0, SC1);
        __builtin_amdgcn_raw_buffer_store_b64(u32x2{t0, tag}, wsr, l == 0 ? myrec + R_T0 : OOB, 0, SC1);
      }
    }
    {
      u32 ga[10];
      bool use3[3];
#pragma unroll
      for (int k = 0; k < 3; ++k) {
        const int fc = (g & 3) * 48 + min(l, 47);
        const int ln = w == 0 ? (g & 3) * 3 + k : 12 + 3 * min(k, 1) + (fc >> 6);
        const int col = w == 0 ? l : (fc & 63);
        const int f0 = (16 * ln) / 9, f1 = (16 * ln + 15) / 9;
        use3[k] = f0 + 2 <= f1;
#pragma unroll
        for (int i = 0; i < 3; ++i) {
          const int pw = min(f0 + i, f1);
          ga[k * 3 + i] = WS_REC + (qd * 32 + pw) * REC + R_G1 + (9 * pw < 16 * ln ? 512 : 0) + col * 8;
        }
      }
      ga[9] = grec + j * REC + R_G1;
      if (w == 1) {
        ga[9] = WS_REC + ((l & 31) * NS + (red_b ? (s & 4) + hh : s)) * REC + R_G1;
        ga[8] = ga[9] + (red_b ? 2 * REC : 0);
      }
      lds_wait(cnt + C_PROG, 8);
      if (w != 2 && (w != 1 || is_red)) {
        u32x2 G[10];
        u32x2 T0 = {0u, 0u};
        if (b == 0 && w == 0) T0 = __builtin_amdgcn_raw_buffer_load_b64(wsr, WS_REC + (l * 4 + 3) * REC + R_T0, 0, SC1);
        bool ok = false;
        u32 spins = 0;
        for (;;) {
          asm volatile("" ::: "memory");
          if (!ok) {
#pragma unroll
            for (int i = 0; i < 10; ++i) if (w == 0 || (w == 3 && i < 6) || (w == 1 && i >= 8)) G[i] = __builtin_amdgcn_raw_buffer_load_b64(wsr, ga[i], 0, SC1);
            u32 d_ = 0;
#pragma unroll
            for (int i = 0; i < 10; ++i) if (w == 0 || (w == 3 && i < 6) || (w == 1 && i >= 8)) d_ |= G[i][1] ^ tag;
            ok = (d_ == 0);
          }
          if (__all(ok) || ++spins > SPIN_CAP) break;
          __builtin_amdgcn_s_sleep(8);
        }
        float t[3];
#pragma unroll
        for (int k = 0; k < 3; ++k) t[k] = __uint_as_float(G[3 * k][0]) + __uint_as_float(G[3 * k + 1][0]) + (use3[k] ? __uint_as_float(G[3 * k + 2][0]) : 0.f);
        if (w == 0) {
#pragma unroll
          for (int k = 0; k < 3; ++k) ((u16*)q_s)[64 * k + l] = __builtin_bit_cast(u16, (__bf16)t[k]);
          { const float d = T0[1] == tag ? (float)(int)(T0[0] - t0) : 500.f;
            const float mx = max_hi3(max_lo3(d)), mn = -max_hi3(max_lo3(-d));
            if (l == 0) cnt[C_TZ] = (u32)(int)(MODE == 0 ? mx : MODE == 1 ? mx - mn : 0.f); }
          if (l == 0) cnt[C_EPOCH] = tag;
          lds_arrive(cnt + C_Q, l);
        } else if (w == 3) {
          const float gate = rbf(t[0]), up = rbf(t[1]);
          const float hv = rbf(gate / (1.f + __expf(-gate))) * up;
          h_s[l] = l < 48 ? pkbf(hv) : 0u;
        }
      }
    }
    __builtin_amdgcn_s_setprio(3);
    wg_barrier();
    {
      const int col = min(tid, 191);
      float pv[16];
#pragma unroll
      for (int i = 0; i < 16; ++i) pv[i] = op_s[i * 192 + col];
      const float mj = m_s[j];
      const float M = max_lo3(mj);
      const float fj = __builtin_amdgcn_exp2f((mj - M) * LOG2E);
      float o = 0.f;
      if (w == 3) {
        const float L = sum_lo3(l_s[j] * fj);
        o = (l & 1) ? L : M;
      } else {
#pragma unroll
        for (int i = 0; i < 8; ++i) o = fmaf(__uint_as_float(__builtin_amdgcn_readlane(__float_as_uint(fj), i)), pv[2 * i] + pv[2 * i + 1], o);
      }
      __builtin_amdgcn_raw_buffer_store_b32(__float_as_uint(o) | 1u, wsr, tid < 194 ? myrec + R_G2 + tid * 4 : OOB, 0, SC1);
    }
    wg_barrier();
    if (w < 2) {
      float y = 0.f;
#pragma unroll
      for (int i = 0; i < 16; ++i) y += yp_s[i * 128 + tid];
      __builtin_amdgcn_raw_buffer_store_b32(__float_as_uint(y) | 1u, wsr, myrec + R_G3 + tid * 4, 0, SC1);
    }
  }
}

extern "C" void kernel_launch(void* const* d_in, const int* in_sizes, int n_in, void* d_out, int out_size, void* d_ws,
                              size_t ws_size, hipStream_t stream) {
  fused_decode_kernel<<<256, 768, 0, stream>>>((const u16*)d_in[0], (const char*)d_in[1], (const char*)d_in[2],
                                               (const char*)d_in[3], (const char*)d_in[4], (const char*)d_in[6],
                                               (const char*)d_in[7], (const char*)d_in[8], (u16*)d_out, (char*)d_ws);
}
```

```cpp
#include <hip/hip_runtime.h>
#include <hip/hip_bf16.h>
#include <stdint.h>

typedef unsigned short u16;
typedef unsigned int u32;
typedef unsigned long long u64;
typedef u32 u32x2 __attribute__((ext_vector_type(2)));
typedef u32 u32x4 __attribute__((ext_vector_type(4)));
typedef float f32x4 __attribute__((ext_vector_type(4)));
typedef __attribute__((ext_vector_type(2))) __bf16 bf16x2;
typedef __attribute__((ext_vector_type(8))) __bf16 bf16x8;
#define LAS __attribute__((address_space(3)))

#define TU 85
#define TB 190
#define D1 22
#define D2 18
#define PD 7
#define GPOS 5

constexpr int DM = 640, NH = 32, DH = 192, SK = 32768, WIN = 4096, FF = 1536;
constexpr int NS = 8;
constexpr u32 WS_EPOCH = 0;
constexpr u32 WS_REC = 4096, REC = 2560;
constexpr u32 R_G1 = 0;
constexpr u32 R_G2 = 1024;
constexpr u32 R_ST = R_G2 + 768;
constexpr u32 R_G3 = 1920;
constexpr u32 WS_END = WS_REC + NH * NS * REC;
constexpr u32 SPIN_CAP = 1u << 19;
constexpr float LOG2E = 1.4426950408889634f;
constexpr u32 OOB = 0xfffffff0u;
constexpr int NT = 2, SC1 = 16;

__device__ __forceinline__ float bf_lo(u32 v) { return __uint_as_float(v << 16); }
__device__ __forceinline__ float bf_hi(u32 v) { return __uint_as_float(v & 0xffff0000u); }
__device__ __forceinline__ float rbf(float f) { return (float)(__bf16)f; }
__device__ __forceinline__ u32 pkbf(float f) { const bf16x2 h = {(__bf16)f, (__bf16)f}; return __builtin_bit_cast(u32, h); }
__device__ __forceinline__ float dot2(u32 a, u32 b, float c) {
  return __builtin_amdgcn_fdot2_f32_bf16(__builtin_bit_cast(bf16x2, a), __builtin_bit_cast(bf16x2, b), c, false);
}
__device__ __forceinline__ f32x4 mfma(u32x4 a, u32x4 b, f32x4 c) {
  return __builtin_amdgcn_mfma_f32_16x16x32_bf16(__builtin_bit_cast(bf16x8, a), __builtin_bit_cast(bf16x8, b), c, 0, 0, 0);
}
__device__ __forceinline__ __amdgpu_buffer_rsrc_t mkrsrc(const void* p, u32 bytes) {
  return __builtin_amdgcn_make_buffer_rsrc(const_cast<void*>(p), 0, bytes, 0x00020000);
}
template <int CTRL> __device__ __forceinline__ float dpp(float x) {
  return __builtin_bit_cast(float, __builtin_amdgcn_mov_dpp(__builtin_bit_cast(int, x), CTRL, 0xf, 0xf, true));
}
constexpr int XOR1 = 0xB1, XOR2 = 0x4E, HMIR = 0x141, ROR8 = 0x128, ROR4 = 0x124;
__device__ __forceinline__ float swap16_add(float a, float b) {
  auto s = __builtin_amdgcn_permlane16_swap(__float_as_uint(a), __float_as_uint(b), false, false);
  return __uint_as_float(s[0]) + __uint_as_float(s[1]);
}
__device__ __forceinline__ float swap32_add(float a, float b) {
  auto s = __builtin_amdgcn_permlane32_swap(__float_as_uint(a), __float_as_uint(b), false, false);
  return __uint_as_float(s[0]) + __uint_as_float(s[1]);
}
__device__ __forceinline__ float swap16_max(float a) {
  auto s = __builtin_amdgcn_permlane16_swap(__float_as_uint(a), __float_as_uint(a), false, false);
  return fmaxf(__uint_as_float(s[0]), __uint_as_float(s[1]));
}
__device__ __forceinline__ float swap32_max(float a) {
  auto s = __builtin_amdgcn_permlane32_swap(__float_as_uint(a), __float_as_uint(a), false, false);
  return fmaxf(__uint_as_float(s[0]), __uint_as_float(s[1]));
}
__device__ __forceinline__ float sum_hi3(float x) { x += dpp<ROR8>(x); x = swap16_add(x, x); return swap32_add(x, x); }
__device__ __forceinline__ float max_hi3(float x) { x = fmaxf(x, dpp<ROR8>(x)); x = swap16_max(x); return swap32_max(x); }
__device__ __forceinline__ float sum_lo3(float x) { x += dpp<XOR1>(x); x += dpp<XOR2>(x); x += dpp<HMIR>(x); return x; }
__device__ __forceinline__ float max_lo3(float x) { x = fmaxf(x, dpp<XOR1>(x)); x = fmaxf(x, dpp<XOR2>(x)); x = fmaxf(x, dpp<HMIR>(x)); return x; }
__device__ __forceinline__ float treduce4(float a0, float a1, float a2, float a3) {
  return swap32_add(swap16_add(a0, a2), swap16_add(a1, a3));
}
__device__ __forceinline__ void lds_arrive(u32* c, int l) {
  asm volatile("s_waitcnt lgkmcnt(0)" ::: "memory");
  if (l == 0) __hip_atomic_fetch_add((LAS u32*)c, 1u, __ATOMIC_RELAXED, __HIP_MEMORY_SCOPE_WORKGROUP);
  asm volatile("" ::: "memory");
}
__device__ __forceinline__ u32 lds_ticket(u32* c, int l) {
  asm volatile("s_waitcnt lgkmcnt(0)" ::: "memory");
  u32 t = 0;
  if (l == 0) t = __hip_atomic_fetch_add((LAS u32*)c, 1u, __ATOMIC_RELAXED, __HIP_MEMORY_SCOPE_WORKGROUP);
  t = __builtin_amdgcn_readfirstlane(t);
  asm volatile("" ::: "memory");
  return t;
}
__device__ __forceinline__ void lds_wait(u32* c, u32 n) {
  asm volatile("" ::: "memory");
  while (__builtin_amdgcn_readfirstlane(__hip_atomic_load((LAS u32*)c, __ATOMIC_RELAXED, __HIP_MEMORY_SCOPE_WORKGROUP)) < n) __builtin_amdgcn_s_sleep(1);
  asm volatile("" ::: "memory");
}
#define SCHED() __builtin_amdgcn_sched_barrier(0)
__device__ __forceinline__ void wg_barrier() {
  asm volatile("s_waitcnt lgkmcnt(0)" ::: "memory");
  __builtin_amdgcn_s_barrier();
  asm volatile("" ::: "memory");
}
#define STR2(x) #x
#define STR(x) STR2(x)
#define PACE() asm volatile("s_waitcnt vmcnt(" STR(PD) ")" ::: "memory")

enum { C_S1 = 0, C_PROG, C_Q, C_EPOCH };

__global__ void __launch_bounds__(768, 3)
fused_decode_kernel(const u16* __restrict__ x, const char* __restrict__ kc, const char* __restrict__ vc,
                    const char* __restrict__ wq, const char* __restrict__ wo, const char* __restrict__ w1,
                    const char* __restrict__ w3, const char* __restrict__ w2, u16* __restrict__ out, char* ws) {
  __shared__ __attribute__((aligned(16))) float lds[640 + 96 + 32 + 64 + 24 * 64 + 16 * 192 + 16 * 128];
  u32* xn_s = (u32*)lds;
  u32* q_s = (u32*)(lds + 640);
  float* m_s = lds + 736;
  float* l_s = lds + 744;
  u32* cnt = (u32*)(lds + 752);
  u32* h_s = (u32*)(lds + 768);
  float* s1_s = lds + 832;
  float* op_s = s1_s + 24 * 64;
  float* yp_s = op_s + 16 * 192;
  u32 t0 = (u32)__builtin_amdgcn_s_memrealtime();
  asm volatile("" : "+s"(t0));
  const int b = blockIdx.x;
  const int g = (b & 7) * 4 + (b >> 6), s = (b >> 3) & 7;
  const int tid = threadIdx.x;
  const int w = __builtin_amdgcn_readfirstlane(tid >> 6), l = tid & 63;
  const int r = l >> 3, j = l & 7;
  const __amdgpu_buffer_rsrc_t wsr = mkrsrc(ws, WS_END);
  const u32 grec = WS_REC + g * NS * REC, myrec = grec + s * REC;
  const bool is_red = (s == (g >> 2)) && !(g & 1);
  const bool red_b = (g & 2) != 0;
  const int hh = l >> 5;
  const bool useful = ((l >> 3) & 1) == hh;
  const int dcol = 8 * j + 4 * ((l >> 4) & 1);
#define MAKE_MSK() u32x4 msk; _Pragma("unroll") for (int d = 0; d < 4; ++d) msk[d] = (j >> 1) == d ? ((j & 1) ? 0xffff0000u : 0x0000ffffu) : 0u

  if (w >= 4) {
    const int sw = w - 4;
    const size_t kvbase = ((size_t)g * SK + (SK - WIN)) * (DH * 2);
    const __amdgpu_buffer_rsrc_t rk = mkrsrc(kc + kvbase, WIN * DH * 2), rv = mkrsrc(vc + kvbase, WIN * DH * 2);
    const u32 ll = (u32)((sw * 8 + s) * 3072 + r * 384 + j * 16);
#define UOFF(u) ((((u) + g) & 7) * 196608)
    u32x4 K[8][3], V[8][3];
    const u32 tz = t0 + TB + ((g & 3) * 8 + sw) * TU / 32;
#define GATE(n) do { while ((int)((u32)__builtin_amdgcn_s_memrealtime() - (tz + (n) * TU)) < 0) __builtin_amdgcn_s_sleep(1); asm volatile("" ::: "memory"); } while (0)
#define LOAD1(dst, rs, u, c) do { PACE(); dst[u][c] = __builtin_amdgcn_raw_buffer_load_b128(rs, ll + (c) * 128, UOFF(u), NT); } while (0)
#define LOAD3(dst, rs, u) do { GATE(8 + (u)); LOAD1(dst, rs, u, 0); LOAD1(dst, rs, u, 1); LOAD1(dst, rs, u, 2); SCHED(); } while (0)
    wg_barrier();
#pragma unroll
    for (int i = 0; i < 24; ++i) { if (i % 3 == 0 && i) GATE(i / 3); LOAD1(K, rk, i / 3, i % 3); if (i == GPOS * 3 + 2) lds_arrive(cnt + C_PROG, l); }
    SCHED();
    LOAD3(V, rv, 0); LOAD3(V, rv, 1); LOAD3(V, rv, 2);
    lds_wait(cnt + C_Q, 1);
    MAKE_MSK();
    u32 ppk[8];
    {
      u32x4 q[3];
      float p[8];
#pragma unroll
      for (int c = 0; c < 3; ++c) q[c] = *reinterpret_cast<const u32x4*>(q_s + c * 32 + j * 4);
#define QK(u) do { float d = 0.f; _Pragma("unroll") for (int c = 0; c < 3; ++c) _Pragma("unroll") for (int k = 0; k < 4; ++k) d = dot2(K[u][c][k], q[c][k], d); \
      p[u] = rbf(rbf(sum_lo3(d)) * 0.07216878364870322f); } while (0)
#define QK2(u) do { QK(u); QK(u + 1); asm volatile("" : "+v"(p[u]), "+v"(p[u + 1])); SCHED(); } while (0)
      QK2(0); LOAD3(V, rv, 3);
      QK2(2); LOAD3(V, rv, 4);
      QK2(4); LOAD3(V, rv, 5);
      QK2(6);
      const float mw = max_hi3(fmaxf(fmaxf(fmaxf(p[0], p[1]), fmaxf(p[2], p[3])), fmaxf(fmaxf(p[4], p[5]), fmaxf(p[6], p[7]))));
      float ls = 0.f;
#pragma unroll
      for (int u = 0; u < 8; ++u) { const float e = __builtin_amdgcn_exp2f((p[u] - mw) * LOG2E); ls += e; ppk[u] = pkbf(e); }
      ls = sum_hi3(ls);
      if (l == 0) { m_s[sw] = mw; l_s[sw] = ls; }
    }
    f32x4 acc[3];
#pragma unroll
    for (int c = 0; c < 3; ++c) acc[c] = f32x4{0.f, 0.f, 0.f, 0.f};
#define PV(u) do { const u32x4 a_ = msk & ppk[u]; _Pragma("unroll") for (int c = 0; c < 3; ++c) acc[c] = mfma(a_, V[u][c], acc[c]); \
      asm volatile("" : "+v"(acc[0]), "+v"(acc[1]), "+v"(acc[2])); SCHED(); } while (0)
    LOAD3(V, rv, 6);
    PV(0); PV(1); PV(2); PV(3);
    LOAD3(V, rv, 7);
    u32x4 WO[5];
    const int ocol = red_b ? 512 + s * 16 + 4 * (sw & 3) : s * 64 + 8 * sw + 4 * (l & 1);
    const int ok2 = 2 * ((l >> 4) & 1) + hh;
    u32 xraw;
    {
      const __amdgpu_buffer_rsrc_t rw = mkrsrc((sw < 6) ? wo + (size_t)(g * DH + sw * 32) * (DM * 2) : w2 + (size_t)(g * 48 + (sw - 6) * 32) * (DM * 2), 32 * DM * 2);
      const u32 wl = ((sw == 7) ? (r & 3) : r) * (4 * DM * 2) + j * 16;
#define WLD(i) WO[i] = __builtin_amdgcn_raw_buffer_load_b128(rw, wl + (i) * (DM * 2), s * 128, NT)
#define WLD4() do { WO[4] = __builtin_amdgcn_raw_buffer_load_b128(rw, wl, (s & 3) * (DM * 2) + (8 + (s >> 2)) * 128, NT); xraw = x[ocol + ok2]; } while (0)
      PV(4); PV(5); PV(6);
      GATE(16);
      WLD(0); WLD(1); WLD(2);
      SCHED();
      PV(7);
      if (useful) {
#pragma unroll
        for (int c = 0; c < 3; ++c) *reinterpret_cast<f32x4*>(op_s + (sw * 2 + hh) * 192 + c * 64 + dcol) = acc[c];
      }
      wg_barrier();
      __builtin_amdgcn_s_sleep(8);
      asm volatile("" ::: "memory");
      WLD(3); WLD4();
      SCHED();
    }
    u32 opk[4];
    if (sw < 6) {
      const u32 goff = grec + j * REC + R_G2 + sw * 128 + r * 16;
      const u32 soff = grec + j * REC + R_ST;
      u32x2 SS;
      u32x4 O;
      __builtin_amdgcn_s_sleep(D1);
      bool ok = false;
      u32 spins = 0;
      for (;;) {
        asm volatile("" ::: "memory");
        if (!ok) {
          SS = __builtin_amdgcn_raw_buffer_load_b64(wsr, soff, 0, SC1);
          O = __builtin_amdgcn_raw_buffer_load_b128(wsr, goff, 0, SC1);
          ok = (O[0] & O[1] & O[2] & O[3] & SS[0] & SS[1] & 1) != 0;
        }
        if (__all(ok) || ++spins > SPIN_CAP) break;
        __builtin_amdgcn_s_sleep(2);
      }
      const float ms = __uint_as_float(SS[0] & ~1u);
      const float f = __builtin_amdgcn_exp2f((ms - max_lo3(ms)) * LOG2E);
      const float sc = f * __builtin_amdgcn_rcpf(sum_lo3(f * __uint_as_float(SS[1])));
#pragma unroll
      for (int i = 0; i < 4; ++i) opk[i] = pkbf(sum_lo3(sc * __uint_as_float(O[i])));
    } else {
      const u32x4 hq = *reinterpret_cast<const u32x4*>(h_s + (sw - 6) * 32 + 4 * r);
#pragma unroll
      for (int i = 0; i < 4; ++i) opk[i] = hq[i];
    }
    {
      f32x4 y1 = {0.f, 0.f, 0.f, 0.f}, y2 = y1;
      const u32 o2 = (s & 2) ? ((s & 1) ? opk[3] : opk[2]) : ((s & 1) ? opk[1] : opk[0]);
      y2 = mfma(msk & o2, WO[4], y2);
#pragma unroll
      for (int i = 0; i < 4; ++i) y1 = mfma(msk & opk[i], WO[i], y1);
      if (useful) {
        *reinterpret_cast<f32x4*>(yp_s + (sw * 2 + hh) * 128 + dcol) = y1;
        *reinterpret_cast<f32x4*>(yp_s + (sw * 2 + hh) * 128 + 64 + dcol) = y2;
      }
    }
    wg_barrier();
    if (is_red && !(red_b && sw >= 4)) {
      const u32 goff = red_b ? WS_REC + ((l & 31) * NS + (s & 4) + hh) * REC + R_G3 + 256 + (s & 3) * 64 + sw * 16
                             : WS_REC + ((l >> 1) * NS + s) * REC + R_G3 + sw * 32 + (l & 1) * 16;
      u32x4 Y, Z;
      __builtin_amdgcn_s_sleep(D2);
      bool ok = false;
      u32 spins = 0;
      for (;;) {
        asm volatile("" ::: "memory");
        if (!ok) {
          Y = __builtin_amdgcn_raw_buffer_load_b128(wsr, goff, 0, SC1);
          Z = __builtin_amdgcn_raw_buffer_load_b128(wsr, red_b ? goff + 2 * REC : OOB, 0, SC1);
          ok = (Y[0] & Y[1] & Y[2] & Y[3] & (red_b ? Z[0] & Z[1] & Z[2] & Z[3] : 1u) & 1) != 0;
        }
        if (__all(ok) || ++spins > SPIN_CAP) break;
        __builtin_amdgcn_s_sleep(4);
      }
      const f32x4 yz = __builtin_bit_cast(f32x4, Y) + __builtin_bit_cast(f32x4, Z);
      float t = treduce4(yz[0], yz[1], yz[2], yz[3]);
      t += dpp<ROR8>(t); t += dpp<ROR4>(t); t += dpp<XOR2>(t);
      const float t1 = t + dpp<XOR1>(t);
      if ((l & (red_b ? 15 : 14)) == 0) out[ocol + ok2] = __builtin_bit_cast(u16, (__bf16)(__uint_as_float(xraw << 16) + (red_b ? t1 : t)));
      if (b == 0 && sw == 0) __builtin_amdgcn_raw_buffer_store_b32(cnt[C_EPOCH], wsr, l == 0 ? WS_EPOCH : OOB, 0, SC1);
    }
  } else {
    const int qd = g >> 2, wi = (g & 3) * 8 + s;
    u32x4 S1[12], xa;
    u32 xb;
    int row0[3];
#pragma unroll
    for (int k = 0; k < 3; ++k) {
      const int z = 9 * wi + (k < 2 ? 2 * w + ((wi & 1) ^ k) : 8), ln = z >> 4;
      row0[k] = (z & 15) * 40 + (k == 2 ? w * 8 : 0);
      const bool isq = ln < 12;
      const u32 rs = isq ? NH * DH * 2 : FF * 2;
      const u32 so = row0[k] * rs + (isq ? qd * 1536 + ln * 128 : qd * 384 + (ln - (ln < 15 ? 12 : 15)) * 128);
      const __amdgpu_buffer_rsrc_t rr = mkrsrc(isq ? wq : ln < 15 ? w1 : w3, DM * rs);
      const u32 vo = r * rs + j * 16;
      if (k < 2) {
#pragma unroll
        for (int i = 0; i < 5; ++i) S1[5 * k + i] = __builtin_amdgcn_raw_buffer_load_b128(rr, vo, so + 8 * i * rs, NT);
      } else {
        S1[10] = __builtin_amdgcn_raw_buffer_load_b128(rr, vo, so, NT);
        S1[11] = __builtin_amdgcn_raw_buffer_load_b128(rr, w == 0 ? vo + so + 32 * rs : OOB, 0, NT);
      }
      if (k == 0) {
        const __amdgpu_buffer_rsrc_t rx = mkrsrc(x, DM * 2);
        xa = __builtin_amdgcn_raw_buffer_load_b128(rx, l * 16, 0, 0);
        xb = __builtin_amdgcn_raw_buffer_load_b32(rx, l * 4, 1024, 0);
      }
      SCHED();
    }
    const u32 epoch = __builtin_amdgcn_raw_buffer_load_b32(wsr, WS_EPOCH, 0, SC1);
    {
      const u32x4 z = {0u, 0u, 0u, 0u};
      __builtin_amdgcn_raw_buffer_store_b128(z, wsr, (w == 3 && l < 49) ? myrec + R_G2 + l * 16 : (w == 2 && l < 32) ? myrec + R_G3 + l * 16 : OOB, 0, SC1);
    }
    if (tid < 8) cnt[tid] = 0;
    SCHED();
    wg_barrier();
    SCHED();
    {
      float xv[10];
#pragma unroll
      for (int k = 0; k < 4; ++k) { xv[2 * k] = bf_lo(xa[k]); xv[2 * k + 1] = bf_hi(xa[k]); }
      xv[8] = bf_lo(xb); xv[9] = bf_hi(xb);
      float sm = 0.f;
#pragma unroll
      for (int k = 0; k < 10; ++k) sm += xv[k];
      sm = sum_hi3(sum_lo3(sm));
      const float mean = sm * (1.f / DM);
      float sv = 0.f;
#pragma unroll
      for (int k = 0; k < 10; ++k) { const float d = xv[k] - mean; sv = fmaf(d, d, sv); }
      sv = sum_hi3(sum_lo3(sv));
      const float var = rbf(sv * (1.f / DM));
      const float mb = rbf(mean);
      const float sd = rbf(sqrtf(rbf(var + 1e-5f)));
      u32 xp[10];
#pragma unroll
      for (int k = 0; k < 10; ++k) xp[k] = pkbf(rbf(xv[k] - mb) / sd);
      *reinterpret_cast<u32x4*>(xn_s + l * 8) = u32x4{xp[0], xp[1], xp[2], xp[3]};
      *reinterpret_cast<u32x4*>(xn_s + l * 8 + 4) = u32x4{xp[4], xp[5], xp[6], xp[7]};
      *reinterpret_cast<u32x2*>(xn_s + 512 + l * 2) = u32x2{xp[8], xp[9]};
      asm volatile("s_waitcnt lgkmcnt(0)" ::: "memory");
    }
    MAKE_MSK();
    const u32 tag = epoch + 1;
    {
      u32 xr[12];
#pragma unroll
      for (int i = 0; i < 10; ++i) xr[i] = xn_s[row0[i / 5] + 8 * (i % 5) + r];
      xr[10] = xn_s[row0[2] + r];
      xr[11] = w == 0 ? xn_s[row0[2] + 32 + r] : 0u;
      f32x4 a[3];
#pragma unroll
      for (int k = 0; k < 3; ++k) a[k] = f32x4{0.f, 0.f, 0.f, 0.f};
#pragma unroll
      for (int i = 0; i < 12; ++i) a[i / 5] = mfma(msk & xr[i], S1[i], a[i / 5]);
      if (useful) {
#pragma unroll
        for (int k = 0; k < 3; ++k) *reinterpret_cast<f32x4*>(s1_s + (k < 2 ? 4 * w + 2 * ((wi & 1) ^ k) + hh : 16 + w * 2 + hh) * 64 + dcol) = a[k];
      }
      asm volatile("s_waitcnt vmcnt(0)" ::: "memory");
      if (lds_ticket(cnt + C_S1, l) == 3) {
        const int nA = 16 - ((9 * wi) & 15);
        float va = 0.f, vb = 0.f;
#pragma unroll
        for (int c = 0; c < 8; ++c) {
          const float v = s1_s[(2 * c) * 64 + l] + s1_s[(2 * c + 1) * 64 + l];
          if (c < nA) va += v; else vb += v;
        }
        float v8 = 0.f;
#pragma unroll
        for (int t = 16; t < 24; ++t) v8 += s1_s[t * 64 + l];
        if (8 < nA) va += v8; else vb += v8;
        __builtin_amdgcn_raw_buffer_store_b64(u32x2{__float_as_uint(va), tag}, wsr, myrec + R_G1 + l * 8, 0, SC1);
        __builtin_amdgcn_raw_buffer_store_b64(u32x2{__float_as_uint(vb), tag}, wsr, nA <= 8 ? myrec + R_G1 + 512 + l * 8 : OOB, 0, SC1);
      }
    }
    {
      u32 ga[10];
      bool use3[3];
#pragma unroll
      for (int k = 0; k < 3; ++k) {
        const int fc = (g & 3) * 48 + min(l, 47);
        const int ln = w == 0 ? (g & 3) * 3 + k : 12 + 3 * min(k, 1) + (fc >> 6);
        const int col = w == 0 ? l : (fc & 63);
        const int f0 = (16 * ln) / 9, f1 = (16 * ln + 15) / 9;
        use3[k] = f0 + 2 <= f1;
#pragma unroll
        for (int i = 0; i < 3; ++i) {
          const int pw = min(f0 + i, f1);
          ga[k * 3 + i] = WS_REC + (qd * 32 + pw) * REC + R_G1 + (9 * pw < 16 * ln ? 512 : 0) + col * 8;
        }
      }
      ga[9] = grec + j * REC + R_G1;
      if (w == 1) {
        ga[9] = WS_REC + ((l & 31) * NS + (red_b ? (s & 4) + hh : s)) * REC + R_G1;
        ga[8] = ga[9] + (red_b ? 2 * REC : 0);
      }
      lds_wait(cnt + C_PROG, 8);
      if (w != 2 && (w != 1 || is_red)) {
        u32x2 G[10];
        bool ok = false;
        u32 spins = 0;
        for (;;) {
          asm volatile("" ::: "memory");
          if (!ok) {
#pragma unroll
            for (int i = 0; i < 10; ++i) if (w == 0 || (w == 3 && i < 6) || (w == 1 && i >= 8)) G[i] = __builtin_amdgcn_raw_buffer_load_b64(wsr, ga[i], 0, SC1);
            u32 d_ = 0;
#pragma unroll
            for (int i = 0; i < 10; ++i) if (w == 0 || (w == 3 && i < 6) || (w == 1 && i >= 8)) d_ |= G[i][1] ^ tag;
            ok = (d_ == 0);
          }
          if (__all(ok) || ++spins > SPIN_CAP) break;
          __builtin_amdgcn_s_sleep(8);
        }
        float t[3];
#pragma unroll
        for (int k = 0; k < 3; ++k) t[k] = __uint_as_float(G[3 * k][0]) + __uint_as_float(G[3 * k + 1][0]) + (use3[k] ? __uint_as_float(G[3 * k + 2][0]) : 0.f);
        if (w == 0) {
#pragma unroll
          for (int k = 0; k < 3; ++k) ((u16*)q_s)[64 * k + l] = __builtin_bit_cast(u16, (__bf16)t[k]);
          if (l == 0) cnt[C_EPOCH] = tag;
          lds_arrive(cnt + C_Q, l);
        } else if (w == 3) {
          const float gate = rbf(t[0]), up = rbf(t[1]);
          const float hv = rbf(gate / (1.f + __expf(-gate))) * up;
          h_s[l] = l < 48 ? pkbf(hv) : 0u;
        }
      }
    }
    __builtin_amdgcn_s_setprio(3);
    wg_barrier();
    {
      const int col = min(tid, 191);
      float pv[16];
#pragma unroll
      for (int i = 0; i < 16; ++i) pv[i] = op_s[i * 192 + col];
      const float mj = m_s[j];
      const float M = max_lo3(mj);
      const float fj = __builtin_amdgcn_exp2f((mj - M) * LOG2E);
      float o = 0.f;
      if (w == 3) {
        const float L = sum_lo3(l_s[j] * fj);
        o = (l & 1) ? L : M;
      } else {
#pragma unroll
        for (int i = 0; i < 8; ++i) o = fmaf(__uint_as_float(__builtin_amdgcn_readlane(__float_as_uint(fj), i)), pv[2 * i] + pv[2 * i + 1], o);
      }
      __builtin_amdgcn_raw_buffer_store_b32(__float_as_uint(o) | 1u, wsr, tid < 194 ? myrec + R_G2 + tid * 4 : OOB, 0, SC1);
    }
    wg_barrier();
    if (w < 2) {
      float y = 0.f;
#pragma unroll
      for (int i = 0; i < 16; ++i) y += yp_s[i * 128 + tid];
      __builtin_amdgcn_raw_buffer_store_b32(__float_as_uint(y) | 1u, wsr, myrec + R_G3 + tid * 4, 0, SC1);
    }
  }
}

extern "C" void kernel_launch(void* const* d_in, const int* in_sizes, int n_in, void* d_out, int out_size, void* d_ws,
                              size_t ws_size, hipStream_t stream) {
  fused_decode_kernel<<<256, 768, 0, stream>>>((const u16*)d_in[0], (const char*)d_in[1], (const char*)d_in[2],
                                               (const char*)d_in[3], (const char*)d_in[4], (const char*)d_in[6],
                                               (const char*)d_in[7], (const char*)d_in[8], (u16*)d_out, (char*)d_ws);
}
```

```cpp
#define PH 1
#include <hip/hip_runtime.h>
#include <hip/hip_bf16.h>
#include <stdint.h>

typedef unsigned short u16;
typedef unsigned int u32;
typedef unsigned long long u64;
typedef u32 u32x2 __attribute__((ext_vector_type(2)));
typedef u32 u32x4 __attribute__((ext_vector_type(4)));
typedef float f32x4 __attribute__((ext_vector_type(4)));
typedef __attribute__((ext_vector_type(2))) __bf16 bf16x2;
typedef __attribute__((ext_vector_type(8))) __bf16 bf16x8;
#define LAS __attribute__((address_space(3)))

#define TU 85
#define TB 190
#define D1 22
#define D2 18
#define PD 7
#define GPOS 5

constexpr int DM = 640, NH = 32, DH = 192, SK = 32768, WIN = 4096, FF = 1536;
constexpr int NS = 8;
constexpr u32 WS_EPOCH = 0;
constexpr u32 WS_REC = 4096, REC = 2560;
constexpr u32 R_G1 = 0;
constexpr u32 R_G2 = 1024;
constexpr u32 R_ST = R_G2 + 768;
constexpr u32 R_G3 = 1920;
constexpr u32 WS_END = WS_REC + NH * NS * REC;
constexpr u32 SPIN_CAP = 1u << 19;
constexpr float LOG2E = 1.4426950408889634f;
constexpr u32 OOB = 0xfffffff0u;
constexpr int NT = 2, SC1 = 16;

__device__ __forceinline__ float bf_lo(u32 v) { return __uint_as_float(v << 16); }
__device__ __forceinline__ float bf_hi(u32 v) { return __uint_as_float(v & 0xffff0000u); }
__device__ __forceinline__ float rbf(float f) { return (float)(__bf16)f; }
__device__ __forceinline__ u32 pkbf(float f) { const bf16x2 h = {(__bf16)f, (__bf16)f}; return __builtin_bit_cast(u32, h); }
__device__ __forceinline__ float dot2(u32 a, u32 b, float c) {
  return __builtin_amdgcn_fdot2_f32_bf16(__builtin_bit_cast(bf16x2, a), __builtin_bit_cast(bf16x2, b), c, false);
}
__device__ __forceinline__ f32x4 mfma(u32x4 a, u32x4 b, f32x4 c) {
  return __builtin_amdgcn_mfma_f32_16x16x32_bf16(__builtin_bit_cast(bf16x8, a), __builtin_bit_cast(bf16x8, b), c, 0, 0, 0);
}
__device__ __forceinline__ __amdgpu_buffer_rsrc_t mkrsrc(const void* p, u32 bytes) {
  return __builtin_amdgcn_make_buffer_rsrc(const_cast<void*>(p), 0, bytes, 0x00020000);
}
template <int CTRL> __device__ __forceinline__ float dpp(float x) {
  return __builtin_bit_cast(float, __builtin_amdgcn_mov_dpp(__builtin_bit_cast(int, x), CTRL, 0xf, 0xf, true));
}
constexpr int XOR1 = 0xB1, XOR2 = 0x4E, HMIR = 0x141, ROR8 = 0x128, ROR4 = 0x124;
__device__ __forceinline__ float swap16_add(float a, float b) {
  auto s = __builtin_amdgcn_permlane16_swap(__float_as_uint(a), __float_as_uint(b), false, false);
  return __uint_as_float(s[0]) + __uint_as_float(s[1]);
}
__device__ __forceinline__ float swap32_add(float a, float b) {
  auto s = __builtin_amdgcn_permlane32_swap(__float_as_uint(a), __float_as_uint(b), false, false);
  return __uint_as_float(s[0]) + __uint_as_float(s[1]);
}
__device__ __forceinline__ float swap16_max(float a) {
  auto s = __builtin_amdgcn_permlane16_swap(__float_as_uint(a), __float_as_uint(a), false, false);
  return fmaxf(__uint_as_float(s[0]), __uint_as_float(s[1]));
}
__device__ __forceinline__ float swap32_max(float a) {
  auto s = __builtin_amdgcn_permlane32_swap(__float_as_uint(a), __float_as_uint(a), false, false);
  return fmaxf(__uint_as_float(s[0]), __uint_as_float(s[1]));
}
__device__ __forceinline__ float sum_hi3(float x) { x += dpp<ROR8>(x); x = swap16_add(x, x); return swap32_add(x, x); }
__device__ __forceinline__ float max_hi3(float x) { x = fmaxf(x, dpp<ROR8>(x)); x = swap16_max(x); return swap32_max(x); }
__device__ __forceinline__ float sum_lo3(float x) { x += dpp<XOR1>(x); x += dpp<XOR2>(x); x += dpp<HMIR>(x); return x; }
__device__ __forceinline__ float max_lo3(float x) { x = fmaxf(x, dpp<XOR1>(x)); x = fmaxf(x, dpp<XOR2>(x)); x = fmaxf(x, dpp<HMIR>(x)); return x; }
__device__ __forceinline__ float treduce4(float a0, float a1, float a2, float a3) {
  return swap32_add(swap16_add(a0, a2), swap16_add(a1, a3));
}
__device__ __forceinline__ void lds_arrive(u32* c, int l) {
  asm volatile("s_waitcnt lgkmcnt(0)" ::: "memory");
  if (l == 0) __hip_atomic_fetch_add((LAS u32*)c, 1u, __ATOMIC_RELAXED, __HIP_MEMORY_SCOPE_WORKGROUP);
  asm volatile("" ::: "memory");
}
__device__ __forceinline__ u32 lds_ticket(u32* c, int l) {
  asm volatile("s_waitcnt lgkmcnt(0)" ::: "memory");
  u32 t = 0;
  if (l == 0) t = __hip_atomic_fetch_add((LAS u32*)c, 1u, __ATOMIC_RELAXED, __HIP_MEMORY_SCOPE_WORKGROUP);
  t = __builtin_amdgcn_readfirstlane(t);
  asm volatile("" ::: "memory");
  return t;
}
__device__ __forceinline__ void lds_wait(u32* c, u32 n) {
  asm volatile("" ::: "memory");
  while (__builtin_amdgcn_readfirstlane(__hip_atomic_load((LAS u32*)c, __ATOMIC_RELAXED, __HIP_MEMORY_SCOPE_WORKGROUP)) < n) __builtin_amdgcn_s_sleep(1);
  asm volatile("" ::: "memory");
}
#define SCHED() __builtin_amdgcn_sched_barrier(0)
__device__ __forceinline__ void wg_barrier() {
  asm volatile("s_waitcnt lgkmcnt(0)" ::: "memory");
  __builtin_amdgcn_s_barrier();
  asm volatile("" ::: "memory");
}
#define STR2(x) #x
#define STR(x) STR2(x)
#define PACE() asm volatile("s_waitcnt vmcnt(" STR(PD) ")" ::: "memory")

enum { C_S1 = 0, C_PROG, C_Q, C_EPOCH };

__global__ void __launch_bounds__(768, 3)
fused_decode_kernel(const u16* __restrict__ x, const char* __restrict__ kc, const char* __restrict__ vc,
                    const char* __restrict__ wq, const char* __restrict__ wo, const char* __restrict__ w1,
                    const char* __restrict__ w3, const char* __restrict__ w2, u16* __restrict__ out, char* ws) {
  __shared__ __attribute__((aligned(16))) float lds[640 + 96 + 32 + 64 + 24 * 64 + 16 * 192 + 16 * 128];
  u32* xn_s = (u32*)lds;
  u32* q_s = (u32*)(lds + 640);
  float* m_s = lds + 736;
  float* l_s = lds + 744;
  u32* cnt = (u32*)(lds + 752);
  u32* h_s = (u32*)(lds + 768);
  float* s1_s = lds + 832;
  float* op_s = s1_s + 24 * 64;
  float* yp_s = op_s + 16 * 192;
  u32 t0 = (u32)__builtin_amdgcn_s_memrealtime();
  asm volatile("" : "+s"(t0));
  const int b = blockIdx.x;
  const int g = (b & 7) * 4 + (b >> 6), s = (b >> 3) & 7;
  const int tid = threadIdx.x;
  const int w = __builtin_amdgcn_readfirstlane(tid >> 6), l = tid & 63;
  const int r = l >> 3, j = l & 7;
  const __amdgpu_buffer_rsrc_t wsr = mkrsrc(ws, WS_END);
  const u32 grec = WS_REC + g * NS * REC, myrec = grec + s * REC;
  const bool is_red = (s == (g >> 2)) && !(g & 1);
  const bool red_b = (g & 2) != 0;
  const int hh = l >> 5;
  const bool useful = ((l >> 3) & 1) == hh;
  const int dcol = 8 * j + 4 * ((l >> 4) & 1);
#define MAKE_MSK() u32x4 msk; _Pragma("unroll") for (int d = 0; d < 4; ++d) msk[d] = (j >> 1) == d ? ((j & 1) ? 0xffff0000u : 0x0000ffffu) : 0u

  if (w >= 4) {
    const int sw = w - 4;
    const size_t kvbase = ((size_t)g * SK + (SK - WIN)) * (DH * 2);
    const __amdgpu_buffer_rsrc_t rk = mkrsrc(kc + kvbase, WIN * DH * 2), rv = mkrsrc(vc + kvbase, WIN * DH * 2);
    const u32 ll = (u32)((sw * 8 + s) * 3072 + r * 384 + j * 16);
#define UOFF(u) ((((u) + g) & 7) * 196608)
    u32x4 K[8][3], V[8][3];
    u32 tph = 0;
    const u32 tz = t0 + TB + ((g & 3) * 8 + sw) * TU / 32;
#define GATE(n) do { while ((int)((u32)__builtin_amdgcn_s_memrealtime() - (tz + (n) * TU)) < 0) __builtin_amdgcn_s_sleep(1); asm volatile("" ::: "memory"); } while (0)
#define LOAD1(dst, rs, u, c) do { PACE(); dst[u][c] = __builtin_amdgcn_raw_buffer_load_b128(rs, ll + (c) * 128, UOFF(u), NT); } while (0)
#define LOAD3(dst, rs, u) do { GATE(8 + (u)); LOAD1(dst, rs, u, 0); LOAD1(dst, rs, u, 1); LOAD1(dst, rs, u, 2); SCHED(); } while (0)
    wg_barrier();
    if (PH == 0) tph = (u32)__builtin_amdgcn_s_memrealtime();
#pragma unroll
    for (int i = 0; i < 24; ++i) { if (i % 3 == 0 && i) GATE(i / 3); LOAD1(K, rk, i / 3, i % 3); if (i == GPOS * 3 + 2) lds_arrive(cnt + C_PROG, l); }
    SCHED();
    LOAD3(V, rv, 0); LOAD3(V, rv, 1); LOAD3(V, rv, 2);
    lds_wait(cnt + C_Q, 1);
    MAKE_MSK();
    u32 ppk[8];
    {
      u32x4 q[3];
      float p[8];
#pragma unroll
      for (int c = 0; c < 3; ++c) q[c] = *reinterpret_cast<const u32x4*>(q_s + c * 32 + j * 4);
#define QK(u) do { float d = 0.f; _Pragma("unroll") for (int c = 0; c < 3; ++c) _Pragma("unroll") for (int k = 0; k < 4; ++k) d = dot2(K[u][c][k], q[c][k], d); \
      p[u] = rbf(rbf(sum_lo3(d)) * 0.07216878364870322f); } while (0)
#define QK2(u) do { QK(u); QK(u + 1); asm volatile("" : "+v"(p[u]), "+v"(p[u + 1])); SCHED(); } while (0)
      QK2(0); LOAD3(V, rv, 3);
      QK2(2); LOAD3(V, rv, 4);
      QK2(4); LOAD3(V, rv, 5);
      QK2(6);
      const float mw = max_hi3(fmaxf(fmaxf(fmaxf(p[0], p[1]), fmaxf(p[2], p[3])), fmaxf(fmaxf(p[4], p[5]), fmaxf(p[6], p[7]))));
      float ls = 0.f;
#pragma unroll
      for (int u = 0; u < 8; ++u) { const float e = __builtin_amdgcn_exp2f((p[u] - mw) * LOG2E); ls += e; ppk[u] = pkbf(e); }
      ls = sum_hi3(ls);
      if (l == 0) { m_s[sw] = mw; l_s[sw] = ls; }
    }
    f32x4 acc[3];
#pragma unroll
    for (int c = 0; c < 3; ++c) acc[c] = f32x4{0.f, 0.f, 0.f, 0.f};
#define PV(u) do { const u32x4 a_ = msk & ppk[u]; _Pragma("unroll") for (int c = 0; c < 3; ++c) acc[c] = mfma(a_, V[u][c], acc[c]); \
      asm volatile("" : "+v"(acc[0]), "+v"(acc[1]), "+v"(acc[2])); SCHED(); } while (0)
    LOAD3(V, rv, 6);
    PV(0); PV(1); PV(2); PV(3);
    LOAD3(V, rv, 7);
    u32x4 WO[5];
    const int ocol = red_b ? 512 + s * 16 + 4 * (sw & 3) : s * 64 + 8 * sw + 4 * (l & 1);
    const int ok2 = 2 * ((l >> 4) & 1) + hh;
    u32 xraw;
    {
      const __amdgpu_buffer_rsrc_t rw = mkrsrc((sw < 6) ? wo + (size_t)(g * DH + sw * 32) * (DM * 2) : w2 + (size_t)(g * 48 + (sw - 6) * 32) * (DM * 2), 32 * DM * 2);
      const u32 wl = ((sw == 7) ? (r & 3) : r) * (4 * DM * 2) + j * 16;
#define WLD(i) WO[i] = __builtin_amdgcn_raw_buffer_load_b128(rw, wl + (i) * (DM * 2), s * 128, NT)
#define WLD4() do { WO[4] = __builtin_amdgcn_raw_buffer_load_b128(rw, wl, (s & 3) * (DM * 2) + (8 + (s >> 2)) * 128, NT); xraw = x[ocol + ok2]; } while (0)
      PV(4); PV(5); PV(6);
      GATE(16);
      WLD(0); WLD(1); WLD(2);
      SCHED();
      PV(7);
      if (useful) {
#pragma unroll
        for (int c = 0; c < 3; ++c) *reinterpret_cast<f32x4*>(op_s + (sw * 2 + hh) * 192 + c * 64 + dcol) = acc[c];
      }
      wg_barrier();
      if (PH == 1) tph = (u32)__builtin_amdgcn_s_memrealtime();
      __builtin_amdgcn_s_sleep(8);
      asm volatile("" ::: "memory");
      WLD(3); WLD4();
      SCHED();
    }
    u32 opk[4];
    if (sw < 6) {
      const u32 goff = grec + j * REC + R_G2 + sw * 128 + r * 16;
      const u32 soff = grec + j * REC + R_ST;
      u32x2 SS;
      u32x4 O;
      __builtin_amdgcn_s_sleep(D1);
      bool ok = false;
      u32 spins = 0;
      for (;;) {
        asm volatile("" ::: "memory");
        if (!ok) {
          SS = __builtin_amdgcn_raw_buffer_load_b64(wsr, soff, 0, SC1);
          O = __builtin_amdgcn_raw_buffer_load_b128(wsr, goff, 0, SC1);
          ok = (O[0] & O[1] & O[2] & O[3] & SS[0] & SS[1] & 1) != 0;
        }
        if (__all(ok) || ++spins > SPIN_CAP) break;
        __builtin_amdgcn_s_sleep(2);
      }
      const float ms = __uint_as_float(SS[0] & ~1u);
      const float f = __builtin_amdgcn_exp2f((ms - max_lo3(ms)) * LOG2E);
      const float sc = f * __builtin_amdgcn_rcpf(sum_lo3(f * __uint_as_float(SS[1])));
#pragma unroll
      for (int i = 0; i < 4; ++i) opk[i] = pkbf(sum_lo3(sc * __uint_as_float(O[i])));
    } else {
      const u32x4 hq = *reinterpret_cast<const u32x4*>(h_s + (sw - 6) * 32 + 4 * r);
#pragma unroll
      for (int i = 0; i < 4; ++i) opk[i] = hq[i];
    }
    {
      f32x4 y1 = {0.f, 0.f, 0.f, 0.f}, y2 = y1;
      const u32 o2 = (s & 2) ? ((s & 1) ? opk[3] : opk[2]) : ((s & 1) ? opk[1] : opk[0]);
      y2 = mfma(msk & o2, WO[4], y2);
#pragma unroll
      for (int i = 0; i < 4; ++i) y1 = mfma(msk & opk[i], WO[i], y1);
      if (useful) {
        *reinterpret_cast<f32x4*>(yp_s + (sw * 2 + hh) * 128 + dcol) = y1;
        *reinterpret_cast<f32x4*>(yp_s + (sw * 2 + hh) * 128 + 64 + dcol) = y2;
      }
    }
    wg_barrier();
    if (PH == 2) tph = (u32)__builtin_amdgcn_s_memrealtime();
    if (is_red && !(red_b && sw >= 4)) {
      const u32 goff = red_b ? WS_REC + ((l & 31) * NS + (s & 4) + hh) * REC + R_G3 + 256 + (s & 3) * 64 + sw * 16
                             : WS_REC + ((l >> 1) * NS + s) * REC + R_G3 + sw * 32 + (l & 1) * 16;
      u32x4 Y, Z;
      __builtin_amdgcn_s_sleep(D2);
      bool ok = false;
      u32 spins = 0;
      for (;;) {
        asm volatile("" ::: "memory");
        if (!ok) {
          Y = __builtin_amdgcn_raw_buffer_load_b128(wsr, goff, 0, SC1);
          Z = __builtin_amdgcn_raw_buffer_load_b128(wsr, red_b ? goff + 2 * REC : OOB, 0, SC1);
          ok = (Y[0] & Y[1] & Y[2] & Y[3] & (red_b ? Z[0] & Z[1] & Z[2] & Z[3] : 1u) & 1) != 0;
        }
        if (__all(ok) || ++spins > SPIN_CAP) break;
        __builtin_amdgcn_s_sleep(4);
      }
      const f32x4 yz = __builtin_bit_cast(f32x4, Y) + __builtin_bit_cast(f32x4, Z);
      float t = treduce4(yz[0], yz[1], yz[2], yz[3]);
      t += dpp<ROR8>(t); t += dpp<ROR4>(t); t += dpp<XOR2>(t);
      const float t1 = t + dpp<XOR1>(t);
      if ((l & (red_b ? 15 : 14)) == 0) out[ocol + ok2] = __builtin_bit_cast(u16, (__bf16)(__uint_as_float(xraw << 16) + (red_b ? t1 : t)));
      if (b == 0 && sw == 0) __builtin_amdgcn_raw_buffer_store_b32(cnt[C_EPOCH], wsr, l == 0 ? WS_EPOCH : OOB, 0, SC1);
      if (b == 0 && sw == 0) { if (PH == 3) tph = (u32)__builtin_amdgcn_s_memrealtime(); const u32 tend = t0 + 3000 + 10 * (tph - t0); while ((int)((u32)__builtin_amdgcn_s_memrealtime() - tend) < 0) __builtin_amdgcn_s_sleep(1); }
    }
  } else {
    const int qd = g >> 2, wi = (g & 3) * 8 + s;
    u32x4 S1[12], xa;
    u32 xb;
    int row0[3];
#pragma unroll
    for (int k = 0; k < 3; ++k) {
      const int z = 9 * wi + (k < 2 ? 2 * w + ((wi & 1) ^ k) : 8), ln = z >> 4;
      row0[k] = (z & 15) * 40 + (k == 2 ? w * 8 : 0);
      const bool isq = ln < 12;
      const u32 rs = isq ? NH * DH * 2 : FF * 2;
      const u32 so = row0[k] * rs + (isq ? qd * 1536 + ln * 128 : qd * 384 + (ln - (ln < 15 ? 12 : 15)) * 128);
      const __amdgpu_buffer_rsrc_t rr = mkrsrc(isq ? wq : ln < 15 ? w1 : w3, DM * rs);
      const u32 vo = r * rs + j * 16;
      if (k < 2) {
#pragma unroll
        for (int i = 0; i < 5; ++i) S1[5 * k + i] = __builtin_amdgcn_raw_buffer_load_b128(rr, vo, so + 8 * i * rs, NT);
      } else {
        S1[10] = __builtin_amdgcn_raw_buffer_load_b128(rr, vo, so, NT);
        S1[11] = __builtin_amdgcn_raw_buffer_load_b128(rr, w == 0 ? vo + so + 32 * rs : OOB, 0, NT);
      }
      if (k == 0) {
        const __amdgpu_buffer_rsrc_t rx = mkrsrc(x, DM * 2);
        xa = __builtin_amdgcn_raw_buffer_load_b128(rx, l * 16, 0, 0);
        xb = __builtin_amdgcn_raw_buffer_load_b32(rx, l * 4, 1024, 0);
      }
      SCHED();
    }
    const u32 epoch = __builtin_amdgcn_raw_buffer_load_b32(wsr, WS_EPOCH, 0, SC1);
    {
      const u32x4 z = {0u, 0u, 0u, 0u};
      __builtin_amdgcn_raw_buffer_store_b128(z, wsr, (w == 3 && l < 49) ? myrec + R_G2 + l * 16 : (w == 2 && l < 32) ? myrec + R_G3 + l * 16 : OOB, 0, SC1);
    }
    if (tid < 8) cnt[tid] = 0;
    SCHED();
    wg_barrier();
    SCHED();
    {
      float xv[10];
#pragma unroll
      for (int k = 0; k < 4; ++k) { xv[2 * k] = bf_lo(xa[k]); xv[2 * k + 1] = bf_hi(xa[k]); }
      xv[8] = bf_lo(xb); xv[9] = bf_hi(xb);
      float sm = 0.f;
#pragma unroll
      for (int k = 0; k < 10; ++k) sm += xv[k];
      sm = sum_hi3(sum_lo3(sm));
      const float mean = sm * (1.f / DM);
      float sv = 0.f;
#pragma unroll
      for (int k = 0; k < 10; ++k) { const float d = xv[k] - mean; sv = fmaf(d, d, sv); }
      sv = sum_hi3(sum_lo3(sv));
      const float var = rbf(sv * (1.f / DM));
      const float mb = rbf(mean);
      const float sd = rbf(sqrtf(rbf(var + 1e-5f)));
      u32 xp[10];
#pragma unroll
      for (int k = 0; k < 10; ++k) xp[k] = pkbf(rbf(xv[k] - mb) / sd);
      *reinterpret_cast<u32x4*>(xn_s + l * 8) = u32x4{xp[0], xp[1], xp[2], xp[3]};
      *reinterpret_cast<u32x4*>(xn_s + l * 8 + 4) = u32x4{xp[4], xp[5], xp[6], xp[7]};
      *reinterpret_cast<u32x2*>(xn_s + 512 + l * 2) = u32x2{xp[8], xp[9]};
      asm volatile("s_waitcnt lgkmcnt(0)" ::: "memory");
    }
    MAKE_MSK();
    const u32 tag = epoch + 1;
    {
      u32 xr[12];
#pragma unroll
      for (int i = 0; i < 10; ++i) xr[i] = xn_s[row0[i / 5] + 8 * (i % 5) + r];
      xr[10] = xn_s[row0[2] + r];
      xr[11] = w == 0 ? xn_s[row0[2] + 32 + r] : 0u;
      f32x4 a[3];
#pragma unroll
      for (int k = 0; k < 3; ++k) a[k] = f32x4{0.f, 0.f, 0.f, 0.f};
#pragma unroll
      for (int i = 0; i < 12; ++i) a[i / 5] = mfma(msk & xr[i], S1[i], a[i / 5]);
      if (useful) {
#pragma unroll
        for (int k = 0; k < 3; ++k) *reinterpret_cast<f32x4*>(s1_s + (k < 2 ? 4 * w + 2 * ((wi & 1) ^ k) + hh : 16 + w * 2 + hh) * 64 + dcol) = a[k];
      }
      asm volatile("s_waitcnt vmcnt(0)" ::: "memory");
      if (lds_ticket(cnt + C_S1, l) == 3) {
        const int nA = 16 - ((9 * wi) & 15);
        float va = 0.f, vb = 0.f;
#pragma unroll
        for (int c = 0; c < 8; ++c) {
          const float v = s1_s[(2 * c) * 64 + l] + s1_s[(2 * c + 1) * 64 + l];
          if (c < nA) va += v; else vb += v;
        }
        float v8 = 0.f;
#pragma unroll
        for (int t = 16; t < 24; ++t) v8 += s1_s[t * 64 + l];
        if (8 < nA) va += v8; else vb += v8;
        __builtin_amdgcn_raw_buffer_store_b64(u32x2{__float_as_uint(va), tag}, wsr, myrec + R_G1 + l * 8, 0, SC1);
        __builtin_amdgcn_raw_buffer_store_b64(u32x2{__float_as_uint(vb), tag}, wsr, nA <= 8 ? myrec + R_G1 + 512 + l * 8 : OOB, 0, SC1);
      }
    }
    {
      u32 ga[10];
      bool use3[3];
#pragma unroll
      for (int k = 0; k < 3; ++k) {
        const int fc = (g & 3) * 48 + min(l, 47);
        const int ln = w == 0 ? (g & 3) * 3 + k : 12 + 3 * min(k, 1) + (fc >> 6);
        const int col = w == 0 ? l : (fc & 63);
        const int f0 = (16 * ln) / 9, f1 = (16 * ln + 15) / 9;
        use3[k] = f0 + 2 <= f1;
#pragma unroll
        for (int i = 0; i < 3; ++i) {
          const int pw = min(f0 + i, f1);
          ga[k * 3 + i] = WS_REC + (qd * 32 + pw) * REC + R_G1 + (9 * pw < 16 * ln ? 512 : 0) + col * 8;
        }
      }
      ga[9] = grec + j * REC + R_G1;
      if (w == 1) {
        ga[9] = WS_REC + ((l & 31) * NS + (red_b ? (s & 4) + hh : s)) * REC + R_G1;
        ga[8] = ga[9] + (red_b ? 2 * REC : 0);
      }
      lds_wait(cnt + C_PROG, 8);
      if (w != 2 && (w != 1 || is_red)) {
        u32x2 G[10];
        bool ok = false;
        u32 spins = 0;
        for (;;) {
          asm volatile("" ::: "memory");
          if (!ok) {
#pragma unroll
            for (int i = 0; i < 10; ++i) if (w == 0 || (w == 3 && i < 6) || (w == 1 && i >= 8)) G[i] = __builtin_amdgcn_raw_buffer_load_b64(wsr, ga[i], 0, SC1);
            u32 d_ = 0;
#pragma unroll
            for (int i = 0; i < 10; ++i) if (w == 0 || (w == 3 && i < 6) || (w == 1 && i >= 8)) d_ |= G[i][1] ^ tag;
            ok = (d_ == 0);
          }
          if (__all(ok) || ++spins > SPIN_CAP) break;
          __builtin_amdgcn_s_sleep(8);
        }
        float t[3];
#pragma unroll
        for (int k = 0; k < 3; ++k) t[k] = __uint_as_float(G[3 * k][0]) + __uint_as_float(G[3 * k + 1][0]) + (use3[k] ? __uint_as_float(G[3 * k + 2][0]) : 0.f);
        if (w == 0) {
#pragma unroll
          for (int k = 0; k < 3; ++k) ((u16*)q_s)[64 * k + l] = __builtin_bit_cast(u16, (__bf16)t[k]);
          if (l == 0) cnt[C_EPOCH] = tag;
          lds_arrive(cnt + C_Q, l);
        } else if (w == 3) {
          const float gate = rbf(t[0]), up = rbf(t[1]);
          const float hv = rbf(gate / (1.f + __expf(-gate))) * up;
          h_s[l] = l < 48 ? pkbf(hv) : 0u;
        }
      }
    }
    __builtin_amdgcn_s_setprio(3);
    wg_barrier();
    {
      const int col = min(tid, 191);
      float pv[16];
#pragma unroll
      for (int i = 0; i < 16; ++i) pv[i] = op_s[i * 192 + col];
      const float mj = m_s[j];
      const float M = max_lo3(mj);
      const float fj = __builtin_amdgcn_exp2f((mj - M) * LOG2E);
      float o = 0.f;
      if (w == 3) {
        const float L = sum_lo3(l_s[j] * fj);
        o = (l & 1) ? L : M;
      } else {
#pragma unroll
        for (int i = 0; i < 8; ++i) o = fmaf(__uint_as_float(__builtin_amdgcn_readlane(__float_as_uint(fj), i)), pv[2 * i] + pv[2 * i + 1], o);
      }
      __builtin_amdgcn_raw_buffer_store_b32(__float_as_uint(o) | 1u, wsr, tid < 194 ? myrec + R_G2 + tid * 4 : OOB, 0, SC1);
    }
    wg_barrier();
    if (w < 2) {
      float y = 0.f;
#pragma unroll
      for (int i = 0; i < 16; ++i) y += yp_s[i * 128 + tid];
      __builtin_amdgcn_raw_buffer_store_b32(__float_as_uint(y) | 1u, wsr, myrec + R_G3 + tid * 4, 0, SC1);
    }
  }
}

extern "C" void kernel_launch(void* const* d_in, const int* in_sizes, int n_in, void* d_out, int out_size, void* d_ws,
                              size_t ws_size, hipStream_t stream) {
  fused_decode_kernel<<<256, 768, 0, stream>>>((const u16*)d_in[0], (const char*)d_in[1], (const char*)d_in[2],
                                               (const char*)d_in[3], (const char*)d_in[4], (const char*)d_in[6],
                                               (const char*)d_in[7], (const char*)d_in[8], (u16*)d_out, (char*)d_ws);
}
```

```cpp
#include <hip/hip_runtime.h>
#include <hip/hip_bf16.h>
#include <stdint.h>

typedef unsigned short u16;
typedef unsigned int u32;
typedef unsigned long long u64;
typedef u32 u32x2 __attribute__((ext_vector_type(2)));
typedef u32 u32x4 __attribute__((ext_vector_type(4)));
typedef float f32x4 __attribute__((ext_vector_type(4)));
typedef __attribute__((ext_vector_type(2))) __bf16 bf16x2;
typedef __attribute__((ext_vector_type(8))) __bf16 bf16x8;
#define LAS __attribute__((address_space(3)))

#define TU 85
#define TB 190
#define D1 88
#define D2 48
#define PD 7
#define GPOS 5

constexpr int DM = 640, NH = 32, DH = 192, SK = 32768, WIN = 4096, FF = 1536;
constexpr int NS = 8;
constexpr u32 WS_EPOCH = 0;
constexpr u32 WS_REC = 4096, REC = 2560;
constexpr u32 R_G1 = 0;
constexpr u32 R_G2 = 1024;
constexpr u32 R_ST = R_G2 + 768;
constexpr u32 R_G3 = 1920;
constexpr u32 WS_END = WS_REC + NH * NS * REC;
constexpr u32 SPIN_CAP = 1u << 19;
constexpr float LOG2E = 1.4426950408889634f;
constexpr u32 OOB = 0xfffffff0u;
constexpr int NT = 2, SC1 = 16;

__device__ __forceinline__ float bf_lo(u32 v) { return __uint_as_float(v << 16); }
__device__ __forceinline__ float bf_hi(u32 v) { return __uint_as_float(v & 0xffff0000u); }
__device__ __forceinline__ float rbf(float f) { return (float)(__bf16)f; }
__device__ __forceinline__ u32 pkbf(float f) { const bf16x2 h = {(__bf16)f, (__bf16)f}; return __builtin_bit_cast(u32, h); }
__device__ __forceinline__ float dot2(u32 a, u32 b, float c) {
  return __builtin_amdgcn_fdot2_f32_bf16(__builtin_bit_cast(bf16x2, a), __builtin_bit_cast(bf16x2, b), c, false);
}
__device__ __forceinline__ f32x4 mfma(u32x4 a, u32x4 b, f32x4 c) {
  return __builtin_amdgcn_mfma_f32_16x16x32_bf16(__builtin_bit_cast(bf16x8, a), __builtin_bit_cast(bf16x8, b), c, 0, 0, 0);
}
__device__ __forceinline__ __amdgpu_buffer_rsrc_t mkrsrc(const void* p, u32 bytes) {
  return __builtin_amdgcn_make_buffer_rsrc(const_cast<void*>(p), 0, bytes, 0x00020000);
}
template <int CTRL> __device__ __forceinline__ float dpp(float x) {
  return __builtin_bit_cast(float, __builtin_amdgcn_mov_dpp(__builtin_bit_cast(int, x), CTRL, 0xf, 0xf, true));
}
constexpr int XOR1 = 0xB1, XOR2 = 0x4E, HMIR = 0x141, ROR8 = 0x128, ROR4 = 0x124;
__device__ __forceinline__ float swap16_add(float a, float b) {
  auto s = __builtin_amdgcn_permlane16_swap(__float_as_uint(a), __float_as_uint(b), false, false);
  return __uint_as_float(s[0]) + __uint_as_float(s[1]);
}
__device__ __forceinline__ float swap32_add(float a, float b) {
  auto s = __builtin_amdgcn_permlane32_swap(__float_as_uint(a), __float_as_uint(b), false, false);
  return __uint_as_float(s[0]) + __uint_as_float(s[1]);
}
__device__ __forceinline__ float swap16_max(float a) {
  auto s = __builtin_amdgcn_permlane16_swap(__float_as_uint(a), __float_as_uint(a), false, false);
  return fmaxf(__uint_as_float(s[0]), __uint_as_float(s[1]));
}
__device__ __forceinline__ float swap32_max(float a) {
  auto s = __builtin_amdgcn_permlane32_swap(__float_as_uint(a), __float_as_uint(a), false, false);
  return fmaxf(__uint_as_float(s[0]), __uint_as_float(s[1]));
}
__device__ __forceinline__ float sum_hi3(float x) { x += dpp<ROR8>(x); x = swap16_add(x, x); return swap32_add(x, x); }
__device__ __forceinline__ float max_hi3(float x) { x = fmaxf(x, dpp<ROR8>(x)); x = swap16_max(x); return swap32_max(x); }
__device__ __forceinline__ float sum_lo3(float x) { x += dpp<XOR1>(x); x += dpp<XOR2>(x); x += dpp<HMIR>(x); return x; }
__device__ __forceinline__ float max_lo3(float x) { x = fmaxf(x, dpp<XOR1>(x)); x = fmaxf(x, dpp<XOR2>(x)); x = fmaxf(x, dpp<HMIR>(x)); return x; }
__device__ __forceinline__ float treduce4(float a0, float a1, float a2, float a3) {
  return swap32_add(swap16_add(a0, a2), swap16_add(a1, a3));
}
__device__ __forceinline__ void lds_arrive(u32* c, int l) {
  asm volatile("s_waitcnt lgkmcnt(0)" ::: "memory");
  if (l == 0) __hip_atomic_fetch_add((LAS u32*)c, 1u, __ATOMIC_RELAXED, __HIP_MEMORY_SCOPE_WORKGROUP);
  asm volatile("" ::: "memory");
}
__device__ __forceinline__ u32 lds_ticket(u32* c, int l) {
  asm volatile("s_waitcnt lgkmcnt(0)" ::: "memory");
  u32 t = 0;
  if (l == 0) t = __hip_atomic_fetch_add((LAS u32*)c, 1u, __ATOMIC_RELAXED, __HIP_MEMORY_SCOPE_WORKGROUP);
  t = __builtin_amdgcn_readfirstlane(t);
  asm volatile("" ::: "memory");
  return t;
}
__device__ __forceinline__ void lds_wait(u32* c, u32 n) {
  asm volatile("" ::: "memory");
  while (__builtin_amdgcn_readfirstlane(__hip_atomic_load((LAS u32*)c, __ATOMIC_RELAXED, __HIP_MEMORY_SCOPE_WORKGROUP)) < n) __builtin_amdgcn_s_sleep(1);
  asm volatile("" ::: "memory");
}
#define SCHED() __builtin_amdgcn_sched_barrier(0)
__device__ __forceinline__ void wg_barrier() {
  asm volatile("s_waitcnt lgkmcnt(0)" ::: "memory");
  __builtin_amdgcn_s_barrier();
  asm volatile("" ::: "memory");
}
#define STR2(x) #x
#define STR(x) STR2(x)
#define PACE() asm volatile("s_waitcnt vmcnt(" STR(PD) ")" ::: "memory")

enum { C_S1 = 0, C_PROG, C_Q, C_EPOCH };

__global__ void __launch_bounds__(768, 3)
fused_decode_kernel(const u16* __restrict__ x, const char* __restrict__ kc, const char* __restrict__ vc,
                    const char* __restrict__ wq, const char* __restrict__ wo, const char* __restrict__ w1,
                    const char* __restrict__ w3, const char* __restrict__ w2, u16* __restrict__ out, char* ws) {
  __shared__ __attribute__((aligned(16))) float lds[640 + 96 + 32 + 64 + 24 * 64 + 16 * 192 + 16 * 128];
  u32* xn_s = (u32*)lds;
  u32* q_s = (u32*)(lds + 640);
  float* m_s = lds + 736;
  float* l_s = lds + 744;
  u32* cnt = (u32*)(lds + 752);
  u32* h_s = (u32*)(lds + 768);
  float* s1_s = lds + 832;
  float* op_s = s1_s + 24 * 64;
  float* yp_s = op_s + 16 * 192;
  u32 t0 = (u32)__builtin_amdgcn_s_memrealtime();
  asm volatile("" : "+s"(t0));
  const int b = blockIdx.x;
  const int g = (b & 7) * 4 + (b >> 6), s = (b >> 3) & 7;
  const int tid = threadIdx.x;
  const int w = __builtin_amdgcn_readfirstlane(tid >> 6), l = tid & 63;
  const int r = l >> 3, j = l & 7;
  const __amdgpu_buffer_rsrc_t wsr = mkrsrc(ws, WS_END);
  const u32 grec = WS_REC + g * NS * REC, myrec = grec + s * REC;
  const bool is_red = (s == (g >> 2)) && !(g & 1);
  const bool red_b = (g & 2) != 0;
  const int hh = l >> 5;
  const bool useful = ((l >> 3) & 1) == hh;
  const int dcol = 8 * j + 4 * ((l >> 4) & 1);
#define MAKE_MSK() u32x4 msk; _Pragma("unroll") for (int d = 0; d < 4; ++d) msk[d] = (j >> 1) == d ? ((j & 1) ? 0xffff0000u : 0x0000ffffu) : 0u

  if (w >= 4) {
    const int sw = w - 4;
    const size_t kvbase = ((size_t)g * SK + (SK - WIN)) * (DH * 2);
    const __amdgpu_buffer_rsrc_t rk = mkrsrc(kc + kvbase, WIN * DH * 2), rv = mkrsrc(vc + kvbase, WIN * DH * 2);
    const u32 ll = (u32)((sw * 8 + s) * 3072 + r * 384 + j * 16);
#define UOFF(u) ((((u) + g) & 7) * 196608)
    u32x4 K[8][3], V[8][3];
    const u32 tz = t0 + TB + ((g & 3) * 8 + sw) * TU / 32;
#define UNTIL(t) do { while ((int)((u32)__builtin_amdgcn_s_memrealtime() - (t)) < 0) __builtin_amdgcn_s_sleep(1); asm volatile("" ::: "memory"); } while (0)
#define GATE(n) UNTIL(tz + (n) * TU)
#define LOAD1(dst, rs, u, c) do { PACE(); dst[u][c] = __builtin_amdgcn_raw_buffer_load_b128(rs, ll + (c) * 128, UOFF(u), NT); } while (0)
#define LOAD3(dst, rs, u) do { GATE(8 + (u)); LOAD1(dst, rs, u, 0); LOAD1(dst, rs, u, 1); LOAD1(dst, rs, u, 2); SCHED(); } while (0)
    wg_barrier();
#pragma unroll
    for (int i = 0; i < 24; ++i) { if (i % 3 == 0 && i) GATE(i / 3); LOAD1(K, rk, i / 3, i % 3); if (i == GPOS * 3 + 2) lds_arrive(cnt + C_PROG, l); }
    SCHED();
    LOAD3(V, rv, 0); LOAD3(V, rv, 1); LOAD3(V, rv, 2);
    lds_wait(cnt + C_Q, 1);
    MAKE_MSK();
    u32 ppk[8];
    {
      u32x4 q[3];
      float p[8];
#pragma unroll
      for (int c = 0; c < 3; ++c) q[c] = *reinterpret_cast<const u32x4*>(q_s + c * 32 + j * 4);
#define QK(u) do { float d = 0.f; _Pragma("unroll") for (int c = 0; c < 3; ++c) _Pragma("unroll") for (int k = 0; k < 4; ++k) d = dot2(K[u][c][k], q[c][k], d); \
      p[u] = rbf(rbf(sum_lo3(d)) * 0.07216878364870322f); } while (0)
#define QK2(u) do { QK(u); QK(u + 1); asm volatile("" : "+v"(p[u]), "+v"(p[u + 1])); SCHED(); } while (0)
      QK2(0); LOAD3(V, rv, 3);
      QK2(2); LOAD3(V, rv, 4);
      QK2(4); LOAD3(V, rv, 5);
      QK2(6);
      const float mw = max_hi3(fmaxf(fmaxf(fmaxf(p[0], p[1]), fmaxf(p[2], p[3])), fmaxf(fmaxf(p[4], p[5]), fmaxf(p[6], p[7]))));
      float ls = 0.f;
#pragma unroll
      for (int u = 0; u < 8; ++u) { const float e = __builtin_amdgcn_exp2f((p[u] - mw) * LOG2E); ls += e; ppk[u] = pkbf(e); }
      ls = sum_hi3(ls);
      if (l == 0) { m_s[sw] = mw; l_s[sw] = ls; }
    }
    f32x4 acc[3];
#pragma unroll
    for (int c = 0; c < 3; ++c) acc[c] = f32x4{0.f, 0.f, 0.f, 0.f};
#define PV(u) do { const u32x4 a_ = msk & ppk[u]; _Pragma("unroll") for (int c = 0; c < 3; ++c) acc[c] = mfma(a_, V[u][c], acc[c]); \
      asm volatile("" : "+v"(acc[0]), "+v"(acc[1]), "+v"(acc[2])); SCHED(); } while (0)
    LOAD3(V, rv, 6);
    PV(0); PV(1); PV(2); PV(3);
    LOAD3(V, rv, 7);
    u32x4 WO[5];
    const int ocol = red_b ? 512 + s * 16 + 4 * (sw & 3) : s * 64 + 8 * sw + 4 * (l & 1);
    const int ok2 = 2 * ((l >> 4) & 1) + hh;
    u32 xraw, tb;
    {
      const __amdgpu_buffer_rsrc_t rw = mkrsrc((sw < 6) ? wo + (size_t)(g * DH + sw * 32) * (DM * 2) : w2 + (size_t)(g * 48 + (sw - 6) * 32) * (DM * 2), 32 * DM * 2);
      const u32 wl = ((sw == 7) ? (r & 3) : r) * (4 * DM * 2) + j * 16;
#define WLD(i) WO[i] = __builtin_amdgcn_raw_buffer_load_b128(rw, wl + (i) * (DM * 2), s * 128, NT)
#define WLD4() do { WO[4] = __builtin_amdgcn_raw_buffer_load_b128(rw, wl, (s & 3) * (DM * 2) + (8 + (s >> 2)) * 128, NT); xraw = x[ocol + ok2]; } while (0)
      PV(4); PV(5); PV(6);
      GATE(16);
      WLD(0); WLD(1); WLD(2);
      SCHED();
      PV(7);
      if (useful) {
#pragma unroll
        for (int c = 0; c < 3; ++c) *reinterpret_cast<f32x4*>(op_s + (sw * 2 + hh) * 192 + c * 64 + dcol) = acc[c];
      }
      wg_barrier();
      tb = (u32)__builtin_amdgcn_s_memrealtime();
      __builtin_amdgcn_s_sleep(8);
      asm volatile("" ::: "memory");
      WLD(3); WLD4();
      SCHED();
    }
    u32 opk[4];
    if (sw < 6) {
      const u32 goff = grec + j * REC + R_G2 + sw * 128 + r * 16;
      const u32 soff = grec + j * REC + R_ST;
      u32x2 SS;
      u32x4 O;
      UNTIL(tb + D1);
      bool ok = false;
      u32 spins = 0;
      for (;;) {
        asm volatile("" ::: "memory");
        if (!ok) {
          SS = __builtin_amdgcn_raw_buffer_load_b64(wsr, soff, 0, SC1);
          O = __builtin_amdgcn_raw_buffer_load_b128(wsr, goff, 0, SC1);
          ok = (O[0] & O[1] & O[2] & O[3] & SS[0] & SS[1] & 1) != 0;
        }
        if (__all(ok) || ++spins > SPIN_CAP) break;
        __builtin_amdgcn_s_sleep(2);
      }
      const float ms = __uint_as_float(SS[0] & ~1u);
      const float f = __builtin_amdgcn_exp2f((ms - max_lo3(ms)) * LOG2E);
      const float sc = f * __builtin_amdgcn_rcpf(sum_lo3(f * __uint_as_float(SS[1])));
#pragma unroll
      for (int i = 0; i < 4; ++i) opk[i] = pkbf(sum_lo3(sc * __uint_as_float(O[i])));
    } else {
      const u32x4 hq = *reinterpret_cast<const u32x4*>(h_s + (sw - 6) * 32 + 4 * r);
#pragma unroll
      for (int i = 0; i < 4; ++i) opk[i] = hq[i];
    }
    {
      f32x4 y1 = {0.f, 0.f, 0.f, 0.f}, y2 = y1;
      const u32 o2 = (s & 2) ? ((s & 1) ? opk[3] : opk[2]) : ((s & 1) ? opk[1] : opk[0]);
      y2 = mfma(msk & o2, WO[4], y2);
#pragma unroll
      for (int i = 0; i < 4; ++i) y1 = mfma(msk & opk[i], WO[i], y1);
      if (useful) {
        *reinterpret_cast<f32x4*>(yp_s + (sw * 2 + hh) * 128 + dcol) = y1;
        *reinterpret_cast<f32x4*>(yp_s + (sw * 2 + hh) * 128 + 64 + dcol) = y2;
      }
    }
    wg_barrier();
    if (is_red && !(red_b && sw >= 4)) {
      const u32 goff = red_b ? WS_REC + ((l & 31) * NS + (s & 4) + hh) * REC + R_G3 + 256 + (s & 3) * 64 + sw * 16
                             : WS_REC + ((l >> 1) * NS + s) * REC + R_G3 + sw * 32 + (l & 1) * 16;
      u32x4 Y, Z;
      tb = (u32)__builtin_amdgcn_s_memrealtime();
      UNTIL(tb + D2);
      bool ok = false;
      u32 spins = 0;
      for (;;) {
        asm volatile("" ::: "memory");
        if (!ok) {
          Y = __builtin_amdgcn_raw_buffer_load_b128(wsr, goff, 0, SC1);
          Z = __builtin_amdgcn_raw_buffer_load_b128(wsr, red_b ? goff + 2 * REC : OOB, 0, SC1);
          ok = (Y[0] & Y[1] & Y[2] & Y[3] & (red_b ? Z[0] & Z[1] & Z[2] & Z[3] : 1u) & 1) != 0;
        }
        if (__all(ok) || ++spins > SPIN_CAP) break;
        __builtin_amdgcn_s_sleep(4);
      }
      const f32x4 yz = __builtin_bit_cast(f32x4, Y) + __builtin_bit_cast(f32x4, Z);
      float t = treduce4(yz[0], yz[1], yz[2], yz[3]);
      t += dpp<ROR8>(t); t += dpp<ROR4>(t); t += dpp<XOR2>(t);
      const float t1 = t + dpp<XOR1>(t);
      if ((l & (red_b ? 15 : 14)) == 0) out[ocol + ok2] = __builtin_bit_cast(u16, (__bf16)(__uint_as_float(xraw << 16) + (red_b ? t1 : t)));
      if (b == 0 && sw == 0) __builtin_amdgcn_raw_buffer_store_b32(cnt[C_EPOCH], wsr, l == 0 ? WS_EPOCH : OOB, 0, SC1);
    }
  } else {
    const int qd = g >> 2, wi = (g & 3) * 8 + s;
    u32x4 S1[12], xa;
    u32 xb;
    int row0[3];
#pragma unroll
    for (int k = 0; k < 3; ++k) {
      const int z = 9 * wi + (k < 2 ? 2 * w + ((wi & 1) ^ k) : 8), ln = z >> 4;
      row0[k] = (z & 15) * 40 + (k == 2 ? w * 8 : 0);
      const bool isq = ln < 12;
      const u32 rs = isq ? NH * DH * 2 : FF * 2;
      const u32 so = row0[k] * rs + (isq ? qd * 1536 + ln * 128 : qd * 384 + (ln - (ln < 15 ? 12 : 15)) * 128);
      const __amdgpu_buffer_rsrc_t rr = mkrsrc(isq ? wq : ln < 15 ? w1 : w3, DM * rs);
      const u32 vo = r * rs + j * 16;
      if (k < 2) {
#pragma unroll
        for (int i = 0; i < 5; ++i) S1[5 * k + i] = __builtin_amdgcn_raw_buffer_load_b128(rr, vo, so + 8 * i * rs, NT);
      } else {
        S1[10] = __builtin_amdgcn_raw_buffer_load_b128(rr, vo, so, NT);
        S1[11] = __builtin_amdgcn_raw_buffer_load_b128(rr, w == 0 ? vo + so + 32 * rs : OOB, 0, NT);
      }
      if (k == 0) {
        const __amdgpu_buffer_rsrc_t rx = mkrsrc(x, DM * 2);
        xa = __builtin_amdgcn_raw_buffer_load_b128(rx, l * 16, 0, 0);
        xb = __builtin_amdgcn_raw_buffer_load_b32(rx, l * 4, 1024, 0);
      }
      SCHED();
    }
    const u32 epoch = __builtin_amdgcn_raw_buffer_load_b32(wsr, WS_EPOCH, 0, SC1);
    {
      const u32x4 z = {0u, 0u, 0u, 0u};
      __builtin_amdgcn_raw_buffer_store_b128(z, wsr, (w == 3 && l < 49) ? myrec + R_G2 + l * 16 : (w == 2 && l < 32) ? myrec + R_G3 + l * 16 : OOB, 0, SC1);
    }
    if (tid < 8) cnt[tid] = 0;
    SCHED();
    wg_barrier();
    SCHED();
    {
      float xv[10];
#pragma unroll
      for (int k = 0; k < 4; ++k) { xv[2 * k] = bf_lo(xa[k]); xv[2 * k + 1] = bf_hi(xa[k]); }
      xv[8] = bf_lo(xb); xv[9] = bf_hi(xb);
      float sm = 0.f;
#pragma unroll
      for (int k = 0; k < 10; ++k) sm += xv[k];
      sm = sum_hi3(sum_lo3(sm));
      const float mean = sm * (1.f / DM);
      float sv = 0.f;
#pragma unroll
      for (int k = 0; k < 10; ++k) { const float d = xv[k] - mean; sv = fmaf(d, d, sv); }
      sv = sum_hi3(sum_lo3(sv));
      const float var = rbf(sv * (1.f / DM));
      const float mb = rbf(mean);
      const float sd = rbf(sqrtf(rbf(var + 1e-5f)));
      u32 xp[10];
#pragma unroll
      for (int k = 0; k < 10; ++k) xp[k] = pkbf(rbf(xv[k] - mb) / sd);
      *reinterpret_cast<u32x4*>(xn_s + l * 8) = u32x4{xp[0], xp[1], xp[2], xp[3]};
      *reinterpret_cast<u32x4*>(xn_s + l * 8 + 4) = u32x4{xp[4], xp[5], xp[6], xp[7]};
      *reinterpret_cast<u32x2*>(xn_s + 512 + l * 2) = u32x2{xp[8], xp[9]};
      asm volatile("s_waitcnt lgkmcnt(0)" ::: "memory");
    }
    MAKE_MSK();
    const u32 tag = epoch + 1;
    {
      u32 xr[12];
#pragma unroll
      for (int i = 0; i < 10; ++i) xr[i] = xn_s[row0[i / 5] + 8 * (i % 5) + r];
      xr[10] = xn_s[row0[2] + r];
      xr[11] = w == 0 ? xn_s[row0[2] + 32 + r] : 0u;
      f32x4 a[3];
#pragma unroll
      for (int k = 0; k < 3; ++k) a[k] = f32x4{0.f, 0.f, 0.f, 0.f};
#pragma unroll
      for (int i = 0; i < 12; ++i) a[i / 5] = mfma(msk & xr[i], S1[i], a[i / 5]);
      if (useful) {
#pragma unroll
        for (int k = 0; k < 3; ++k) *reinterpret_cast<f32x4*>(s1_s + (k < 2 ? 4 * w + 2 * ((wi & 1) ^ k) + hh : 16 + w * 2 + hh) * 64 + dcol) = a[k];
      }
      asm volatile("s_waitcnt vmcnt(0)" ::: "memory");
      if (lds_ticket(cnt + C_S1, l) == 3) {
        const int nA = 16 - ((9 * wi) & 15);
        float va = 0.f, vb = 0.f;
#pragma unroll
        for (int c = 0; c < 8; ++c) {
          const float v = s1_s[(2 * c) * 64 + l] + s1_s[(2 * c + 1) * 64 + l];
          if (c < nA) va += v; else vb += v;
        }
        float v8 = 0.f;
#pragma unroll
        for (int t = 16; t < 24; ++t) v8 += s1_s[t * 64 + l];
        if (8 < nA) va += v8; else vb += v8;
        __builtin_amdgcn_raw_buffer_store_b64(u32x2{__float_as_uint(va), tag}, wsr, myrec + R_G1 + l * 8, 0, SC1);
        __builtin_amdgcn_raw_buffer_store_b64(u32x2{__float_as_uint(vb), tag}, wsr, nA <= 8 ? myrec + R_G1 + 512 + l * 8 : OOB, 0, SC1);
      }
    }
    {
      u32 ga[10];
      bool use3[3];
#pragma unroll
      for (int k = 0; k < 3; ++k) {
        const int fc = (g & 3) * 48 + min(l, 47);
        const int ln = w == 0 ? (g & 3) * 3 + k : 12 + 3 * min(k, 1) + (fc >> 6);
        const int col = w == 0 ? l : (fc & 63);
        const int f0 = (16 * ln) / 9, f1 = (16 * ln + 15) / 9;
        use3[k] = f0 + 2 <= f1;
#pragma unroll
        for (int i = 0; i < 3; ++i) {
          const int pw = min(f0 + i, f1);
          ga[k * 3 + i] = WS_REC + (qd * 32 + pw) * REC + R_G1 + (9 * pw < 16 * ln ? 512 : 0) + col * 8;
        }
      }
      ga[9] = grec + j * REC + R_G1;
      if (w == 1) {
        ga[9] = WS_REC + ((l & 31) * NS + (red_b ? (s & 4) + hh : s)) * REC + R_G1;
        ga[8] = ga[9] + (red_b ? 2 * REC : 0);
      }
      lds_wait(cnt + C_PROG, 8);
      if (w != 2 && (w != 1 || is_red)) {
        u32x2 G[10];
        bool ok = false;
        u32 spins = 0;
        for (;;) {
          asm volatile("" ::: "memory");
          if (!ok) {
#pragma unroll
            for (int i = 0; i < 10; ++i) if (w == 0 || (w == 3 && i < 6) || (w == 1 && i >= 8)) G[i] = __builtin_amdgcn_raw_buffer_load_b64(wsr, ga[i], 0, SC1);
            u32 d_ = 0;
#pragma unroll
            for (int i = 0; i < 10; ++i) if (w == 0 || (w == 3 && i < 6) || (w == 1 && i >= 8)) d_ |= G[i][1] ^ tag;
            ok = (d_ == 0);
          }
          if (__all(ok) || ++spins > SPIN_CAP) break;
          __builtin_amdgcn_s_sleep(8);
        }
        float t[3];
#pragma unroll
        for (int k = 0; k < 3; ++k) t[k] = __uint_as_float(G[3 * k][0]) + __uint_as_float(G[3 * k + 1][0]) + (use3[k] ? __uint_as_float(G[3 * k + 2][0]) : 0.f);
        if (w == 0) {
#pragma unroll
          for (int k = 0; k < 3; ++k) ((u16*)q_s)[64 * k + l] = __builtin_bit_cast(u16, (__bf16)t[k]);
          if (l == 0) cnt[C_EPOCH] = tag;
          lds_arrive(cnt + C_Q, l);
        } else if (w == 3) {
          const float gate = rbf(t[0]), up = rbf(t[1]);
          const float hv = rbf(gate / (1.f + __expf(-gate))) * up;
          h_s[l] = l < 48 ? pkbf(hv) : 0u;
        }
      }
    }
    __builtin_amdgcn_s_setprio(3);
    wg_barrier();
    {
      const int col = min(tid, 191);
      float pv[16];
#pragma unroll
      for (int i = 0; i < 16; ++i) pv[i] = op_s[i * 192 + col];
      const float mj = m_s[j];
      const float M = max_lo3(mj);
      const float fj = __builtin_amdgcn_exp2f((mj - M) * LOG2E);
      float o = 0.f;
      if (w == 3) {
        const float L = sum_lo3(l_s[j] * fj);
        o = (l & 1) ? L : M;
      } else {
#pragma unroll
        for (int i = 0; i < 8; ++i) o = fmaf(__uint_as_float(__builtin_amdgcn_readlane(__float_as_uint(fj), i)), pv[2 * i] + pv[2 * i + 1], o);
      }
      __builtin_amdgcn_raw_buffer_store_b32(__float_as_uint(o) | 1u, wsr, tid < 194 ? myrec + R_G2 + tid * 4 : OOB, 0, SC1);
    }
    wg_barrier();
    if (w < 2) {
      float y = 0.f;
#pragma unroll
      for (int i = 0; i < 16; ++i) y += yp_s[i * 128 + tid];
      __builtin_amdgcn_raw_buffer_store_b32(__float_as_uint(y) | 1u, wsr, myrec + R_G3 + tid * 4, 0, SC1);
    }
  }
}

extern "C" void kernel_launch(void* const* d_in, const int* in_sizes, int n_in, void* d_out, int out_size, void* d_ws,
                              size_t ws_size, hipStream_t stream) {
  fused_decode_kernel<<<256, 768, 0, stream>>>((const u16*)d_in[0], (const char*)d_in[1], (const char*)d_in[2],
                                               (const char*)d_in[3], (const char*)d_in[4], (const char*)d_in[6],
                                               (const char*)d_in[7], (const char*)d_in[8], (u16*)d_out, (char*)d_ws);
}
```
